```python
import torch

def _softmax(x, axis=-1):
    return torch.softmax(x, dim=axis)
def _gelu(x):
    return torch.nn.functional.gelu(x, approximate="tanh")
def _silu(x):
    return torch.nn.functional.silu(x)
def _sigmoid(x):
    return torch.sigmoid(x)
def _layernorm(x, axis=-1, eps=1e-5):
    m = x.mean(dim=axis, keepdim=True)
    v = x.var(dim=axis, keepdim=True, unbiased=False)
    return (x - m) / torch.sqrt(v + eps)
def _scatter_add(tgt, idx, src, axis=0):
    return tgt.clone().index_add_(axis, idx, src)
def _segment_sum(src, seg, n):
    out = torch.zeros((n,) + src.shape[1:], dtype=src.dtype)
    return out.index_add_(0, seg, src)
def _pad(x, pw):
    flat = tuple(v for pair in reversed(pw) for v in pair)
    return torch.nn.functional.pad(x, flat)

kernel_name = 'parallel_sum__sliding_local_dilated__geglu_ffn__prompt'

def setup_inputs():

    _w = (lambda a: (lambda t: t.to(torch.bfloat16) if t.is_floating_point() else t)(torch.as_tensor(a)))
    def _A_setup():
        B, S, D = 1, 2048, 448
        return {
            "x": _w((lambda *s: torch.randn(*s, dtype=torch.bfloat16) * 0.05)(B, S, D)),
            "wqkv": _w((lambda *s: torch.randn(*s, dtype=torch.bfloat16) * 0.05)(D, 3 * D)),
            "wo": _w((lambda *s: torch.randn(*s, dtype=torch.bfloat16) * 0.05)(D, D)),
        }
    _A_in = _A_setup()

    def _B_setup():
        B, S, D, F = 1, 2048, 448, 1792
        return {
            "x": _w((lambda *s: torch.randn(*s, dtype=torch.bfloat16) * 0.05)(B, S, D)),
            "w_gate": _w((lambda *s: torch.randn(*s, dtype=torch.bfloat16) * 0.05)(D, F)),
            "w_up": _w((lambda *s: torch.randn(*s, dtype=torch.bfloat16) * 0.05)(D, F)),
            "w_down": _w((lambda *s: torch.randn(*s, dtype=torch.bfloat16) * 0.05)(F, D)),
        }
    _B_in = _B_setup()

    return {
        "A_x": _A_in["x"],
        "A_wqkv": _A_in["wqkv"],
        "A_wo": _A_in["wo"],
        "B_x": _B_in["x"],
        "B_w_gate": _B_in["w_gate"],
        "B_w_up": _B_in["w_up"],
        "B_w_down": _B_in["w_down"],
    }

def reference(A_x, A_wqkv, A_wo, B_x, B_w_gate, B_w_up, B_w_down):
    def _A_ref(x, wqkv, wo):
        B, S, D = x.shape
        H = 16; Dh = D // H
        W, dil = 8, 2
        qkv = (lambda x, *d: x.permute(*d))((x @ wqkv).reshape(B, S, 3, H, Dh), 2, 0, 3, 1, 4)
        q, k, v = qkv[0], qkv[1], qkv[2]
        scale = Dh ** -0.5
        pos = torch.arange(S)
        off = torch.arange(W) * dil
        idx = pos[:, None] - off[None, :]
        mask = idx >= 0
        idx_c = torch.where(mask, idx, 0).reshape(-1)
        kg = k[:, :, idx_c, :].reshape(B, H, S, W, Dh)
        vg = v[:, :, idx_c, :].reshape(B, H, S, W, Dh)
        logits = torch.einsum("bhsd,bhswd->bhsw", q, kg) * scale
        logits = torch.where(mask[None, None, :, :], logits, -1e9)
        attn = _softmax(logits, axis=-1)
        o = torch.einsum("bhsw,bhswd->bhsd", attn, vg)
        return (lambda x, *d: x.permute(*d))(o, 0, 2, 1, 3).reshape(B, S, D) @ wo

    def _B_ref(x, w_gate, w_up, w_down):
        return (_gelu(x @ w_gate) * (x @ w_up)) @ w_down

    x = A_x
    xn = _layernorm(x, axis=-1)
    return x + _A_ref(xn, A_wqkv, A_wo) + _B_ref(xn, B_w_gate, B_w_up, B_w_down)
```

```cpp
#include <hip/hip_runtime.h>
#include <hip/hip_bf16.h>
#include <stdint.h>
typedef unsigned short u16;
constexpr int S = 2048, D = 448, H = 16, DH = 28, F = 1792;
__device__ __forceinline__ float bf2f(u16 v) { return __uint_as_float(((unsigned)v) << 16); }
__device__ __forceinline__ u16 f2bf(float f) { __hip_bfloat16 b = __float2bfloat16(f); return *(u16*)&b; }

__global__ void k_ln(const u16* x, u16* xn) {
    int row = blockIdx.x, t = threadIdx.x;
    float v[7]; float s = 0;
    for (int i = 0; i < 7; ++i) { v[i] = bf2f(x[row * D + t + 64 * i]); s += v[i]; }
    for (int m = 32; m >= 1; m >>= 1) s += __shfl_xor(s, m);
    float mean = s / D; float q = 0;
    for (int i = 0; i < 7; ++i) { float d = v[i] - mean; q += d * d; }
    for (int m = 32; m >= 1; m >>= 1) q += __shfl_xor(q, m);
    float r = rsqrtf(q / D + 1e-5f);
    for (int i = 0; i < 7; ++i) xn[row * D + t + 64 * i] = f2bf((v[i] - mean) * r);
}
__global__ void k_gemm(const u16* A, const u16* B, float* C, int M, int N, int K) {
    int n = blockIdx.x * 64 + threadIdx.x, m = blockIdx.y;
    if (n >= N) return;
    float acc = 0;
    for (int k = 0; k < K; ++k) acc += bf2f(A[m * K + k]) * bf2f(B[k * N + n]);
    C[m * N + n] = acc;
}
__global__ void k_attn(const float* qkv, u16* o) {
    int s = blockIdx.x, h = threadIdx.x;
    if (h >= H) return;
    float l[8]; float mx = -1e30f;
    for (int w = 0; w < 8; ++w) {
        int i = s - 2 * w; float a = 0;
        if (i >= 0) { for (int d = 0; d < DH; ++d) a += bf2f(f2bf(qkv[s * 3 * D + h * DH + d])) * bf2f(f2bf(qkv[i * 3 * D + D + h * DH + d])); a *= rsqrtf((float)DH); }
        else a = -1e9f;
        l[w] = a; mx = fmaxf(mx, a);
    }
    float sum = 0;
    for (int w = 0; w < 8; ++w) { l[w] = __expf(l[w] - mx); sum += l[w]; }
    for (int d = 0; d < DH; ++d) {
        float a = 0;
        for (int w = 0; w < 8; ++w) { int i = s - 2 * w; if (i < 0) i = 0; a += l[w] / sum * bf2f(f2bf(qkv[i * 3 * D + 2 * D + h * DH + d])); }
        o[s * D + h * DH + d] = f2bf(a);
    }
}
__global__ void k_glu(const float* g, const float* u, u16* h) {
    int i = blockIdx.x * 256 + threadIdx.x;
    float x = g[i];
    float ge = 0.5f * x * (1.f + tanhf(0.7978845608028654f * (x + 0.044715f * x * x * x)));
    h[i] = f2bf(ge * u[i]);
}
__global__ void k_fin(const u16* x, const float* a, const float* b, u16* out) {
    int i = blockIdx.x * 256 + threadIdx.x;
    out[i] = f2bf(bf2f(x[i]) + a[i] + b[i]);
}
extern "C" void kernel_launch(void* const* d_in, const int* in_sizes, int n_in, void* d_out, int out_size, void* d_ws, size_t ws_size, hipStream_t stream) {
    const u16* x = (const u16*)d_in[0]; const u16* wqkv = (const u16*)d_in[1]; const u16* wo = (const u16*)d_in[2];
    const u16* wg = (const u16*)d_in[4]; const u16* wu = (const u16*)d_in[5]; const u16* wd = (const u16*)d_in[6];
    char* ws = (char*)d_ws;
    u16* xn = (u16*)ws; float* qkv = (float*)(ws + (4 << 20)); u16* o = (u16*)(ws + (20 << 20));
    float* g = (float*)(ws + (24 << 20)); float* u = (float*)(ws + (40 << 20)); u16* h = (u16*)(ws + (56 << 20));
    float* a = (float*)(ws + (64 << 20)); float* b = (float*)(ws + (72 << 20));
    k_ln<<<S, 64, 0, stream>>>(x, xn);
    k_gemm<<<dim3(3 * D / 64, S), 64, 0, stream>>>(xn, wqkv, qkv, S, 3 * D, D);
    k_attn<<<S, 64, 0, stream>>>(qkv, o);
    k_gemm<<<dim3(D / 64, S), 64, 0, stream>>>(o, wo, a, S, D, D);
    k_gemm<<<dim3(F / 64, S), 64, 0, stream>>>(xn, wg, g, S, F, D);
    k_gemm<<<dim3(F / 64, S), 64, 0, stream>>>(xn, wu, u, S, F, D);
    k_glu<<<S * F / 256, 256, 0, stream>>>(g, u, h);
    k_gemm<<<dim3(D / 64, S), 64, 0, stream>>>(h, wd, b, S, D, F);
    k_fin<<<S * D / 256, 256, 0, stream>>>(x, a, b, (u16*)d_out);
}
```

```cpp
#include <hip/hip_runtime.h>

struct Args {
    const void* x; const void* wqkv; const void* wo; const void* wg;
    const void* wu; const void* wd; void* out; void* ws;
};

__global__ void __launch_bounds__(512, 2) __attribute__((aligned(4096))) fused_block_kernel(Args args) {
    __shared__ __attribute__((aligned(16))) char lds[163840];
    asm volatile(
        "Lq0:\n"
        ".set Lp0, Lq0-4\n"
        "s_load_dwordx16 s[16:31], %0, 0\n"
        "v_readfirstlane_b32 s64, %2\n"
        "s_mov_b32 s94, %1\n"
        "v_and_b32 v1, 63, %2\n"
        "s_setprio 1\n"
        "s_lshr_b32 s68, s94, 3\n"
        "s_lshr_b32 s64, s64, 6\n"
        "s_getpc_b64 s[56:57]\n"
        "Lc0:\n"
        "s_and_b32 s57, s57, 0xffff\n"
        "s_cmp_eq_u32 s64, 6\n"
        "s_cselect_b32 s58, Lend-Lc0, 0\n"
        "s_mov_b32 s59, 0x20000\n"
        "s_lshl_b32 s66, s94, 4\n"
        "s_and_b32 s66, s66, 0xf80\n"
        "v_lshlrev_b32 v116, 12, v1\n"
        "buffer_load_dword v117, v116, s[56:59], s66 offen\n"
        "s_sub_u32 s67, s68, 4\n"
        "s_mul_i32 s69, s67, 37\n"
        "v_lshrrev_b32 v2, 3, v1\n"
        "s_lshr_b32 s69, s69, 8\n"
        "v_and_b32 v5, 7, v1\n"
        "s_mul_i32 s75, s69, 7\n"
        "v_mul_u32_u24 v6, 896, v2\n"
        "s_sub_u32 s75, s67, s75\n"
        "v_lshl_add_u32 v7, v5, 4, v6\n"
        "s_cmp_lt_u32 s68, 4\n"
        "s_cselect_b32 s74, s68, s69\n"
        "s_cselect_b32 s75, 7, s75\n"
        "s_bfe_u32 s67, s94, 0x20001\n"
        "s_lshl2_add_u32 s72, s67, s74\n"
        "s_and_b32 s68, s74, 1\n"
        "s_lshl2_add_u32 s67, s68, s67\n"
        "s_add_u32 s67, s67, s75\n"
        "s_mul_i32 s67, s67, 3\n"
        "s_mov_b32 s70, 0xd11ac688\n"
        "s_mov_b32 s71, 0x6b1a2358\n"
        "s_lshr_b64 s[70:71], s[70:71], s67\n"
        "s_lshr_b32 s96, s64, 2\n"
        "s_and_b32 s97, s64, 3\n"
        "s_lshl1_add_u32 s98, s97, s96\n"
        "s_lshl3_add_u32 s68, s72, s98\n"
        "s_and_b32 s99, s75, 1\n"
        "s_lshl1_add_u32 s69, s68, s99\n"
        "s_mul_i32 s68, s69, 7168\n"
        "s_mov_b32 s34, 1835008\n"
        "v_add_u32 v6, s68, v7\n"
        "s_mov_b32 s35, 0x20000\n"
        "s_and_b32 s4, s70, 7\n"
        "s_waitcnt lgkmcnt(0)\n"
        "s_mov_b32 s32, s16\n"
        "s_and_b32 s33, s17, 0xffff\n"
        "s_lshl_b32 s66, s4, 7\n"
        "buffer_load_dwordx4 v[120:123], v6, s[32:35], s66 offen\n"
        "s_bfe_u32 s5, s70, 0x30003\n"
        "s_lshl_b32 s66, s5, 7\n"
        "buffer_load_dwordx4 v[124:127], v6, s[32:35], s66 offen\n"
        "s_bfe_u32 s6, s70, 0x30006\n"
        "s_lshl_b32 s66, s6, 7\n"
        "buffer_load_dwordx4 v[128:131], v6, s[32:35], s66 offen\n"
        "s_bfe_u32 s7, s70, 0x30009\n"
        "s_lshl_b32 s66, s7, 7\n"
        "buffer_load_dwordx4 v[132:135], v6, s[32:35], s66 offen\n"
        "s_bfe_u32 s8, s70, 0x3000c\n"
        "s_lshl_b32 s66, s8, 7\n"
        "buffer_load_dwordx4 v[136:139], v6, s[32:35], s66 offen\n"
        "s_bfe_u32 s9, s70, 0x3000f\n"
        "s_bfe_u32 s10, s70, 0x30012\n"
        "s_xor_b32 s69, s69, 1\n"
        "s_mul_i32 s69, s69, 7168\n"
        "v_add_u32 v7, s69, v7\n"
        "s_mul_i32 s99, s99, 0x440\n"
        "s_mul_i32 s98, s96, 28\n"
        "s_add_u32 s98, s98, s97\n"
        "s_lshl_b32 s98, s98, 11\n"
        "s_add_u32 s98, s98, 49152\n"
        "v_and_b32 v3, 15, v1\n"
        "v_lshrrev_b32 v4, 4, v1\n"
        "s_and_b32 s76, s94, 1\n"
        "s_and_b32 s67, s94, 7\n"
        "s_lshl2_add_u32 s74, s67, s74\n"
        "s_mul_i32 s73, s76, 7\n"
        "s_add_u32 s73, s73, s75\n"
        "s_mov_b32 s70, 0x49873210\n"
        "s_mov_b32 s71, 0x00dcba65\n"
        "s_lshl_b32 s67, s73, 2\n"
        "s_lshr_b64 s[70:71], s[70:71], s67\n"
        "s_and_b32 s15, s70, 15\n"
        "s_mov_b32 s60, s30\n"
        "s_and_b32 s61, s31, 0xffff\n"
        "s_mov_b32 s62, 20971520\n"
        "s_mov_b32 s63, 0x20000\n"
        "s_cmp_eq_u32 s75, 7\n"
        "s_cselect_b64 vcc, -1, 0\n"
        "v_and_b32 v216, 1, v4\n"
        "v_lshlrev_b32 v216, 1, v216\n"
        "v_xor_b32 v216, v216, v5\n"
        "v_lshlrev_b32 v222, 4, v216\n"
        "v_lshl_add_u32 v223, s97, 4, v2\n"
        "v_lshrrev_b32 v216, 5, v1\n"
        "v_lshl_add_u32 v216, s97, 1, v216\n"
        "v_and_b32 v217, 3, v216\n"
        "v_lshlrev_b32 v217, 1, v217\n"
        "v_and_b32 v218, 31, v1\n"
        "v_xor_b32 v217, v217, v218\n"
        "v_lshlrev_b32 v217, 4, v217\n"
        "v_cndmask_b32 v216, v223, v216, vcc\n"
        "v_cndmask_b32 v217, v222, v217, vcc\n"
        "s_cselect_b32 s68, 128, 64\n"
        "v_mul_u32_u24 v216, 2688, v216\n"
        "v_add_u32 v224, v216, v217\n"
        "v_xor_b32 v217, s68, v217\n"
        "s_mov_b32 s68, 21504\n"
        "v_add3_u32 v225, v216, v217, s68\n"
        "s_cselect_b32 s67, 10, 11\n"
        "s_cselect_b32 s68, 12, 10\n"
        "s_cselect_b32 s69, s96, s97\n"
        "s_cselect_b32 s66, 12, 11\n"
        "s_cselect_b32 s80, 3, 1\n"
        "s_cselect_b32 s79, s76, s15\n"
        "s_cselect_b32 s95, 0, 7\n"
        "s_cselect_b32 s83, s4, s5\n"
        "s_cselect_b32 s84, s4, s6\n"
        "s_cselect_b32 s85, s4, s7\n"
        "s_cselect_b32 s86, s5, s8\n"
        "s_cselect_b32 s87, s5, s9\n"
        "s_cselect_b32 s82, 43008, 0\n"
        "s_lshl_b32 s65, s97, s67\n"
        "s_lshl_b32 s68, s96, s68\n"
        "s_add_u32 s81, s65, s68\n"
        "s_lshl_b32 s67, s69, s66\n"
        "s_add_u32 s67, s67, 49152\n"
        "v_lshrrev_b32 v216, 1, v3\n"
        "v_xor_b32 v216, v216, v4\n"
        "v_lshlrev_b32 v216, 4, v216\n"
        "v_lshl_add_u32 v216, v3, 7, v216\n"
        "v_add_u32 v250, s67, v216\n"
        "v_xor_b32 v251, 64, v250\n"
        "v_add_u32 v252, 57344, v250\n"
        "v_add_u32 v253, 57344, v251\n"
        "s_mul_i32 s69, s79, s80\n"
        "s_add_u32 s69, s69, s95\n"
        "s_lshl_b32 s69, s69, 7\n"
        "s_add_u32 s36, s18, s69\n"
        "s_addc_u32 s37, s19, 0\n"
        "s_and_b32 s37, s37, 0xffff\n"
        "s_sub_u32 s38, 1204224, s69\n"
        "s_mov_b32 s39, 0x20000\n"
        "s_cmp_eq_u32 s96, 1\n"
        "s_cselect_b64 vcc, -1, 0\n"
        "v_cndmask_b32 v248, v224, v225, vcc\n"
        "s_cmp_eq_u32 s97, 1\n"
        "s_cselect_b64 s[52:53], s[24:25], s[22:23]\n"
        "s_cmp_eq_u32 s97, 2\n"
        "s_cselect_b64 s[52:53], s[26:27], s[52:53]\n"
        "s_cmp_eq_u32 s97, 3\n"
        "s_cselect_b64 s[52:53], s[20:21], s[52:53]\n"
        "s_cselect_b32 s67, 13, 49\n"
        "s_mov_b32 s54, 1605632\n"
        "s_cselect_b32 s54, 401408, s54\n"
        "s_cmp_lt_u32 s64, 2\n"
        "s_cselect_b32 s54, s54, 0\n"
        "s_and_b32 s53, s53, 0xffff\n"
        "s_mov_b32 s55, 0x20000\n"
        "s_sub_u32 s68, s67, 1\n"
        "v_min_u32 v246, s68, v1\n"
        "s_cmp_lt_u32 s97, 2\n"
        "s_cbranch_scc1 Lwa1\n"
        "s_mul_i32 s67, s67, s94\n"
        "v_add_u32 v246, s67, v246\n"
        "s_branch Lwb1\n"
        "Lwa1:\n"
        "s_lshr_b32 s67, s94, 1\n"
        "s_mul_i32 s67, s67, 49\n"
        "v_add_u32 v246, s67, v246\n"
        "v_mul_u32_u24 v247, 18725, v246\n"
        "v_lshrrev_b32 v247, 18, v247\n"
        "v_add_u32 v247, s76, v247\n"
        "v_mad_u32_u24 v246, v247, 14, v246\n"
        "Lwb1:\n"
        "v_lshlrev_b32 v246, 7, v246\n"
        "v_bfe_u32 v216, v1, 2, 2\n"
        "v_lshl_add_u32 v217, v4, 3, v216\n"
        "v_lshlrev_b32 v217, 7, v217\n"
        "v_and_b32 v218, 3, v1\n"
        "v_lshl_add_u32 v217, v218, 3, v217\n"
        "v_lshrrev_b32 v216, 1, v216\n"
        "v_and_b32 v218, 1, v4\n"
        "v_lshl_add_u32 v216, v218, 1, v216\n"
        "v_lshl_add_u32 v230, v216, 5, v217\n"
        "s_mov_b32 s67, 3584\n"
        "v_mad_u32_u24 v226, v223, s67, v222\n"
        "v_xor_b32 v227, 64, v226\n"
        "v_add_u32 v227, 28672, v227\n"
        "s_lshl_b32 s69, s73, 8\n"
        "s_add_u32 s40, s22, s69\n"
        "s_addc_u32 s41, s23, 0\n"
        "s_and_b32 s41, s41, 0xffff\n"
        "s_sub_u32 s42, 1605632, s69\n"
        "s_mov_b32 s43, 0x20000\n"
        "s_add_u32 s44, s24, s69\n"
        "s_addc_u32 s45, s25, 0\n"
        "s_and_b32 s45, s45, 0xffff\n"
        "s_sub_u32 s46, 1605632, s69\n"
        "s_mov_b32 s47, 0x20000\n"
        "s_lshl_b32 s67, s74, 3\n"
        "s_add_u32 s67, s67, s64\n"
        "s_mul_i32 s67, s67, 7168\n"
        "s_lshl_b32 s68, s75, 7\n"
        "s_add_u32 s67, s67, s68\n"
        "v_lshrrev_b32 v216, 3, v1\n"
        "v_mul_u32_u24 v216, 896, v216\n"
        "v_lshl_add_u32 v216, v5, 4, v216\n"
        "v_add_u32 v239, s67, v216\n"
        "v_lshlrev_b32 v2, 3, v3\n"
        "s_mov_b32 s77, 0x3f803f80\n"
        "s_mov_b32 s78, 0xffff0000\n"
        "s_mov_b32 s69, 0xffff\n"
        "v_xor_b32 v184, v4, v5\n"
        "v_lshlrev_b32 v184, 4, v184\n"
        "v_lshrrev_b32 v185, 3, v1\n"
        "v_lshl_add_u32 v184, v185, 7, v184\n"
        "v_xor_b32 v184, s99, v184\n"
        "v_add_u32 v208, s98, v184\n"
        "v_xor_b32 v209, 0x440, v208\n"
        "v_and_b32 v184, 1, v1\n"
        "v_lshlrev_b32 v184, 4, v184\n"
        "v_lshlrev_b32 v184, v184, s69\n"
        "v_bfe_i32 v185, v1, 1, 1\n"
        "v_and_b32 v213, v184, v185\n"
        "v_xor_b32 v212, v184, v213\n"
        "v_mov_b32 v218, 0x3727c5ac\n"
        "s_mov_b32 s69, 0x3b124925\n"
        "s_waitcnt vmcnt(4)\n"
        "v_dot2_f32_bf16 v214, v120, s77, 0\n"
        "s_lshl_b32 s66, s9, 7\n"
        "buffer_load_dwordx4 v[140:143], v6, s[32:35], s66 offen\n"
        "v_dot2_f32_bf16 v215, v120, v120, 0\n"
        "v_dot2_f32_bf16 v214, v121, s77, v214\n"
        "v_dot2_f32_bf16 v215, v121, v121, v215\n"
        "v_dot2_f32_bf16 v214, v122, s77, v214\n"
        "v_dot2_f32_bf16 v215, v122, v122, v215\n"
        "v_dot2_f32_bf16 v214, v123, s77, v214\n"
        "v_dot2_f32_bf16 v215, v123, v123, v215\n"
        "s_waitcnt vmcnt(4)\n"
        "v_dot2_f32_bf16 v214, v124, s77, v214\n"
        "s_lshl_b32 s66, s10, 7\n"
        "buffer_load_dwordx4 v[144:147], v6, s[32:35], s66 offen\n"
        "v_dot2_f32_bf16 v215, v124, v124, v215\n"
        "v_dot2_f32_bf16 v214, v125, s77, v214\n"
        "v_dot2_f32_bf16 v215, v125, v125, v215\n"
        "v_dot2_f32_bf16 v214, v126, s77, v214\n"
        "v_dot2_f32_bf16 v215, v126, v126, v215\n"
        "v_dot2_f32_bf16 v214, v127, s77, v214\n"
        "v_dot2_f32_bf16 v215, v127, v127, v215\n"
        "s_waitcnt vmcnt(4)\n"
        "v_dot2_f32_bf16 v214, v128, s77, v214\n"
        "s_lshl_b32 s66, s4, 7\n"
        "buffer_load_dwordx4 v[148:151], v7, s[32:35], s66 offen\n"
        "v_dot2_f32_bf16 v215, v128, v128, v215\n"
        "v_dot2_f32_bf16 v214, v129, s77, v214\n"
        "v_dot2_f32_bf16 v215, v129, v129, v215\n"
        "v_dot2_f32_bf16 v214, v130, s77, v214\n"
        "v_dot2_f32_bf16 v215, v130, v130, v215\n"
        "v_dot2_f32_bf16 v214, v131, s77, v214\n"
        "v_dot2_f32_bf16 v215, v131, v131, v215\n"
        "s_waitcnt vmcnt(4)\n"
        "v_dot2_f32_bf16 v214, v132, s77, v214\n"
        "s_lshl_b32 s66, s5, 7\n"
        "buffer_load_dwordx4 v[152:155], v7, s[32:35], s66 offen\n"
        "v_dot2_f32_bf16 v215, v132, v132, v215\n"
        "v_dot2_f32_bf16 v214, v133, s77, v214\n"
        "v_dot2_f32_bf16 v215, v133, v133, v215\n"
        "v_dot2_f32_bf16 v214, v134, s77, v214\n"
        "v_dot2_f32_bf16 v215, v134, v134, v215\n"
        "v_dot2_f32_bf16 v214, v135, s77, v214\n"
        "v_dot2_f32_bf16 v215, v135, v135, v215\n"
        "s_waitcnt vmcnt(4)\n"
        "v_dot2_f32_bf16 v214, v136, s77, v214\n"
        "s_lshl_b32 s66, s6, 7\n"
        "buffer_load_dwordx4 v[156:159], v7, s[32:35], s66 offen\n"
        "v_dot2_f32_bf16 v215, v136, v136, v215\n"
        "v_dot2_f32_bf16 v214, v137, s77, v214\n"
        "v_dot2_f32_bf16 v215, v137, v137, v215\n"
        "v_dot2_f32_bf16 v214, v138, s77, v214\n"
        "v_dot2_f32_bf16 v215, v138, v138, v215\n"
        "v_dot2_f32_bf16 v214, v139, s77, v214\n"
        "v_dot2_f32_bf16 v215, v139, v139, v215\n"
        "s_waitcnt vmcnt(4)\n"
        "v_dot2_f32_bf16 v214, v140, s77, v214\n"
        "s_lshl_b32 s66, s7, 7\n"
        "buffer_load_dwordx4 v[160:163], v7, s[32:35], s66 offen\n"
        "v_dot2_f32_bf16 v215, v140, v140, v215\n"
        "v_dot2_f32_bf16 v214, v141, s77, v214\n"
        "v_dot2_f32_bf16 v215, v141, v141, v215\n"
        "v_dot2_f32_bf16 v214, v142, s77, v214\n"
        "v_dot2_f32_bf16 v215, v142, v142, v215\n"
        "v_dot2_f32_bf16 v214, v143, s77, v214\n"
        "v_dot2_f32_bf16 v215, v143, v143, v215\n"
        "s_waitcnt vmcnt(4)\n"
        "v_dot2_f32_bf16 v214, v144, s77, v214\n"
        "s_lshl_b32 s66, s8, 7\n"
        "buffer_load_dwordx4 v[164:167], v7, s[32:35], s66 offen\n"
        "v_dot2_f32_bf16 v215, v144, v144, v215\n"
        "v_dot2_f32_bf16 v214, v145, s77, v214\n"
        "v_dot2_f32_bf16 v215, v145, v145, v215\n"
        "v_dot2_f32_bf16 v214, v146, s77, v214\n"
        "v_dot2_f32_bf16 v215, v146, v146, v215\n"
        "v_dot2_f32_bf16 v214, v147, s77, v214\n"
        "v_dot2_f32_bf16 v215, v147, v147, v215\n"
        "s_nop 1\n"
        "v_add_f32_dpp v214, v214, v214 quad_perm:[1,0,3,2] row_mask:0xf bank_mask:0xf\n"
        "v_add_f32_dpp v215, v215, v215 quad_perm:[1,0,3,2] row_mask:0xf bank_mask:0xf\n"
        "s_nop 0\n"
        "v_add_f32_dpp v214, v214, v214 quad_perm:[2,3,0,1] row_mask:0xf bank_mask:0xf\n"
        "v_add_f32_dpp v215, v215, v215 quad_perm:[2,3,0,1] row_mask:0xf bank_mask:0xf\n"
        "s_nop 0\n"
        "v_add_f32_dpp v214, v214, v214 row_half_mirror row_mask:0xf bank_mask:0xf\n"
        "v_add_f32_dpp v215, v215, v215 row_half_mirror row_mask:0xf bank_mask:0xf\n"
        "v_mul_f32 v214, s69, v214\n"
        "v_fma_f32 v215, v215, s69, v218\n"
        "v_fma_f32 v215, -v214, v214, v215\n"
        "v_max_f32 v215, v215, v218\n"
        "v_rsq_f32 v215, v215\n"
        "s_nop 0\n"
        "v_cvt_pk_bf16_f32 v184, v215, v215\n"
        "v_mul_f32 v200, -v214, v215\n"
        "v_and_b32 v185, s78, v184\n"
        "v_mul_f32 v201, -v214, v215\n"
        "v_sub_f32 v185, v215, v185\n"
        "v_mul_f32 v202, -v214, v215\n"
        "v_cvt_pk_bf16_f32 v185, v185, v185\n"
        "v_mul_f32 v203, -v214, v215\n"
        "v_and_b32 v206, v185, v212\n"
        "v_and_b32 v207, v185, v213\n"
        "v_and_b32 v204, v184, v212\n"
        "v_and_b32 v205, v184, v213\n"
        "v_mfma_f32_4x4x4_16b_bf16 v[184:187], v[206:207], v[120:121], v[200:203]\n"
        "v_mfma_f32_4x4x4_16b_bf16 v[188:191], v[206:207], v[122:123], v[200:203]\n"
        "s_nop 0\n"
        "v_mfma_f32_4x4x4_16b_bf16 v[184:187], v[204:205], v[120:121], v[184:187]\n"
        "v_mfma_f32_4x4x4_16b_bf16 v[188:191], v[204:205], v[122:123], v[188:191]\n"
        "v_mfma_f32_4x4x4_16b_bf16 v[192:195], v[206:207], v[124:125], v[200:203]\n"
        "v_mfma_f32_4x4x4_16b_bf16 v[196:199], v[206:207], v[126:127], v[200:203]\n"
        "s_nop 1\n"
        "v_cvt_pk_bf16_f32 v120, v184, v185\n"
        "v_cvt_pk_bf16_f32 v121, v186, v187\n"
        "v_mfma_f32_4x4x4_16b_bf16 v[192:195], v[204:205], v[124:125], v[192:195]\n"
        "v_mfma_f32_4x4x4_16b_bf16 v[196:199], v[204:205], v[126:127], v[196:199]\n"
        "v_cvt_pk_bf16_f32 v122, v188, v189\n"
        "v_cvt_pk_bf16_f32 v123, v190, v191\n"
        "ds_write_b128 v208, v[120:123] offset:0\n"
        "v_mfma_f32_4x4x4_16b_bf16 v[184:187], v[206:207], v[128:129], v[200:203]\n"
        "v_mfma_f32_4x4x4_16b_bf16 v[188:191], v[206:207], v[130:131], v[200:203]\n"
        "v_cvt_pk_bf16_f32 v124, v192, v193\n"
        "v_cvt_pk_bf16_f32 v125, v194, v195\n"
        "v_mfma_f32_4x4x4_16b_bf16 v[184:187], v[204:205], v[128:129], v[184:187]\n"
        "v_mfma_f32_4x4x4_16b_bf16 v[188:191], v[204:205], v[130:131], v[188:191]\n"
        "v_cvt_pk_bf16_f32 v126, v196, v197\n"
        "v_cvt_pk_bf16_f32 v127, v198, v199\n"
        "ds_write_b128 v208, v[124:127] offset:8192\n"
        "s_lshl_b32 s66, s9, 7\n"
        "buffer_load_dwordx4 v[168:171], v7, s[32:35], s66 offen\n"
        "v_mfma_f32_4x4x4_16b_bf16 v[192:195], v[206:207], v[132:133], v[200:203]\n"
        "v_mfma_f32_4x4x4_16b_bf16 v[196:199], v[206:207], v[134:135], v[200:203]\n"
        "v_cvt_pk_bf16_f32 v128, v184, v185\n"
        "v_cvt_pk_bf16_f32 v129, v186, v187\n"
        "v_mfma_f32_4x4x4_16b_bf16 v[192:195], v[204:205], v[132:133], v[192:195]\n"
        "v_mfma_f32_4x4x4_16b_bf16 v[196:199], v[204:205], v[134:135], v[196:199]\n"
        "v_cvt_pk_bf16_f32 v130, v188, v189\n"
        "v_cvt_pk_bf16_f32 v131, v190, v191\n"
        "ds_write_b128 v208, v[128:131] offset:16384\n"
        "v_mfma_f32_4x4x4_16b_bf16 v[184:187], v[206:207], v[136:137], v[200:203]\n"
        "v_mfma_f32_4x4x4_16b_bf16 v[188:191], v[206:207], v[138:139], v[200:203]\n"
        "v_cvt_pk_bf16_f32 v132, v192, v193\n"
        "v_cvt_pk_bf16_f32 v133, v194, v195\n"
        "v_mfma_f32_4x4x4_16b_bf16 v[184:187], v[204:205], v[136:137], v[184:187]\n"
        "v_mfma_f32_4x4x4_16b_bf16 v[188:191], v[204:205], v[138:139], v[188:191]\n"
        "v_cvt_pk_bf16_f32 v134, v196, v197\n"
        "v_cvt_pk_bf16_f32 v135, v198, v199\n"
        "ds_write_b128 v208, v[132:135] offset:24576\n"
        "s_lshl_b32 s66, s10, 7\n"
        "buffer_load_dwordx4 v[172:175], v7, s[32:35], s66 offen\n"
        "v_mfma_f32_4x4x4_16b_bf16 v[192:195], v[206:207], v[140:141], v[200:203]\n"
        "v_mfma_f32_4x4x4_16b_bf16 v[196:199], v[206:207], v[142:143], v[200:203]\n"
        "v_cvt_pk_bf16_f32 v136, v184, v185\n"
        "v_cvt_pk_bf16_f32 v137, v186, v187\n"
        "v_mfma_f32_4x4x4_16b_bf16 v[192:195], v[204:205], v[140:141], v[192:195]\n"
        "v_mfma_f32_4x4x4_16b_bf16 v[196:199], v[204:205], v[142:143], v[196:199]\n"
        "v_cvt_pk_bf16_f32 v138, v188, v189\n"
        "v_cvt_pk_bf16_f32 v139, v190, v191\n"
        "ds_write_b128 v208, v[136:139] offset:32768\n"
        "v_mfma_f32_4x4x4_16b_bf16 v[184:187], v[206:207], v[144:145], v[200:203]\n"
        "v_mfma_f32_4x4x4_16b_bf16 v[188:191], v[206:207], v[146:147], v[200:203]\n"
        "v_cvt_pk_bf16_f32 v140, v192, v193\n"
        "v_cvt_pk_bf16_f32 v141, v194, v195\n"
        "v_mfma_f32_4x4x4_16b_bf16 v[184:187], v[204:205], v[144:145], v[184:187]\n"
        "v_mfma_f32_4x4x4_16b_bf16 v[188:191], v[204:205], v[146:147], v[188:191]\n"
        "v_cvt_pk_bf16_f32 v142, v196, v197\n"
        "v_cvt_pk_bf16_f32 v143, v198, v199\n"
        "ds_write_b128 v208, v[140:143] offset:40960\n"
        "buffer_load_dwordx2 v[244:245], off, s[60:63], 0 sc1\n"
        "v_cvt_pk_bf16_f32 v144, v184, v185\n"
        "v_cvt_pk_bf16_f32 v145, v186, v187\n"
        "v_cvt_pk_bf16_f32 v146, v188, v189\n"
        "v_cvt_pk_bf16_f32 v147, v190, v191\n"
        "ds_write_b128 v208, v[144:147] offset:49152\n"
        "s_waitcnt vmcnt(7)\n"
        "v_dot2_f32_bf16 v216, v148, s77, 0\n"
        "s_add_u32 m0, s81, 0\n"
        "s_mul_i32 s66, s4, 172032\n"
        "buffer_load_dwordx4 v248, s[36:39], s66 offen lds\n"
        "v_dot2_f32_bf16 v217, v148, v148, 0\n"
        "v_dot2_f32_bf16 v216, v149, s77, v216\n"
        "v_dot2_f32_bf16 v217, v149, v149, v217\n"
        "v_dot2_f32_bf16 v216, v150, s77, v216\n"
        "v_dot2_f32_bf16 v217, v150, v150, v217\n"
        "v_dot2_f32_bf16 v216, v151, s77, v216\n"
        "v_dot2_f32_bf16 v217, v151, v151, v217\n"
        "s_waitcnt vmcnt(7)\n"
        "v_dot2_f32_bf16 v216, v152, s77, v216\n"
        "v_dot2_f32_bf16 v217, v152, v152, v217\n"
        "v_dot2_f32_bf16 v216, v153, s77, v216\n"
        "v_dot2_f32_bf16 v217, v153, v153, v217\n"
        "v_dot2_f32_bf16 v216, v154, s77, v216\n"
        "v_dot2_f32_bf16 v217, v154, v154, v217\n"
        "v_dot2_f32_bf16 v216, v155, s77, v216\n"
        "v_dot2_f32_bf16 v217, v155, v155, v217\n"
        "s_waitcnt vmcnt(6)\n"
        "v_dot2_f32_bf16 v216, v156, s77, v216\n"
        "s_add_u32 m0, s81, 8192\n"
        "s_mul_i32 s66, s83, 172032\n"
        "s_mul_i32 s67, s82, 1\n"
        "s_add_u32 s66, s66, s67\n"
        "buffer_load_dwordx4 v248, s[36:39], s66 offen lds\n"
        "v_dot2_f32_bf16 v217, v156, v156, v217\n"
        "v_dot2_f32_bf16 v216, v157, s77, v216\n"
        "v_dot2_f32_bf16 v217, v157, v157, v217\n"
        "v_dot2_f32_bf16 v216, v158, s77, v216\n"
        "v_dot2_f32_bf16 v217, v158, v158, v217\n"
        "v_dot2_f32_bf16 v216, v159, s77, v216\n"
        "v_dot2_f32_bf16 v217, v159, v159, v217\n"
        "s_waitcnt vmcnt(6)\n"
        "v_dot2_f32_bf16 v216, v160, s77, v216\n"
        "v_dot2_f32_bf16 v217, v160, v160, v217\n"
        "v_dot2_f32_bf16 v216, v161, s77, v216\n"
        "v_dot2_f32_bf16 v217, v161, v161, v217\n"
        "v_dot2_f32_bf16 v216, v162, s77, v216\n"
        "v_dot2_f32_bf16 v217, v162, v162, v217\n"
        "v_dot2_f32_bf16 v216, v163, s77, v216\n"
        "v_dot2_f32_bf16 v217, v163, v163, v217\n"
        "s_waitcnt vmcnt(5)\n"
        "v_dot2_f32_bf16 v216, v164, s77, v216\n"
        "s_add_u32 m0, s81, 16384\n"
        "s_mul_i32 s66, s84, 172032\n"
        "s_mul_i32 s67, s82, 2\n"
        "s_add_u32 s66, s66, s67\n"
        "buffer_load_dwordx4 v248, s[36:39], s66 offen lds\n"
        "v_dot2_f32_bf16 v217, v164, v164, v217\n"
        "v_dot2_f32_bf16 v216, v165, s77, v216\n"
        "v_dot2_f32_bf16 v217, v165, v165, v217\n"
        "v_dot2_f32_bf16 v216, v166, s77, v216\n"
        "v_dot2_f32_bf16 v217, v166, v166, v217\n"
        "v_dot2_f32_bf16 v216, v167, s77, v216\n"
        "v_dot2_f32_bf16 v217, v167, v167, v217\n"
        "s_waitcnt vmcnt(5)\n"
        "v_dot2_f32_bf16 v216, v168, s77, v216\n"
        "v_dot2_f32_bf16 v217, v168, v168, v217\n"
        "v_dot2_f32_bf16 v216, v169, s77, v216\n"
        "v_dot2_f32_bf16 v217, v169, v169, v217\n"
        "v_dot2_f32_bf16 v216, v170, s77, v216\n"
        "v_dot2_f32_bf16 v217, v170, v170, v217\n"
        "v_dot2_f32_bf16 v216, v171, s77, v216\n"
        "v_dot2_f32_bf16 v217, v171, v171, v217\n"
        "s_waitcnt vmcnt(4)\n"
        "v_dot2_f32_bf16 v216, v172, s77, v216\n"
        "s_add_u32 m0, s81, 24576\n"
        "s_mul_i32 s66, s85, 172032\n"
        "s_mul_i32 s67, s82, 3\n"
        "s_add_u32 s66, s66, s67\n"
        "buffer_load_dwordx4 v248, s[36:39], s66 offen lds\n"
        "v_dot2_f32_bf16 v217, v172, v172, v217\n"
        "v_dot2_f32_bf16 v216, v173, s77, v216\n"
        "v_dot2_f32_bf16 v217, v173, v173, v217\n"
        "v_dot2_f32_bf16 v216, v174, s77, v216\n"
        "v_dot2_f32_bf16 v217, v174, v174, v217\n"
        "v_dot2_f32_bf16 v216, v175, s77, v216\n"
        "v_dot2_f32_bf16 v217, v175, v175, v217\n"
        "s_nop 1\n"
        "v_add_f32_dpp v216, v216, v216 quad_perm:[1,0,3,2] row_mask:0xf bank_mask:0xf\n"
        "v_add_f32_dpp v217, v217, v217 quad_perm:[1,0,3,2] row_mask:0xf bank_mask:0xf\n"
        "s_nop 0\n"
        "v_add_f32_dpp v216, v216, v216 quad_perm:[2,3,0,1] row_mask:0xf bank_mask:0xf\n"
        "v_add_f32_dpp v217, v217, v217 quad_perm:[2,3,0,1] row_mask:0xf bank_mask:0xf\n"
        "s_nop 0\n"
        "v_add_f32_dpp v216, v216, v216 row_half_mirror row_mask:0xf bank_mask:0xf\n"
        "v_add_f32_dpp v217, v217, v217 row_half_mirror row_mask:0xf bank_mask:0xf\n"
        "v_mul_f32 v216, s69, v216\n"
        "v_fma_f32 v217, v217, s69, v218\n"
        "v_fma_f32 v217, -v216, v216, v217\n"
        "v_max_f32 v217, v217, v218\n"
        "v_rsq_f32 v217, v217\n"
        "s_nop 0\n"
        "v_cvt_pk_bf16_f32 v186, v217, v217\n"
        "v_mul_f32 v176, -v216, v217\n"
        "v_and_b32 v187, s78, v186\n"
        "v_mul_f32 v177, -v216, v217\n"
        "v_sub_f32 v187, v217, v187\n"
        "v_mul_f32 v178, -v216, v217\n"
        "v_cvt_pk_bf16_f32 v187, v187, v187\n"
        "v_mul_f32 v179, -v216, v217\n"
        "v_and_b32 v182, v187, v212\n"
        "v_and_b32 v183, v187, v213\n"
        "v_and_b32 v180, v186, v212\n"
        "v_and_b32 v181, v186, v213\n"
        "v_mfma_f32_4x4x4_16b_bf16 v[184:187], v[182:183], v[148:149], v[176:179]\n"
        "v_mfma_f32_4x4x4_16b_bf16 v[188:191], v[182:183], v[150:151], v[176:179]\n"
        "s_nop 0\n"
        "v_mfma_f32_4x4x4_16b_bf16 v[184:187], v[180:181], v[148:149], v[184:187]\n"
        "v_mfma_f32_4x4x4_16b_bf16 v[188:191], v[180:181], v[150:151], v[188:191]\n"
        "v_mfma_f32_4x4x4_16b_bf16 v[192:195], v[182:183], v[152:153], v[176:179]\n"
        "v_mfma_f32_4x4x4_16b_bf16 v[196:199], v[182:183], v[154:155], v[176:179]\n"
        "s_nop 1\n"
        "v_cvt_pk_bf16_f32 v148, v184, v185\n"
        "v_cvt_pk_bf16_f32 v149, v186, v187\n"
        "v_mfma_f32_4x4x4_16b_bf16 v[192:195], v[180:181], v[152:153], v[192:195]\n"
        "v_mfma_f32_4x4x4_16b_bf16 v[196:199], v[180:181], v[154:155], v[196:199]\n"
        "v_cvt_pk_bf16_f32 v150, v188, v189\n"
        "v_cvt_pk_bf16_f32 v151, v190, v191\n"
        "ds_write_b128 v209, v[148:151] offset:0\n"
        "v_mfma_f32_4x4x4_16b_bf16 v[184:187], v[182:183], v[156:157], v[176:179]\n"
        "v_mfma_f32_4x4x4_16b_bf16 v[188:191], v[182:183], v[158:159], v[176:179]\n"
        "v_cvt_pk_bf16_f32 v152, v192, v193\n"
        "v_cvt_pk_bf16_f32 v153, v194, v195\n"
        "v_mfma_f32_4x4x4_16b_bf16 v[184:187], v[180:181], v[156:157], v[184:187]\n"
        "v_mfma_f32_4x4x4_16b_bf16 v[188:191], v[180:181], v[158:159], v[188:191]\n"
        "v_cvt_pk_bf16_f32 v154, v196, v197\n"
        "v_cvt_pk_bf16_f32 v155, v198, v199\n"
        "ds_write_b128 v209, v[152:155] offset:8192\n"
        "s_add_u32 m0, s81, 32768\n"
        "s_mul_i32 s66, s86, 172032\n"
        "buffer_load_dwordx4 v248, s[36:39], s66 offen lds\n"
        "v_mfma_f32_4x4x4_16b_bf16 v[192:195], v[182:183], v[160:161], v[176:179]\n"
        "v_mfma_f32_4x4x4_16b_bf16 v[196:199], v[182:183], v[162:163], v[176:179]\n"
        "v_cvt_pk_bf16_f32 v156, v184, v185\n"
        "v_cvt_pk_bf16_f32 v157, v186, v187\n"
        "v_mfma_f32_4x4x4_16b_bf16 v[192:195], v[180:181], v[160:161], v[192:195]\n"
        "v_mfma_f32_4x4x4_16b_bf16 v[196:199], v[180:181], v[162:163], v[196:199]\n"
        "v_cvt_pk_bf16_f32 v158, v188, v189\n"
        "v_cvt_pk_bf16_f32 v159, v190, v191\n"
        "ds_write_b128 v209, v[156:159] offset:16384\n"
        "v_mfma_f32_4x4x4_16b_bf16 v[184:187], v[182:183], v[164:165], v[176:179]\n"
        "v_mfma_f32_4x4x4_16b_bf16 v[188:191], v[182:183], v[166:167], v[176:179]\n"
        "v_cvt_pk_bf16_f32 v160, v192, v193\n"
        "v_cvt_pk_bf16_f32 v161, v194, v195\n"
        "v_mfma_f32_4x4x4_16b_bf16 v[184:187], v[180:181], v[164:165], v[184:187]\n"
        "v_mfma_f32_4x4x4_16b_bf16 v[188:191], v[180:181], v[166:167], v[188:191]\n"
        "v_cvt_pk_bf16_f32 v162, v196, v197\n"
        "v_cvt_pk_bf16_f32 v163, v198, v199\n"
        "ds_write_b128 v209, v[160:163] offset:24576\n"
        "s_add_u32 m0, s81, 40960\n"
        "s_mul_i32 s66, s87, 172032\n"
        "s_mul_i32 s67, s82, 1\n"
        "s_add_u32 s66, s66, s67\n"
        "buffer_load_dwordx4 v248, s[36:39], s66 offen lds\n"
        "v_mfma_f32_4x4x4_16b_bf16 v[192:195], v[182:183], v[168:169], v[176:179]\n"
        "v_mfma_f32_4x4x4_16b_bf16 v[196:199], v[182:183], v[170:171], v[176:179]\n"
        "v_cvt_pk_bf16_f32 v164, v184, v185\n"
        "v_cvt_pk_bf16_f32 v165, v186, v187\n"
        "v_mfma_f32_4x4x4_16b_bf16 v[192:195], v[180:181], v[168:169], v[192:195]\n"
        "v_mfma_f32_4x4x4_16b_bf16 v[196:199], v[180:181], v[170:171], v[196:199]\n"
        "v_cvt_pk_bf16_f32 v166, v188, v189\n"
        "v_cvt_pk_bf16_f32 v167, v190, v191\n"
        "ds_write_b128 v209, v[164:167] offset:32768\n"
        "v_mfma_f32_4x4x4_16b_bf16 v[184:187], v[182:183], v[172:173], v[176:179]\n"
        "v_mfma_f32_4x4x4_16b_bf16 v[188:191], v[182:183], v[174:175], v[176:179]\n"
        "v_cvt_pk_bf16_f32 v168, v192, v193\n"
        "v_cvt_pk_bf16_f32 v169, v194, v195\n"
        "v_mfma_f32_4x4x4_16b_bf16 v[184:187], v[180:181], v[172:173], v[184:187]\n"
        "v_mfma_f32_4x4x4_16b_bf16 v[188:191], v[180:181], v[174:175], v[188:191]\n"
        "v_cvt_pk_bf16_f32 v170, v196, v197\n"
        "v_cvt_pk_bf16_f32 v171, v198, v199\n"
        "ds_write_b128 v209, v[168:171] offset:40960\n"
        "buffer_load_dword v247, v246, s[52:55], 0 offen\n"
        "v_cvt_pk_bf16_f32 v172, v184, v185\n"
        "v_cvt_pk_bf16_f32 v173, v186, v187\n"
        "v_cvt_pk_bf16_f32 v174, v188, v189\n"
        "v_cvt_pk_bf16_f32 v175, v190, v191\n"
        "ds_write_b128 v209, v[172:175] offset:49152\n"
        "s_cbranch_execnz Lgo\n"
        "Lback:\n"
        "s_setpc_b64 vcc\n"
        "Lgo:\n"
        "s_cmp_eq_u32 s75, 7\n"
        "s_cbranch_scc1 Lrole_Y\n"
        "s_cmp_ge_u32 s15, 7\n"
        "s_cbranch_scc1 Lrole_V\n"
        "Lrole_K:\n"
        "s_cmp_eq_u32 s96, 0\n"
        "s_cbranch_scc1 Lrole_G\n"
        "s_waitcnt vmcnt(5) lgkmcnt(0)\n"
        "s_barrier\n"
        "ds_read_b128 v[8:11], v250 offset:0\n"
        "v_xor_b32 v232, 0x0, v230\n"
        "ds_read_b64_tr_b16 v[184:185], v232 offset:0\n"
        "ds_read_b64_tr_b16 v[186:187], v232 offset:512\n"
        "ds_read_b128 v[12:15], v252 offset:0\n"
        "v_xor_b32 v233, 0x20, v230\n"
        "ds_read_b64_tr_b16 v[188:189], v233 offset:0\n"
        "ds_read_b64_tr_b16 v[190:191], v233 offset:512\n"
        "v_xor_b32 v234, 0x40, v230\n"
        "ds_read_b64_tr_b16 v[192:193], v234 offset:0\n"
        "ds_read_b64_tr_b16 v[194:195], v234 offset:512\n"
        "v_xor_b32 v235, 0x60, v230\n"
        "ds_read_b64_tr_b16 v[196:197], v235 offset:0\n"
        "ds_read_b64_tr_b16 v[198:199], v235 offset:512\n"
        "ds_read_b64_tr_b16 v[200:201], v232 offset:4096\n"
        "ds_read_b64_tr_b16 v[202:203], v232 offset:4608\n"
        "s_setprio 0\n"
        "s_waitcnt lgkmcnt(6)\n"
        "v_mfma_f32_16x16x32_bf16 v[120:123], v[184:187], v[8:11], 0\n"
        "ds_read_b64_tr_b16 v[204:205], v233 offset:4096\n"
        "ds_read_b128 v[16:19], v251 offset:0\n"
        "v_mfma_f32_16x16x32_bf16 v[124:127], v[184:187], v[12:15], 0\n"
        "ds_read_b64_tr_b16 v[206:207], v233 offset:4608\n"
        "ds_read_b128 v[20:23], v253 offset:0\n"
        "v_mfma_f32_16x16x32_bf16 v[128:131], v[188:191], v[8:11], 0\n"
        "ds_read_b64_tr_b16 v[208:209], v234 offset:4096\n"
        "ds_read_b128 v[24:27], v250 offset:8192\n"
        "v_mfma_f32_16x16x32_bf16 v[132:135], v[188:191], v[12:15], 0\n"
        "ds_read_b64_tr_b16 v[210:211], v234 offset:4608\n"
        "s_cbranch_execz Lback\n"
        "ds_read_b128 v[32:35], v251 offset:8192\n"
        "s_waitcnt lgkmcnt(10)\n"
        "v_mfma_f32_16x16x32_bf16 v[136:139], v[192:195], v[8:11], 0\n"
        "ds_read_b64_tr_b16 v[184:185], v235 offset:4096\n"
        "ds_read_b128 v[40:43], v250 offset:16384\n"
        "v_mfma_f32_16x16x32_bf16 v[140:143], v[192:195], v[12:15], 0\n"
        "ds_read_b64_tr_b16 v[186:187], v235 offset:4608\n"
        "ds_read_b128 v[48:51], v251 offset:16384\n"
        "v_mfma_f32_16x16x32_bf16 v[144:147], v[196:199], v[8:11], 0\n"
        "ds_read_b64_tr_b16 v[188:189], v232 offset:8192\n"
        "s_waitcnt lgkmcnt(8)\n"
        "ds_read_b128 v[56:59], v250 offset:24576\n"
        "v_mfma_f32_16x16x32_bf16 v[148:151], v[196:199], v[12:15], 0\n"
        "ds_read_b64_tr_b16 v[190:191], v232 offset:8704\n"
        "ds_read_b128 v[64:67], v251 offset:24576\n"
        "v_mfma_f32_16x16x32_bf16 v[120:123], v[200:203], v[16:19], v[120:123]\n"
        "ds_read_b64_tr_b16 v[192:193], v233 offset:8192\n"
        "ds_read_b128 v[28:31], v252 offset:8192\n"
        "v_mfma_f32_16x16x32_bf16 v[124:127], v[200:203], v[20:23], v[124:127]\n"
        "ds_read_b64_tr_b16 v[194:195], v233 offset:8704\n"
        "ds_read_b128 v[72:75], v250 offset:32768\n"
        "v_mfma_f32_16x16x32_bf16 v[128:131], v[204:207], v[16:19], v[128:131]\n"
        "s_waitcnt lgkmcnt(8)\n"
        "ds_read_b64_tr_b16 v[196:197], v234 offset:8192\n"
        "ds_read_b128 v[80:83], v251 offset:32768\n"
        "v_mfma_f32_16x16x32_bf16 v[132:135], v[204:207], v[20:23], v[132:135]\n"
        "ds_read_b64_tr_b16 v[198:199], v234 offset:8704\n"
        "ds_read_b128 v[88:91], v250 offset:40960\n"
        "v_mfma_f32_16x16x32_bf16 v[136:139], v[208:211], v[16:19], v[136:139]\n"
        "ds_read_b64_tr_b16 v[200:201], v235 offset:8192\n"
        "ds_read_b128 v[96:99], v251 offset:40960\n"
        "v_mfma_f32_16x16x32_bf16 v[140:143], v[208:211], v[20:23], v[140:143]\n"
        "ds_read_b64_tr_b16 v[202:203], v235 offset:8704\n"
        "s_waitcnt lgkmcnt(8)\n"
        "ds_read_b128 v[104:107], v250 offset:49152\n"
        "v_mfma_f32_16x16x32_bf16 v[144:147], v[184:187], v[16:19], v[144:147]\n"
        "ds_read_b64_tr_b16 v[204:205], v232 offset:12288\n"
        "ds_read_b128 v[112:115], v251 offset:49152\n"
        "v_mfma_f32_16x16x32_bf16 v[148:151], v[184:187], v[20:23], v[148:151]\n"
        "ds_read_b64_tr_b16 v[206:207], v232 offset:12800\n"
        "ds_read_b128 v[36:39], v253 offset:8192\n"
        "s_waitcnt vmcnt(4) lgkmcnt(2)\n"
        "s_barrier\n"
        "v_mfma_f32_16x16x32_bf16 v[120:123], v[188:191], v[24:27], v[120:123]\n"
        "ds_read_b64_tr_b16 v[208:209], v233 offset:12288\n"
        "v_mfma_f32_16x16x32_bf16 v[124:127], v[188:191], v[28:31], v[124:127]\n"
        "ds_read_b64_tr_b16 v[210:211], v233 offset:12800\n"
        "ds_read_b128 v[44:47], v252 offset:16384\n"
        "v_mfma_f32_16x16x32_bf16 v[128:131], v[192:195], v[24:27], v[128:131]\n"
        "ds_read_b64_tr_b16 v[184:185], v234 offset:12288\n"
        "v_mfma_f32_16x16x32_bf16 v[132:135], v[192:195], v[28:31], v[132:135]\n"
        "ds_read_b64_tr_b16 v[186:187], v234 offset:12800\n"
        "ds_read_b128 v[52:55], v253 offset:16384\n"
        "v_mfma_f32_16x16x32_bf16 v[136:139], v[196:199], v[24:27], v[136:139]\n"
        "ds_read_b64_tr_b16 v[188:189], v235 offset:12288\n"
        "v_mfma_f32_16x16x32_bf16 v[140:143], v[196:199], v[28:31], v[140:143]\n"
        "ds_read_b64_tr_b16 v[190:191], v235 offset:12800\n"
        "ds_read_b128 v[60:63], v252 offset:24576\n"
        "v_mfma_f32_16x16x32_bf16 v[144:147], v[200:203], v[24:27], v[144:147]\n"
        "ds_read_b64_tr_b16 v[192:193], v232 offset:16384\n"
        "v_mfma_f32_16x16x32_bf16 v[148:151], v[200:203], v[28:31], v[148:151]\n"
        "ds_read_b64_tr_b16 v[194:195], v232 offset:16896\n"
        "ds_read_b128 v[68:71], v253 offset:24576\n"
        "s_waitcnt lgkmcnt(10)\n"
        "v_mfma_f32_16x16x32_bf16 v[120:123], v[204:207], v[32:35], v[120:123]\n"
        "ds_read_b64_tr_b16 v[196:197], v233 offset:16384\n"
        "v_mfma_f32_16x16x32_bf16 v[124:127], v[204:207], v[36:39], v[124:127]\n"
        "ds_read_b64_tr_b16 v[198:199], v233 offset:16896\n"
        "ds_read_b128 v[76:79], v252 offset:32768\n"
        "v_mfma_f32_16x16x32_bf16 v[128:131], v[208:211], v[32:35], v[128:131]\n"
        "ds_read_b64_tr_b16 v[200:201], v234 offset:16384\n"
        "v_mfma_f32_16x16x32_bf16 v[132:135], v[208:211], v[36:39], v[132:135]\n"
        "ds_read_b64_tr_b16 v[202:203], v234 offset:16896\n"
        "s_waitcnt lgkmcnt(8)\n"
        "ds_read_b128 v[84:87], v253 offset:32768\n"
        "v_mfma_f32_16x16x32_bf16 v[136:139], v[184:187], v[32:35], v[136:139]\n"
        "ds_read_b64_tr_b16 v[204:205], v235 offset:16384\n"
        "v_mfma_f32_16x16x32_bf16 v[140:143], v[184:187], v[36:39], v[140:143]\n"
        "ds_read_b64_tr_b16 v[206:207], v235 offset:16896\n"
        "ds_read_b128 v[92:95], v252 offset:40960\n"
        "v_mfma_f32_16x16x32_bf16 v[144:147], v[188:191], v[32:35], v[144:147]\n"
        "ds_read_b64_tr_b16 v[208:209], v232 offset:20480\n"
        "v_mfma_f32_16x16x32_bf16 v[148:151], v[188:191], v[36:39], v[148:151]\n"
        "ds_read_b64_tr_b16 v[210:211], v232 offset:20992\n"
        "ds_read_b128 v[100:103], v253 offset:40960\n"
        "s_waitcnt vmcnt(2)\n"
        "s_barrier\n"
        "s_waitcnt lgkmcnt(10)\n"
        "v_mfma_f32_16x16x32_bf16 v[120:123], v[192:195], v[40:43], v[120:123]\n"
        "ds_read_b64_tr_b16 v[184:185], v233 offset:20480\n"
        "v_mfma_f32_16x16x32_bf16 v[124:127], v[192:195], v[44:47], v[124:127]\n"
        "ds_read_b64_tr_b16 v[186:187], v233 offset:20992\n"
        "ds_read_b128 v[108:111], v252 offset:49152\n"
        "v_mfma_f32_16x16x32_bf16 v[128:131], v[196:199], v[40:43], v[128:131]\n"
        "ds_read_b64_tr_b16 v[188:189], v234 offset:20480\n"
        "v_mfma_f32_16x16x32_bf16 v[132:135], v[196:199], v[44:47], v[132:135]\n"
        "ds_read_b64_tr_b16 v[190:191], v234 offset:20992\n"
        "s_waitcnt lgkmcnt(8)\n"
        "ds_read_b128 v[116:119], v253 offset:49152\n"
        "v_mfma_f32_16x16x32_bf16 v[136:139], v[200:203], v[40:43], v[136:139]\n"
        "ds_read_b64_tr_b16 v[192:193], v235 offset:20480\n"
        "v_mfma_f32_16x16x32_bf16 v[140:143], v[200:203], v[44:47], v[140:143]\n"
        "ds_read_b64_tr_b16 v[194:195], v235 offset:20992\n"
        "v_mfma_f32_16x16x32_bf16 v[144:147], v[204:207], v[40:43], v[144:147]\n"
        "ds_read_b64_tr_b16 v[196:197], v232 offset:24576\n"
        "v_mfma_f32_16x16x32_bf16 v[148:151], v[204:207], v[44:47], v[148:151]\n"
        "ds_read_b64_tr_b16 v[198:199], v232 offset:25088\n"
        "s_waitcnt lgkmcnt(8)\n"
        "v_mfma_f32_16x16x32_bf16 v[120:123], v[208:211], v[48:51], v[120:123]\n"
        "ds_read_b64_tr_b16 v[200:201], v233 offset:24576\n"
        "v_mfma_f32_16x16x32_bf16 v[124:127], v[208:211], v[52:55], v[124:127]\n"
        "ds_read_b64_tr_b16 v[202:203], v233 offset:25088\n"
        "v_mfma_f32_16x16x32_bf16 v[128:131], v[184:187], v[48:51], v[128:131]\n"
        "ds_read_b64_tr_b16 v[204:205], v234 offset:24576\n"
        "v_mfma_f32_16x16x32_bf16 v[132:135], v[184:187], v[52:55], v[132:135]\n"
        "ds_read_b64_tr_b16 v[206:207], v234 offset:25088\n"
        "s_waitcnt lgkmcnt(6)\n"
        "v_mfma_f32_16x16x32_bf16 v[136:139], v[188:191], v[48:51], v[136:139]\n"
        "ds_read_b64_tr_b16 v[208:209], v235 offset:24576\n"
        "v_mfma_f32_16x16x32_bf16 v[140:143], v[188:191], v[52:55], v[140:143]\n"
        "ds_read_b64_tr_b16 v[210:211], v235 offset:25088\n"
        "v_mfma_f32_16x16x32_bf16 v[144:147], v[192:195], v[48:51], v[144:147]\n"
        "ds_read_b64_tr_b16 v[184:185], v232 offset:28672\n"
        "v_mfma_f32_16x16x32_bf16 v[148:151], v[192:195], v[52:55], v[148:151]\n"
        "ds_read_b64_tr_b16 v[186:187], v232 offset:29184\n"
        "s_waitcnt lgkmcnt(6)\n"
        "v_mfma_f32_16x16x32_bf16 v[120:123], v[196:199], v[56:59], v[120:123]\n"
        "ds_read_b64_tr_b16 v[188:189], v233 offset:28672\n"
        "v_mfma_f32_16x16x32_bf16 v[124:127], v[196:199], v[60:63], v[124:127]\n"
        "ds_read_b64_tr_b16 v[190:191], v233 offset:29184\n"
        "v_mfma_f32_16x16x32_bf16 v[128:131], v[200:203], v[56:59], v[128:131]\n"
        "ds_read_b64_tr_b16 v[192:193], v234 offset:28672\n"
        "v_mfma_f32_16x16x32_bf16 v[132:135], v[200:203], v[60:63], v[132:135]\n"
        "ds_read_b64_tr_b16 v[194:195], v234 offset:29184\n"
        "s_waitcnt lgkmcnt(6)\n"
        "v_mfma_f32_16x16x32_bf16 v[136:139], v[204:207], v[56:59], v[136:139]\n"
        "ds_read_b64_tr_b16 v[196:197], v235 offset:28672\n"
        "v_mfma_f32_16x16x32_bf16 v[140:143], v[204:207], v[60:63], v[140:143]\n"
        "ds_read_b64_tr_b16 v[198:199], v235 offset:29184\n"
        "v_mfma_f32_16x16x32_bf16 v[144:147], v[208:211], v[56:59], v[144:147]\n"
        "ds_read_b64_tr_b16 v[200:201], v232 offset:32768\n"
        "v_mfma_f32_16x16x32_bf16 v[148:151], v[208:211], v[60:63], v[148:151]\n"
        "ds_read_b64_tr_b16 v[202:203], v232 offset:33280\n"
        "s_waitcnt lgkmcnt(6)\n"
        "v_mfma_f32_16x16x32_bf16 v[120:123], v[184:187], v[64:67], v[120:123]\n"
        "ds_read_b64_tr_b16 v[204:205], v233 offset:32768\n"
        "v_mfma_f32_16x16x32_bf16 v[124:127], v[184:187], v[68:71], v[124:127]\n"
        "ds_read_b64_tr_b16 v[206:207], v233 offset:33280\n"
        "v_mfma_f32_16x16x32_bf16 v[128:131], v[188:191], v[64:67], v[128:131]\n"
        "ds_read_b64_tr_b16 v[208:209], v234 offset:32768\n"
        "v_mfma_f32_16x16x32_bf16 v[132:135], v[188:191], v[68:71], v[132:135]\n"
        "ds_read_b64_tr_b16 v[210:211], v234 offset:33280\n"
        "s_waitcnt lgkmcnt(6)\n"
        "v_mfma_f32_16x16x32_bf16 v[136:139], v[192:195], v[64:67], v[136:139]\n"
        "ds_read_b64_tr_b16 v[184:185], v235 offset:32768\n"
        "v_mfma_f32_16x16x32_bf16 v[140:143], v[192:195], v[68:71], v[140:143]\n"
        "ds_read_b64_tr_b16 v[186:187], v235 offset:33280\n"
        "v_mfma_f32_16x16x32_bf16 v[144:147], v[196:199], v[64:67], v[144:147]\n"
        "ds_read_b64_tr_b16 v[188:189], v232 offset:36864\n"
        "v_mfma_f32_16x16x32_bf16 v[148:151], v[196:199], v[68:71], v[148:151]\n"
        "ds_read_b64_tr_b16 v[190:191], v232 offset:37376\n"
        "s_waitcnt vmcnt(1)\n"
        "s_barrier\n"
        "s_waitcnt lgkmcnt(6)\n"
        "v_mfma_f32_16x16x32_bf16 v[120:123], v[200:203], v[72:75], v[120:123]\n"
        "ds_read_b64_tr_b16 v[192:193], v233 offset:36864\n"
        "v_mfma_f32_16x16x32_bf16 v[124:127], v[200:203], v[76:79], v[124:127]\n"
        "ds_read_b64_tr_b16 v[194:195], v233 offset:37376\n"
        "v_mfma_f32_16x16x32_bf16 v[128:131], v[204:207], v[72:75], v[128:131]\n"
        "ds_read_b64_tr_b16 v[196:197], v234 offset:36864\n"
        "v_mfma_f32_16x16x32_bf16 v[132:135], v[204:207], v[76:79], v[132:135]\n"
        "ds_read_b64_tr_b16 v[198:199], v234 offset:37376\n"
        "s_waitcnt lgkmcnt(6)\n"
        "v_mfma_f32_16x16x32_bf16 v[136:139], v[208:211], v[72:75], v[136:139]\n"
        "ds_read_b64_tr_b16 v[200:201], v235 offset:36864\n"
        "v_mfma_f32_16x16x32_bf16 v[140:143], v[208:211], v[76:79], v[140:143]\n"
        "ds_read_b64_tr_b16 v[202:203], v235 offset:37376\n"
        "v_mfma_f32_16x16x32_bf16 v[144:147], v[184:187], v[72:75], v[144:147]\n"
        "ds_read_b64_tr_b16 v[204:205], v232 offset:40960\n"
        "v_mfma_f32_16x16x32_bf16 v[148:151], v[184:187], v[76:79], v[148:151]\n"
        "ds_read_b64_tr_b16 v[206:207], v232 offset:41472\n"
        "s_waitcnt lgkmcnt(6)\n"
        "v_mfma_f32_16x16x32_bf16 v[120:123], v[188:191], v[80:83], v[120:123]\n"
        "ds_read_b64_tr_b16 v[208:209], v233 offset:40960\n"
        "v_mfma_f32_16x16x32_bf16 v[124:127], v[188:191], v[84:87], v[124:127]\n"
        "ds_read_b64_tr_b16 v[210:211], v233 offset:41472\n"
        "v_mfma_f32_16x16x32_bf16 v[128:131], v[192:195], v[80:83], v[128:131]\n"
        "ds_read_b64_tr_b16 v[184:185], v234 offset:40960\n"
        "v_mfma_f32_16x16x32_bf16 v[132:135], v[192:195], v[84:87], v[132:135]\n"
        "ds_read_b64_tr_b16 v[186:187], v234 offset:41472\n"
        "s_waitcnt lgkmcnt(6)\n"
        "v_mfma_f32_16x16x32_bf16 v[136:139], v[196:199], v[80:83], v[136:139]\n"
        "ds_read_b64_tr_b16 v[188:189], v235 offset:40960\n"
        "v_mfma_f32_16x16x32_bf16 v[140:143], v[196:199], v[84:87], v[140:143]\n"
        "ds_read_b64_tr_b16 v[190:191], v235 offset:41472\n"
        "v_mfma_f32_16x16x32_bf16 v[144:147], v[200:203], v[80:83], v[144:147]\n"
        "ds_read_b64_tr_b16 v[192:193], v232 offset:45056\n"
        "v_mfma_f32_16x16x32_bf16 v[148:151], v[200:203], v[84:87], v[148:151]\n"
        "ds_read_b64_tr_b16 v[194:195], v232 offset:45568\n"
        "s_waitcnt lgkmcnt(6)\n"
        "v_mfma_f32_16x16x32_bf16 v[120:123], v[204:207], v[88:91], v[120:123]\n"
        "ds_read_b64_tr_b16 v[196:197], v233 offset:45056\n"
        "v_mfma_f32_16x16x32_bf16 v[124:127], v[204:207], v[92:95], v[124:127]\n"
        "ds_read_b64_tr_b16 v[198:199], v233 offset:45568\n"
        "v_mfma_f32_16x16x32_bf16 v[128:131], v[208:211], v[88:91], v[128:131]\n"
        "ds_read_b64_tr_b16 v[200:201], v234 offset:45056\n"
        "v_mfma_f32_16x16x32_bf16 v[132:135], v[208:211], v[92:95], v[132:135]\n"
        "ds_read_b64_tr_b16 v[202:203], v234 offset:45568\n"
        "s_waitcnt lgkmcnt(6)\n"
        "v_mfma_f32_16x16x32_bf16 v[136:139], v[184:187], v[88:91], v[136:139]\n"
        "ds_read_b64_tr_b16 v[204:205], v235 offset:45056\n"
        "v_mfma_f32_16x16x32_bf16 v[140:143], v[184:187], v[92:95], v[140:143]\n"
        "ds_read_b64_tr_b16 v[206:207], v235 offset:45568\n"
        "v_mfma_f32_16x16x32_bf16 v[144:147], v[188:191], v[88:91], v[144:147]\n"
        "v_xor_b32 v232, 0x10000, v230\n"
        "ds_read_b64_tr_b16 v[208:209], v232 offset:32768\n"
        "v_mfma_f32_16x16x32_bf16 v[148:151], v[188:191], v[92:95], v[148:151]\n"
        "ds_read_b64_tr_b16 v[210:211], v232 offset:33280\n"
        "s_waitcnt lgkmcnt(6)\n"
        "v_mfma_f32_16x16x32_bf16 v[120:123], v[192:195], v[96:99], v[120:123]\n"
        "v_xor_b32 v233, 0x10020, v230\n"
        "ds_read_b64_tr_b16 v[184:185], v233 offset:32768\n"
        "v_mfma_f32_16x16x32_bf16 v[124:127], v[192:195], v[100:103], v[124:127]\n"
        "ds_read_b64_tr_b16 v[186:187], v233 offset:33280\n"
        "v_mfma_f32_16x16x32_bf16 v[128:131], v[196:199], v[96:99], v[128:131]\n"
        "v_xor_b32 v234, 0x10040, v230\n"
        "ds_read_b64_tr_b16 v[188:189], v234 offset:32768\n"
        "v_mfma_f32_16x16x32_bf16 v[132:135], v[196:199], v[100:103], v[132:135]\n"
        "ds_read_b64_tr_b16 v[190:191], v234 offset:33280\n"
        "s_waitcnt lgkmcnt(6)\n"
        "v_mfma_f32_16x16x32_bf16 v[136:139], v[200:203], v[96:99], v[136:139]\n"
        "v_xor_b32 v235, 0x10060, v230\n"
        "ds_read_b64_tr_b16 v[192:193], v235 offset:32768\n"
        "v_mfma_f32_16x16x32_bf16 v[140:143], v[200:203], v[100:103], v[140:143]\n"
        "ds_read_b64_tr_b16 v[194:195], v235 offset:33280\n"
        "v_mfma_f32_16x16x32_bf16 v[144:147], v[204:207], v[96:99], v[144:147]\n"
        "ds_read_b64_tr_b16 v[196:197], v232 offset:36864\n"
        "v_mfma_f32_16x16x32_bf16 v[148:151], v[204:207], v[100:103], v[148:151]\n"
        "ds_read_b64_tr_b16 v[198:199], v232 offset:37376\n"
        "s_barrier\n"
        "s_waitcnt lgkmcnt(6)\n"
        "v_mfma_f32_16x16x32_bf16 v[120:123], v[208:211], v[104:107], v[120:123]\n"
        "ds_read_b64_tr_b16 v[200:201], v233 offset:36864\n"
        "v_mfma_f32_16x16x32_bf16 v[124:127], v[208:211], v[108:111], v[124:127]\n"
        "ds_read_b64_tr_b16 v[202:203], v233 offset:37376\n"
        "v_mfma_f32_16x16x32_bf16 v[128:131], v[184:187], v[104:107], v[128:131]\n"
        "ds_read_b64_tr_b16 v[204:205], v234 offset:36864\n"
        "v_mfma_f32_16x16x32_bf16 v[132:135], v[184:187], v[108:111], v[132:135]\n"
        "ds_read_b64_tr_b16 v[206:207], v234 offset:37376\n"
        "s_waitcnt lgkmcnt(6)\n"
        "v_mfma_f32_16x16x32_bf16 v[136:139], v[188:191], v[104:107], v[136:139]\n"
        "ds_read_b64_tr_b16 v[208:209], v235 offset:36864\n"
        "v_mfma_f32_16x16x32_bf16 v[140:143], v[188:191], v[108:111], v[140:143]\n"
        "ds_read_b64_tr_b16 v[210:211], v235 offset:37376\n"
        "v_mfma_f32_16x16x32_bf16 v[144:147], v[192:195], v[104:107], v[144:147]\n"
        "v_mfma_f32_16x16x32_bf16 v[148:151], v[192:195], v[108:111], v[148:151]\n"
        "s_waitcnt lgkmcnt(4)\n"
        "v_mfma_f32_16x16x32_bf16 v[120:123], v[196:199], v[112:115], v[120:123]\n"
        "v_mfma_f32_16x16x32_bf16 v[124:127], v[196:199], v[116:119], v[124:127]\n"
        "v_mfma_f32_16x16x32_bf16 v[128:131], v[200:203], v[112:115], v[128:131]\n"
        "v_mfma_f32_16x16x32_bf16 v[132:135], v[200:203], v[116:119], v[132:135]\n"
        "s_waitcnt lgkmcnt(0)\n"
        "v_mfma_f32_16x16x32_bf16 v[136:139], v[204:207], v[112:115], v[136:139]\n"
        "v_mfma_f32_16x16x32_bf16 v[140:143], v[204:207], v[116:119], v[140:143]\n"
        "v_mfma_f32_16x16x32_bf16 v[144:147], v[208:211], v[112:115], v[144:147]\n"
        "v_mfma_f32_16x16x32_bf16 v[148:151], v[208:211], v[116:119], v[148:151]\n"
        "s_barrier\n"
        "s_lshl_b32 s67, s72, 2\n"
        "s_add_u32 s67, s67, s97\n"
        "s_lshl_b32 s95, s67, 4\n"
        "s_add_u32 s95, s95, s73\n"
        "s_lshl_b32 s95, s95, 7\n"
        "s_add_u32 s95, s95, 4096\n"
        "s_mul_i32 s67, s67, 28672\n"
        "s_lshl_b32 s68, s15, 12\n"
        "s_add_u32 s67, s67, s68\n"
        "s_add_u32 s67, s67, 11534336\n"
        "v_lshl_add_u32 v238, v1, 4, s67\n"
        "s_sub_u32 s15, s15, s73\n"
        "s_lshl_b32 s15, s15, 7\n"
        "s_add_u32 s15, s15, s95\n"
        "v_cvt_pk_bf16_f32 v120, v120, v121\n"
        "v_cvt_pk_bf16_f32 v121, v122, v123\n"
        "v_cvt_pk_bf16_f32 v122, v124, v125\n"
        "v_cvt_pk_bf16_f32 v123, v126, v127\n"
        "buffer_store_dwordx4 v[120:123], v238, s[60:63], 0 offen offset:0 sc1\n"
        "v_cvt_pk_bf16_f32 v128, v128, v129\n"
        "v_cvt_pk_bf16_f32 v129, v130, v131\n"
        "v_cvt_pk_bf16_f32 v130, v132, v133\n"
        "v_cvt_pk_bf16_f32 v131, v134, v135\n"
        "buffer_store_dwordx4 v[128:131], v238, s[60:63], 0 offen offset:1024 sc1\n"
        "v_cvt_pk_bf16_f32 v136, v136, v137\n"
        "v_cvt_pk_bf16_f32 v137, v138, v139\n"
        "v_cvt_pk_bf16_f32 v138, v140, v141\n"
        "v_cvt_pk_bf16_f32 v139, v142, v143\n"
        "buffer_store_dwordx4 v[136:139], v238, s[60:63], 0 offen offset:2048 sc1\n"
        "v_cvt_pk_bf16_f32 v144, v144, v145\n"
        "v_cvt_pk_bf16_f32 v145, v146, v147\n"
        "v_cvt_pk_bf16_f32 v146, v148, v149\n"
        "v_cvt_pk_bf16_f32 v147, v150, v151\n"
        "buffer_store_dwordx4 v[144:147], v238, s[60:63], 0 offen offset:3072 sc1\n"
        "s_add_u32 m0, s65, 140288\n"
        "s_mul_i32 s66, s9, 229376\n"
        "buffer_load_dwordx4 v227, s[44:47], s66 offen lds\n"
        "v_mov_b32 v6, 0xc0135761\n"
        "s_add_u32 m0, s65, 147456\n"
        "s_mul_i32 s66, s10, 229376\n"
        "buffer_load_dwordx4 v226, s[40:43], s66 offen lds\n"
        "v_mov_b32 v7, 0xc0135761\n"
        "s_mov_b32 s88, 1.0\n"
        "s_add_u32 m0, s65, 148480\n"
        "s_mul_i32 s66, s10, 229376\n"
        "buffer_load_dwordx4 v227, s[40:43], s66 offen lds\n"
        "s_mov_b32 s89, 1.0\n"
        "v_readfirstlane_b32 s67, v244\n"
        "s_add_u32 m0, s65, 155648\n"
        "s_mul_i32 s66, s10, 229376\n"
        "buffer_load_dwordx4 v226, s[44:47], s66 offen lds\n"
        "v_readfirstlane_b32 s68, v245\n"
        "s_mul_hi_u32 s71, s67, 0x4c957f2d\n"
        "s_add_u32 m0, s65, 156672\n"
        "s_mul_i32 s66, s10, 229376\n"
        "buffer_load_dwordx4 v227, s[44:47], s66 offen lds\n"
        "s_mul_i32 s69, s67, 0x5851f42d\n"
        "s_add_u32 m0, s65, 16384\n"
        "s_mul_i32 s66, s4, 229376\n"
        "s_add_u32 s66, s66, 128\n"
        "buffer_load_dwordx4 v226, s[40:43], s66 offen lds\n"
        "s_add_u32 s71, s71, s69\n"
        "s_mul_i32 s69, s68, 0x4c957f2d\n"
        "s_add_u32 m0, s65, 17408\n"
        "s_mul_i32 s66, s4, 229376\n"
        "s_add_u32 s66, s66, 128\n"
        "buffer_load_dwordx4 v227, s[40:43], s66 offen lds\n"
        "s_add_u32 s71, s71, s69\n"
        "s_mul_i32 s70, s67, 0x4c957f2d\n"
        "s_add_u32 m0, s65, 24576\n"
        "s_mul_i32 s66, s4, 229376\n"
        "s_add_u32 s66, s66, 128\n"
        "buffer_load_dwordx4 v226, s[44:47], s66 offen lds\n"
        "s_add_u32 s70, s70, 0xf767814f\n"
        "s_addc_u32 s71, s71, 0x14057b7e\n"
        "v_mov_b32 v242, s70\n"
        "s_add_u32 m0, s65, 25600\n"
        "s_mul_i32 s66, s4, 229376\n"
        "s_add_u32 s66, s66, 128\n"
        "buffer_load_dwordx4 v227, s[44:47], s66 offen lds\n"
        "v_mov_b32 v243, s71\n"
        "s_barrier\n"
        "s_waitcnt vmcnt(9)\n"
        "s_add_u32 s66, s15, 0\n"
        "s_mov_b64 exec, 0xffff\n"
        "buffer_store_dwordx2 v[242:243], v2, s[60:63], s66 offen sc1\n"
        "s_mov_b64 exec, -1\n"
        "s_add_u32 m0, s65, 32768\n"
        "s_mul_i32 s66, s5, 229376\n"
        "s_add_u32 s66, s66, 128\n"
        "buffer_load_dwordx4 v226, s[40:43], s66 offen lds\n"
        "s_mov_b32 s80, 0xbdd2d3e8\n"
        "s_mov_b32 s86, s80\n"
        "s_mov_b32 s87, s80\n"
        "s_and_b32 s68, s73, 1\n"
        "s_add_u32 m0, s65, 33792\n"
        "s_mul_i32 s66, s5, 229376\n"
        "s_add_u32 s66, s66, 128\n"
        "buffer_load_dwordx4 v227, s[40:43], s66 offen lds\n"
        "s_mul_i32 s68, s68, 36\n"
        "s_lshr_b32 s67, s73, 1\n"
        "s_add_u32 s67, s67, s68\n"
        "s_lshl_b32 s82, s67, 12\n"
        "s_add_u32 s83, s82, 73728\n"
        "s_add_u32 m0, s65, 40960\n"
        "s_mul_i32 s66, s5, 229376\n"
        "s_add_u32 s66, s66, 128\n"
        "buffer_load_dwordx4 v226, s[44:47], s66 offen lds\n"
        "s_lshr_b32 s67, s97, 1\n"
        "s_lshl1_add_u32 s67, s72, s67\n"
        "s_mul_i32 s67, s67, 294912\n"
        "s_and_b32 s68, s97, 1\n"
        "s_add_u32 m0, s65, 41984\n"
        "s_mul_i32 s66, s5, 229376\n"
        "s_add_u32 s66, s66, 128\n"
        "buffer_load_dwordx4 v227, s[44:47], s66 offen lds\n"
        "s_lshl_b32 s68, s68, 11\n"
        "s_add_u32 s67, s67, s68\n"
        "s_add_u32 s67, s67, 1077248\n"
        "v_lshl_add_u32 v236, v1, 4, s67\n"
        "s_lshl_b32 s69, s75, 7\n"
        "s_add_u32 m0, s65, 98304\n"
        "s_mul_i32 s66, s6, 229376\n"
        "s_add_u32 s66, s66, 128\n"
        "buffer_load_dwordx4 v226, s[40:43], s66 offen lds\n"
        "s_add_u32 s48, s26, s69\n"
        "s_addc_u32 s49, s27, 0\n"
        "s_and_b32 s49, s49, 0xffff\n"
        "s_sub_u32 s50, 1605632, s69\n"
        "s_mov_b32 s51, 0x20000\n"
        "s_add_u32 m0, s65, 99328\n"
        "s_mul_i32 s66, s6, 229376\n"
        "s_add_u32 s66, s66, 128\n"
        "buffer_load_dwordx4 v227, s[40:43], s66 offen lds\n"
        "s_mov_b32 s56, s28\n"
        "s_and_b32 s57, s29, 0xffff\n"
        "s_mov_b32 s58, 1835008\n"
        "s_mov_b32 s59, 0x20000\n"
        "v_lshrrev_b32 v217, 5, v1\n"
        "s_add_u32 m0, s65, 106496\n"
        "s_mul_i32 s66, s6, 229376\n"
        "s_add_u32 s66, s66, 128\n"
        "buffer_load_dwordx4 v226, s[44:47], s66 offen lds\n"
        "v_bfe_u32 v216, v1, 3, 2\n"
        "v_lshl_add_u32 v216, v217, 4, v216\n"
        "s_mov_b32 s69, 896\n"
        "v_mad_u32_u24 v228, v216, s69, v222\n"
        "s_add_u32 m0, s65, 107520\n"
        "s_mul_i32 s66, s6, 229376\n"
        "s_add_u32 s66, s66, 128\n"
        "buffer_load_dwordx4 v227, s[44:47], s66 offen lds\n"
        "v_xor_b32 v229, 64, v228\n"
        "s_lshl_b32 s84, s97, 12\n"
        "s_add_u32 s68, s75, s74\n"
        "s_and_b32 s68, s68, 0\n"
        "s_lshl_b32 s67, s68, 4\n"
        "s_add_u32 m0, s65, 49152\n"
        "s_mul_i32 s66, s7, 229376\n"
        "s_add_u32 s66, s66, 128\n"
        "buffer_load_dwordx4 v226, s[40:43], s66 offen lds\n"
        "s_lshl_b32 s68, s68, 1\n"
        "s_mov_b32 s28, 0x06543210\n"
        "s_mov_b32 s29, 0x00654321\n"
        "s_lshr_b64 s[28:29], s[28:29], s67\n"
        "s_add_u32 m0, s65, 50176\n"
        "s_mul_i32 s66, s7, 229376\n"
        "s_add_u32 s66, s66, 128\n"
        "buffer_load_dwordx4 v227, s[40:43], s66 offen lds\n"
        "s_lshr_b32 s67, s97, 1\n"
        "s_mul_i32 s67, s67, 57344\n"
        "s_mul_i32 s68, s97, 28672\n"
        "s_add_u32 s67, s67, s68\n"
        "s_bfe_u32 s20, s28, 0x40010\n"
        "s_add_u32 m0, s65, 57344\n"
        "s_mul_i32 s66, s7, 229376\n"
        "s_add_u32 s66, s66, 128\n"
        "buffer_load_dwordx4 v226, s[44:47], s66 offen lds\n"
        "s_mul_i32 s20, s20, 229376\n"
        "s_add_u32 s20, s20, s67\n"
        "s_bfe_u32 s21, s28, 0x40014\n"
        "s_mul_i32 s21, s21, 229376\n"
        "s_add_u32 s21, s21, s67\n"
        "buffer_load_dwordx4 v[246:249], v239, s[32:35], 0 offen\n"
        "s_waitcnt vmcnt(17)\n"
        "s_barrier\n"
        "s_add_u32 m0, s65, 58368\n"
        "s_mul_i32 s66, s7, 229376\n"
        "s_add_u32 s66, s66, 128\n"
        "buffer_load_dwordx4 v227, s[44:47], s66 offen lds\n"
        "s_bfe_u32 s22, s28, 0x40018\n"
        "s_mul_i32 s22, s22, 229376\n"
        "s_add_u32 s22, s22, s67\n"
        "s_add_u32 m0, s65, 65536\n"
        "s_mul_i32 s66, s8, 229376\n"
        "s_add_u32 s66, s66, 128\n"
        "buffer_load_dwordx4 v226, s[40:43], s66 offen lds\n"
        "s_add_u32 m0, s65, 66560\n"
        "s_mul_i32 s66, s8, 229376\n"
        "s_add_u32 s66, s66, 128\n"
        "buffer_load_dwordx4 v227, s[40:43], s66 offen lds\n"
        "s_add_u32 m0, s65, 73728\n"
        "s_mul_i32 s66, s8, 229376\n"
        "s_add_u32 s66, s66, 128\n"
        "buffer_load_dwordx4 v226, s[44:47], s66 offen lds\n"
        "s_add_u32 m0, s65, 74752\n"
        "s_mul_i32 s66, s8, 229376\n"
        "s_add_u32 s66, s66, 128\n"
        "buffer_load_dwordx4 v227, s[44:47], s66 offen lds\n"
        "s_add_u32 m0, s65, 81920\n"
        "s_mul_i32 s66, s9, 229376\n"
        "s_add_u32 s66, s66, 128\n"
        "buffer_load_dwordx4 v226, s[40:43], s66 offen lds\n"
        "s_add_u32 m0, s65, 82944\n"
        "s_mul_i32 s66, s9, 229376\n"
        "s_add_u32 s66, s66, 128\n"
        "buffer_load_dwordx4 v227, s[40:43], s66 offen lds\n"
        "s_add_u32 m0, s65, 90112\n"
        "s_mul_i32 s66, s9, 229376\n"
        "s_add_u32 s66, s66, 128\n"
        "buffer_load_dwordx4 v226, s[44:47], s66 offen lds\n"
        "s_add_u32 m0, s65, 91136\n"
        "s_mul_i32 s66, s9, 229376\n"
        "s_add_u32 s66, s66, 128\n"
        "buffer_load_dwordx4 v227, s[44:47], s66 offen lds\n"
        "s_add_u32 m0, s65, 0\n"
        "s_mul_i32 s66, s10, 229376\n"
        "s_add_u32 s66, s66, 128\n"
        "buffer_load_dwordx4 v226, s[40:43], s66 offen lds\n"
        "s_add_u32 m0, s65, 1024\n"
        "s_mul_i32 s66, s10, 229376\n"
        "s_add_u32 s66, s66, 128\n"
        "buffer_load_dwordx4 v227, s[40:43], s66 offen lds\n"
        "s_add_u32 m0, s65, 8192\n"
        "s_mul_i32 s66, s10, 229376\n"
        "s_add_u32 s66, s66, 128\n"
        "buffer_load_dwordx4 v226, s[44:47], s66 offen lds\n"
        "s_waitcnt vmcnt(25)\n"
        "s_barrier\n"
        "s_add_u32 m0, s65, 9216\n"
        "s_mul_i32 s66, s10, 229376\n"
        "s_add_u32 s66, s66, 128\n"
        "buffer_load_dwordx4 v227, s[44:47], s66 offen lds\n"
        "s_add_u32 m0, s84, 114688\n"
        "s_add_u32 s66, s20, 0\n"
        "buffer_load_dwordx4 v228, s[48:51], s66 offen lds\n"
        "s_add_u32 m0, s84, 115712\n"
        "s_add_u32 s66, s20, 3584\n"
        "buffer_load_dwordx4 v229, s[48:51], s66 offen lds\n"
        "s_add_u32 m0, s84, 116736\n"
        "s_add_u32 s66, s20, 7168\n"
        "buffer_load_dwordx4 v228, s[48:51], s66 offen lds\n"
        "s_add_u32 m0, s84, 117760\n"
        "s_add_u32 s66, s20, 10752\n"
        "buffer_load_dwordx4 v229, s[48:51], s66 offen lds\n"
        "s_add_u32 m0, s84, 131072\n"
        "s_add_u32 s66, s21, 0\n"
        "buffer_load_dwordx4 v228, s[48:51], s66 offen lds\n"
        "s_barrier\n"
        "s_add_u32 m0, s84, 132096\n"
        "s_add_u32 s66, s21, 3584\n"
        "buffer_load_dwordx4 v229, s[48:51], s66 offen lds\n"
        "s_add_u32 m0, s84, 133120\n"
        "s_add_u32 s66, s21, 7168\n"
        "buffer_load_dwordx4 v228, s[48:51], s66 offen lds\n"
        "s_add_u32 m0, s84, 134144\n"
        "s_add_u32 s66, s21, 10752\n"
        "buffer_load_dwordx4 v229, s[48:51], s66 offen lds\n"
        "s_add_u32 m0, s84, 147456\n"
        "s_add_u32 s66, s22, 0\n"
        "buffer_load_dwordx4 v228, s[48:51], s66 offen lds\n"
        "s_add_u32 m0, s84, 148480\n"
        "s_add_u32 s66, s22, 3584\n"
        "buffer_load_dwordx4 v229, s[48:51], s66 offen lds\n"
        "s_add_u32 m0, s84, 149504\n"
        "s_add_u32 s66, s22, 7168\n"
        "buffer_load_dwordx4 v228, s[48:51], s66 offen lds\n"
        "s_mov_b32 s66, 0x8029731\n"
        "s_lshl_b32 s79, s97, 3\n"
        "s_add_u32 s79, s79, 0x40004\n"
        "s_bfe_u32 s66, s66, s79\n"
        "s_cbranch_scc0 Lvb4\n"
        "s_lshl_b32 s66, s66, 12\n"
        "s_mov_b64 exec, 0\n"
        "s_getpc_b64 s[90:91]\n"
        "Lvp4:\n"
        "s_sub_u32 s66, s66, Lvp4-Lp0\n"
        "s_ashr_i32 s79, s66, 31\n"
        "s_add_u32 s90, s90, s66\n"
        "s_addc_u32 s91, s91, s79\n"
        "s_swappc_b64 vcc, s[90:91]\n"
        "s_mov_b64 exec, -1\n"
        "Lvb4:\n"
        "v_xor_b32 v232, 0x0, v230\n"
        "ds_read_b64_tr_b16 v[184:185], v232 offset:16384\n"
        "ds_read_b64_tr_b16 v[186:187], v232 offset:16896\n"
        "v_xor_b32 v233, 0x20, v230\n"
        "ds_read_b64_tr_b16 v[188:189], v233 offset:16384\n"
        "ds_read_b64_tr_b16 v[190:191], v233 offset:16896\n"
        "v_xor_b32 v234, 0x40, v230\n"
        "ds_read_b64_tr_b16 v[192:193], v234 offset:16384\n"
        "s_nop 0\n"
        "s_cbranch_execz Lback\n"
        "ds_read_b64_tr_b16 v[194:195], v234 offset:16896\n"
        "v_xor_b32 v235, 0x60, v230\n"
        "ds_read_b64_tr_b16 v[196:197], v235 offset:16384\n"
        "ds_read_b64_tr_b16 v[198:199], v235 offset:16896\n"
        "ds_read_b64_tr_b16 v[200:201], v232 offset:24576\n"
        "ds_read_b64_tr_b16 v[202:203], v232 offset:25088\n"
        "s_waitcnt vmcnt(3)\n"
        "s_barrier\n"
        "s_waitcnt lgkmcnt(6)\n"
        "v_mfma_f32_16x16x32_bf16 v[120:123], v[184:187], v[8:11], 0\n"
        "ds_read_b64_tr_b16 v[204:205], v233 offset:24576\n"
        "v_mfma_f32_16x16x32_bf16 v[128:131], v[184:187], v[12:15], 0\n"
        "ds_read_b64_tr_b16 v[206:207], v233 offset:25088\n"
        "v_mfma_f32_16x16x32_bf16 v[124:127], v[188:191], v[8:11], 0\n"
        "ds_read_b64_tr_b16 v[208:209], v234 offset:24576\n"
        "v_mfma_f32_16x16x32_bf16 v[132:135], v[188:191], v[12:15], 0\n"
        "ds_read_b64_tr_b16 v[210:211], v234 offset:25088\n"
        "s_waitcnt lgkmcnt(6)\n"
        "v_mfma_f32_16x16x32_bf16 v[136:139], v[192:195], v[8:11], 0\n"
        "ds_read_b64_tr_b16 v[184:185], v235 offset:24576\n"
        "v_mfma_f32_16x16x32_bf16 v[144:147], v[192:195], v[12:15], 0\n"
        "ds_read_b64_tr_b16 v[186:187], v235 offset:25088\n"
        "v_mfma_f32_16x16x32_bf16 v[140:143], v[196:199], v[8:11], 0\n"
        "ds_read_b64_tr_b16 v[188:189], v232 offset:20480\n"
        "v_mfma_f32_16x16x32_bf16 v[148:151], v[196:199], v[12:15], 0\n"
        "ds_read_b64_tr_b16 v[190:191], v232 offset:20992\n"
        "s_waitcnt lgkmcnt(6)\n"
        "v_mfma_f32_16x16x32_bf16 v[152:155], v[200:203], v[8:11], 0\n"
        "ds_read_b64_tr_b16 v[192:193], v233 offset:20480\n"
        "v_mfma_f32_16x16x32_bf16 v[160:163], v[200:203], v[12:15], 0\n"
        "ds_read_b64_tr_b16 v[194:195], v233 offset:20992\n"
        "v_mfma_f32_16x16x32_bf16 v[156:159], v[204:207], v[8:11], 0\n"
        "ds_read_b64_tr_b16 v[196:197], v234 offset:20480\n"
        "v_mfma_f32_16x16x32_bf16 v[164:167], v[204:207], v[12:15], 0\n"
        "ds_read_b64_tr_b16 v[198:199], v234 offset:20992\n"
        "s_waitcnt lgkmcnt(6)\n"
        "v_mfma_f32_16x16x32_bf16 v[168:171], v[208:211], v[8:11], 0\n"
        "ds_read_b64_tr_b16 v[200:201], v235 offset:20480\n"
        "v_mfma_f32_16x16x32_bf16 v[176:179], v[208:211], v[12:15], 0\n"
        "ds_read_b64_tr_b16 v[202:203], v235 offset:20992\n"
        "v_mfma_f32_16x16x32_bf16 v[172:175], v[184:187], v[8:11], 0\n"
        "ds_read_b64_tr_b16 v[204:205], v232 offset:28672\n"
        "v_mfma_f32_16x16x32_bf16 v[180:183], v[184:187], v[12:15], 0\n"
        "ds_read_b64_tr_b16 v[206:207], v232 offset:29184\n"
        "s_waitcnt lgkmcnt(6)\n"
        "v_mfma_f32_16x16x32_bf16 v[120:123], v[188:191], v[16:19], v[120:123]\n"
        "ds_read_b64_tr_b16 v[208:209], v233 offset:28672\n"
        "v_mfma_f32_16x16x32_bf16 v[128:131], v[188:191], v[20:23], v[128:131]\n"
        "ds_read_b64_tr_b16 v[210:211], v233 offset:29184\n"
        "v_mfma_f32_16x16x32_bf16 v[124:127], v[192:195], v[16:19], v[124:127]\n"
        "ds_read_b64_tr_b16 v[184:185], v234 offset:28672\n"
        "v_mfma_f32_16x16x32_bf16 v[132:135], v[192:195], v[20:23], v[132:135]\n"
        "ds_read_b64_tr_b16 v[186:187], v234 offset:29184\n"
        "s_waitcnt lgkmcnt(6)\n"
        "v_mfma_f32_16x16x32_bf16 v[136:139], v[196:199], v[16:19], v[136:139]\n"
        "ds_read_b64_tr_b16 v[188:189], v235 offset:28672\n"
        "v_mfma_f32_16x16x32_bf16 v[144:147], v[196:199], v[20:23], v[144:147]\n"
        "ds_read_b64_tr_b16 v[190:191], v235 offset:29184\n"
        "v_mfma_f32_16x16x32_bf16 v[140:143], v[200:203], v[16:19], v[140:143]\n"
        "ds_read_b64_tr_b16 v[192:193], v232 offset:32768\n"
        "v_mfma_f32_16x16x32_bf16 v[148:151], v[200:203], v[20:23], v[148:151]\n"
        "ds_read_b64_tr_b16 v[194:195], v232 offset:33280\n"
        "s_waitcnt lgkmcnt(6)\n"
        "v_mfma_f32_16x16x32_bf16 v[152:155], v[204:207], v[16:19], v[152:155]\n"
        "ds_read_b64_tr_b16 v[196:197], v233 offset:32768\n"
        "v_mfma_f32_16x16x32_bf16 v[160:163], v[204:207], v[20:23], v[160:163]\n"
        "ds_read_b64_tr_b16 v[198:199], v233 offset:33280\n"
        "v_mfma_f32_16x16x32_bf16 v[156:159], v[208:211], v[16:19], v[156:159]\n"
        "ds_read_b64_tr_b16 v[200:201], v234 offset:32768\n"
        "v_mfma_f32_16x16x32_bf16 v[164:167], v[208:211], v[20:23], v[164:167]\n"
        "ds_read_b64_tr_b16 v[202:203], v234 offset:33280\n"
        "s_waitcnt lgkmcnt(6)\n"
        "v_mfma_f32_16x16x32_bf16 v[168:171], v[184:187], v[16:19], v[168:171]\n"
        "ds_read_b64_tr_b16 v[204:205], v235 offset:32768\n"
        "v_mfma_f32_16x16x32_bf16 v[176:179], v[184:187], v[20:23], v[176:179]\n"
        "ds_read_b64_tr_b16 v[206:207], v235 offset:33280\n"
        "v_mfma_f32_16x16x32_bf16 v[172:175], v[188:191], v[16:19], v[172:175]\n"
        "ds_read_b64_tr_b16 v[208:209], v232 offset:40960\n"
        "v_mfma_f32_16x16x32_bf16 v[180:183], v[188:191], v[20:23], v[180:183]\n"
        "ds_read_b64_tr_b16 v[210:211], v232 offset:41472\n"
        "s_waitcnt lgkmcnt(6)\n"
        "v_mfma_f32_16x16x32_bf16 v[120:123], v[192:195], v[24:27], v[120:123]\n"
        "ds_read_b64_tr_b16 v[184:185], v233 offset:40960\n"
        "v_mfma_f32_16x16x32_bf16 v[128:131], v[192:195], v[28:31], v[128:131]\n"
        "ds_read_b64_tr_b16 v[186:187], v233 offset:41472\n"
        "v_mfma_f32_16x16x32_bf16 v[124:127], v[196:199], v[24:27], v[124:127]\n"
        "ds_read_b64_tr_b16 v[188:189], v234 offset:40960\n"
        "v_mfma_f32_16x16x32_bf16 v[132:135], v[196:199], v[28:31], v[132:135]\n"
        "ds_read_b64_tr_b16 v[190:191], v234 offset:41472\n"
        "s_waitcnt lgkmcnt(6)\n"
        "v_mfma_f32_16x16x32_bf16 v[136:139], v[200:203], v[24:27], v[136:139]\n"
        "ds_read_b64_tr_b16 v[192:193], v235 offset:40960\n"
        "v_mfma_f32_16x16x32_bf16 v[144:147], v[200:203], v[28:31], v[144:147]\n"
        "ds_read_b64_tr_b16 v[194:195], v235 offset:41472\n"
        "v_mfma_f32_16x16x32_bf16 v[140:143], v[204:207], v[24:27], v[140:143]\n"
        "ds_read_b64_tr_b16 v[196:197], v232 offset:36864\n"
        "v_mfma_f32_16x16x32_bf16 v[148:151], v[204:207], v[28:31], v[148:151]\n"
        "ds_read_b64_tr_b16 v[198:199], v232 offset:37376\n"
        "s_waitcnt lgkmcnt(6)\n"
        "v_mfma_f32_16x16x32_bf16 v[152:155], v[208:211], v[24:27], v[152:155]\n"
        "ds_read_b64_tr_b16 v[200:201], v233 offset:36864\n"
        "v_mfma_f32_16x16x32_bf16 v[160:163], v[208:211], v[28:31], v[160:163]\n"
        "ds_read_b64_tr_b16 v[202:203], v233 offset:37376\n"
        "v_mfma_f32_16x16x32_bf16 v[156:159], v[184:187], v[24:27], v[156:159]\n"
        "ds_read_b64_tr_b16 v[204:205], v234 offset:36864\n"
        "v_mfma_f32_16x16x32_bf16 v[164:167], v[184:187], v[28:31], v[164:167]\n"
        "ds_read_b64_tr_b16 v[206:207], v234 offset:37376\n"
        "s_waitcnt lgkmcnt(6)\n"
        "v_mfma_f32_16x16x32_bf16 v[168:171], v[188:191], v[24:27], v[168:171]\n"
        "ds_read_b64_tr_b16 v[208:209], v235 offset:36864\n"
        "v_mfma_f32_16x16x32_bf16 v[176:179], v[188:191], v[28:31], v[176:179]\n"
        "ds_read_b64_tr_b16 v[210:211], v235 offset:37376\n"
        "v_mfma_f32_16x16x32_bf16 v[172:175], v[192:195], v[24:27], v[172:175]\n"
        "ds_read_b64_tr_b16 v[184:185], v232 offset:45056\n"
        "v_mfma_f32_16x16x32_bf16 v[180:183], v[192:195], v[28:31], v[180:183]\n"
        "ds_read_b64_tr_b16 v[186:187], v232 offset:45568\n"
        "s_waitcnt lgkmcnt(6)\n"
        "v_mfma_f32_16x16x32_bf16 v[120:123], v[196:199], v[32:35], v[120:123]\n"
        "ds_read_b64_tr_b16 v[188:189], v233 offset:45056\n"
        "v_mfma_f32_16x16x32_bf16 v[128:131], v[196:199], v[36:39], v[128:131]\n"
        "ds_read_b64_tr_b16 v[190:191], v233 offset:45568\n"
        "v_mfma_f32_16x16x32_bf16 v[124:127], v[200:203], v[32:35], v[124:127]\n"
        "ds_read_b64_tr_b16 v[192:193], v234 offset:45056\n"
        "v_mfma_f32_16x16x32_bf16 v[132:135], v[200:203], v[36:39], v[132:135]\n"
        "ds_read_b64_tr_b16 v[194:195], v234 offset:45568\n"
        "s_waitcnt lgkmcnt(6)\n"
        "v_mfma_f32_16x16x32_bf16 v[136:139], v[204:207], v[32:35], v[136:139]\n"
        "ds_read_b64_tr_b16 v[196:197], v235 offset:45056\n"
        "v_mfma_f32_16x16x32_bf16 v[144:147], v[204:207], v[36:39], v[144:147]\n"
        "ds_read_b64_tr_b16 v[198:199], v235 offset:45568\n"
        "v_mfma_f32_16x16x32_bf16 v[140:143], v[208:211], v[32:35], v[140:143]\n"
        "v_xor_b32 v232, 0x10000, v230\n"
        "ds_read_b64_tr_b16 v[200:201], v232 offset:32768\n"
        "v_mfma_f32_16x16x32_bf16 v[148:151], v[208:211], v[36:39], v[148:151]\n"
        "ds_read_b64_tr_b16 v[202:203], v232 offset:33280\n"
        "s_waitcnt lgkmcnt(6)\n"
        "v_mfma_f32_16x16x32_bf16 v[152:155], v[184:187], v[32:35], v[152:155]\n"
        "v_xor_b32 v233, 0x10020, v230\n"
        "ds_read_b64_tr_b16 v[204:205], v233 offset:32768\n"
        "v_mfma_f32_16x16x32_bf16 v[160:163], v[184:187], v[36:39], v[160:163]\n"
        "ds_read_b64_tr_b16 v[206:207], v233 offset:33280\n"
        "v_mfma_f32_16x16x32_bf16 v[156:159], v[188:191], v[32:35], v[156:159]\n"
        "v_xor_b32 v234, 0x10040, v230\n"
        "ds_read_b64_tr_b16 v[208:209], v234 offset:32768\n"
        "v_mfma_f32_16x16x32_bf16 v[164:167], v[188:191], v[36:39], v[164:167]\n"
        "ds_read_b64_tr_b16 v[210:211], v234 offset:33280\n"
        "s_waitcnt lgkmcnt(6)\n"
        "v_mfma_f32_16x16x32_bf16 v[168:171], v[192:195], v[32:35], v[168:171]\n"
        "v_xor_b32 v235, 0x10060, v230\n"
        "ds_read_b64_tr_b16 v[184:185], v235 offset:32768\n"
        "v_mfma_f32_16x16x32_bf16 v[176:179], v[192:195], v[36:39], v[176:179]\n"
        "ds_read_b64_tr_b16 v[186:187], v235 offset:33280\n"
        "v_mfma_f32_16x16x32_bf16 v[172:175], v[196:199], v[32:35], v[172:175]\n"
        "ds_read_b64_tr_b16 v[188:189], v232 offset:40960\n"
        "v_mfma_f32_16x16x32_bf16 v[180:183], v[196:199], v[36:39], v[180:183]\n"
        "ds_read_b64_tr_b16 v[190:191], v232 offset:41472\n"
        "s_waitcnt vmcnt(0)\n"
        "s_barrier\n"
        "s_waitcnt lgkmcnt(6)\n"
        "v_mfma_f32_16x16x32_bf16 v[120:123], v[200:203], v[40:43], v[120:123]\n"
        "ds_read_b64_tr_b16 v[192:193], v233 offset:40960\n"
        "v_mfma_f32_16x16x32_bf16 v[128:131], v[200:203], v[44:47], v[128:131]\n"
        "ds_read_b64_tr_b16 v[194:195], v233 offset:41472\n"
        "v_mfma_f32_16x16x32_bf16 v[124:127], v[204:207], v[40:43], v[124:127]\n"
        "ds_read_b64_tr_b16 v[196:197], v234 offset:40960\n"
        "v_mfma_f32_16x16x32_bf16 v[132:135], v[204:207], v[44:47], v[132:135]\n"
        "ds_read_b64_tr_b16 v[198:199], v234 offset:41472\n"
        "s_waitcnt lgkmcnt(6)\n"
        "v_mfma_f32_16x16x32_bf16 v[136:139], v[208:211], v[40:43], v[136:139]\n"
        "ds_read_b64_tr_b16 v[200:201], v235 offset:40960\n"
        "v_mfma_f32_16x16x32_bf16 v[144:147], v[208:211], v[44:47], v[144:147]\n"
        "ds_read_b64_tr_b16 v[202:203], v235 offset:41472\n"
        "v_mfma_f32_16x16x32_bf16 v[140:143], v[184:187], v[40:43], v[140:143]\n"
        "ds_read_b64_tr_b16 v[204:205], v232 offset:36864\n"
        "v_mfma_f32_16x16x32_bf16 v[148:151], v[184:187], v[44:47], v[148:151]\n"
        "ds_read_b64_tr_b16 v[206:207], v232 offset:37376\n"
        "s_waitcnt lgkmcnt(6)\n"
        "v_mfma_f32_16x16x32_bf16 v[152:155], v[188:191], v[40:43], v[152:155]\n"
        "ds_read_b64_tr_b16 v[208:209], v233 offset:36864\n"
        "v_mfma_f32_16x16x32_bf16 v[160:163], v[188:191], v[44:47], v[160:163]\n"
        "ds_read_b64_tr_b16 v[210:211], v233 offset:37376\n"
        "v_mfma_f32_16x16x32_bf16 v[156:159], v[192:195], v[40:43], v[156:159]\n"
        "ds_read_b64_tr_b16 v[184:185], v234 offset:36864\n"
        "v_mfma_f32_16x16x32_bf16 v[164:167], v[192:195], v[44:47], v[164:167]\n"
        "ds_read_b64_tr_b16 v[186:187], v234 offset:37376\n"
        "s_waitcnt lgkmcnt(6)\n"
        "v_mfma_f32_16x16x32_bf16 v[168:171], v[196:199], v[40:43], v[168:171]\n"
        "ds_read_b64_tr_b16 v[188:189], v235 offset:36864\n"
        "v_mfma_f32_16x16x32_bf16 v[176:179], v[196:199], v[44:47], v[176:179]\n"
        "ds_read_b64_tr_b16 v[190:191], v235 offset:37376\n"
        "v_mfma_f32_16x16x32_bf16 v[172:175], v[200:203], v[40:43], v[172:175]\n"
        "ds_read_b64_tr_b16 v[192:193], v232 offset:45056\n"
        "v_mfma_f32_16x16x32_bf16 v[180:183], v[200:203], v[44:47], v[180:183]\n"
        "ds_read_b64_tr_b16 v[194:195], v232 offset:45568\n"
        "s_waitcnt lgkmcnt(6)\n"
        "v_mfma_f32_16x16x32_bf16 v[120:123], v[204:207], v[48:51], v[120:123]\n"
        "ds_read_b64_tr_b16 v[196:197], v233 offset:45056\n"
        "v_mfma_f32_16x16x32_bf16 v[128:131], v[204:207], v[52:55], v[128:131]\n"
        "ds_read_b64_tr_b16 v[198:199], v233 offset:45568\n"
        "v_mfma_f32_16x16x32_bf16 v[124:127], v[208:211], v[48:51], v[124:127]\n"
        "ds_read_b64_tr_b16 v[200:201], v234 offset:45056\n"
        "v_mfma_f32_16x16x32_bf16 v[132:135], v[208:211], v[52:55], v[132:135]\n"
        "ds_read_b64_tr_b16 v[202:203], v234 offset:45568\n"
        "s_waitcnt lgkmcnt(6)\n"
        "v_mfma_f32_16x16x32_bf16 v[136:139], v[184:187], v[48:51], v[136:139]\n"
        "ds_read_b64_tr_b16 v[204:205], v235 offset:45056\n"
        "v_mfma_f32_16x16x32_bf16 v[144:147], v[184:187], v[52:55], v[144:147]\n"
        "ds_read_b64_tr_b16 v[206:207], v235 offset:45568\n"
        "v_mfma_f32_16x16x32_bf16 v[140:143], v[188:191], v[48:51], v[140:143]\n"
        "v_xor_b32 v232, 0x0, v230\n"
        "ds_read_b64_tr_b16 v[208:209], v232 offset:49152\n"
        "v_mfma_f32_16x16x32_bf16 v[148:151], v[188:191], v[52:55], v[148:151]\n"
        "ds_read_b64_tr_b16 v[210:211], v232 offset:49664\n"
        "s_waitcnt lgkmcnt(6)\n"
        "v_mfma_f32_16x16x32_bf16 v[152:155], v[192:195], v[48:51], v[152:155]\n"
        "v_xor_b32 v233, 0x20, v230\n"
        "ds_read_b64_tr_b16 v[184:185], v233 offset:49152\n"
        "v_mfma_f32_16x16x32_bf16 v[160:163], v[192:195], v[52:55], v[160:163]\n"
        "ds_read_b64_tr_b16 v[186:187], v233 offset:49664\n"
        "v_mfma_f32_16x16x32_bf16 v[156:159], v[196:199], v[48:51], v[156:159]\n"
        "v_xor_b32 v234, 0x40, v230\n"
        "ds_read_b64_tr_b16 v[188:189], v234 offset:49152\n"
        "v_mfma_f32_16x16x32_bf16 v[164:167], v[196:199], v[52:55], v[164:167]\n"
        "ds_read_b64_tr_b16 v[190:191], v234 offset:49664\n"
        "s_waitcnt lgkmcnt(6)\n"
        "v_mfma_f32_16x16x32_bf16 v[168:171], v[200:203], v[48:51], v[168:171]\n"
        "v_xor_b32 v235, 0x60, v230\n"
        "ds_read_b64_tr_b16 v[192:193], v235 offset:49152\n"
        "v_mfma_f32_16x16x32_bf16 v[176:179], v[200:203], v[52:55], v[176:179]\n"
        "ds_read_b64_tr_b16 v[194:195], v235 offset:49664\n"
        "v_mfma_f32_16x16x32_bf16 v[172:175], v[204:207], v[48:51], v[172:175]\n"
        "ds_read_b64_tr_b16 v[196:197], v232 offset:57344\n"
        "v_mfma_f32_16x16x32_bf16 v[180:183], v[204:207], v[52:55], v[180:183]\n"
        "ds_read_b64_tr_b16 v[198:199], v232 offset:57856\n"
        "s_barrier\n"
        "s_waitcnt lgkmcnt(6)\n"
        "v_mfma_f32_16x16x32_bf16 v[120:123], v[208:211], v[56:59], v[120:123]\n"
        "ds_read_b64_tr_b16 v[200:201], v233 offset:57344\n"
        "v_mfma_f32_16x16x32_bf16 v[128:131], v[208:211], v[60:63], v[128:131]\n"
        "ds_read_b64_tr_b16 v[202:203], v233 offset:57856\n"
        "v_mfma_f32_16x16x32_bf16 v[124:127], v[184:187], v[56:59], v[124:127]\n"
        "ds_read_b64_tr_b16 v[204:205], v234 offset:57344\n"
        "v_mfma_f32_16x16x32_bf16 v[132:135], v[184:187], v[60:63], v[132:135]\n"
        "ds_read_b64_tr_b16 v[206:207], v234 offset:57856\n"
        "s_waitcnt lgkmcnt(6)\n"
        "v_mfma_f32_16x16x32_bf16 v[136:139], v[188:191], v[56:59], v[136:139]\n"
        "ds_read_b64_tr_b16 v[208:209], v235 offset:57344\n"
        "v_mfma_f32_16x16x32_bf16 v[144:147], v[188:191], v[60:63], v[144:147]\n"
        "ds_read_b64_tr_b16 v[210:211], v235 offset:57856\n"
        "v_mfma_f32_16x16x32_bf16 v[140:143], v[192:195], v[56:59], v[140:143]\n"
        "ds_read_b64_tr_b16 v[184:185], v232 offset:53248\n"
        "v_mfma_f32_16x16x32_bf16 v[148:151], v[192:195], v[60:63], v[148:151]\n"
        "ds_read_b64_tr_b16 v[186:187], v232 offset:53760\n"
        "s_waitcnt lgkmcnt(6)\n"
        "v_mfma_f32_16x16x32_bf16 v[152:155], v[196:199], v[56:59], v[152:155]\n"
        "ds_read_b64_tr_b16 v[188:189], v233 offset:53248\n"
        "v_mfma_f32_16x16x32_bf16 v[160:163], v[196:199], v[60:63], v[160:163]\n"
        "ds_read_b64_tr_b16 v[190:191], v233 offset:53760\n"
        "v_mfma_f32_16x16x32_bf16 v[156:159], v[200:203], v[56:59], v[156:159]\n"
        "ds_read_b64_tr_b16 v[192:193], v234 offset:53248\n"
        "v_mfma_f32_16x16x32_bf16 v[164:167], v[200:203], v[60:63], v[164:167]\n"
        "ds_read_b64_tr_b16 v[194:195], v234 offset:53760\n"
        "s_waitcnt lgkmcnt(6)\n"
        "v_mfma_f32_16x16x32_bf16 v[168:171], v[204:207], v[56:59], v[168:171]\n"
        "ds_read_b64_tr_b16 v[196:197], v235 offset:53248\n"
        "v_mfma_f32_16x16x32_bf16 v[176:179], v[204:207], v[60:63], v[176:179]\n"
        "ds_read_b64_tr_b16 v[198:199], v235 offset:53760\n"
        "v_mfma_f32_16x16x32_bf16 v[172:175], v[208:211], v[56:59], v[172:175]\n"
        "ds_read_b64_tr_b16 v[200:201], v232 offset:61440\n"
        "v_mfma_f32_16x16x32_bf16 v[180:183], v[208:211], v[60:63], v[180:183]\n"
        "ds_read_b64_tr_b16 v[202:203], v232 offset:61952\n"
        "s_waitcnt lgkmcnt(6)\n"
        "v_mfma_f32_16x16x32_bf16 v[120:123], v[184:187], v[64:67], v[120:123]\n"
        "ds_read_b64_tr_b16 v[204:205], v233 offset:61440\n"
        "v_mfma_f32_16x16x32_bf16 v[128:131], v[184:187], v[68:71], v[128:131]\n"
        "ds_read_b64_tr_b16 v[206:207], v233 offset:61952\n"
        "v_mfma_f32_16x16x32_bf16 v[124:127], v[188:191], v[64:67], v[124:127]\n"
        "ds_read_b64_tr_b16 v[208:209], v234 offset:61440\n"
        "v_mfma_f32_16x16x32_bf16 v[132:135], v[188:191], v[68:71], v[132:135]\n"
        "ds_read_b64_tr_b16 v[210:211], v234 offset:61952\n"
        "s_waitcnt lgkmcnt(6)\n"
        "v_mfma_f32_16x16x32_bf16 v[136:139], v[192:195], v[64:67], v[136:139]\n"
        "ds_read_b64_tr_b16 v[184:185], v235 offset:61440\n"
        "v_mfma_f32_16x16x32_bf16 v[144:147], v[192:195], v[68:71], v[144:147]\n"
        "ds_read_b64_tr_b16 v[186:187], v235 offset:61952\n"
        "v_mfma_f32_16x16x32_bf16 v[140:143], v[196:199], v[64:67], v[140:143]\n"
        "v_xor_b32 v232, 0x10000, v230\n"
        "ds_read_b64_tr_b16 v[188:189], v232 offset:0\n"
        "v_mfma_f32_16x16x32_bf16 v[148:151], v[196:199], v[68:71], v[148:151]\n"
        "ds_read_b64_tr_b16 v[190:191], v232 offset:512\n"
        "s_waitcnt lgkmcnt(6)\n"
        "v_mfma_f32_16x16x32_bf16 v[152:155], v[200:203], v[64:67], v[152:155]\n"
        "v_xor_b32 v233, 0x10020, v230\n"
        "ds_read_b64_tr_b16 v[192:193], v233 offset:0\n"
        "v_mfma_f32_16x16x32_bf16 v[160:163], v[200:203], v[68:71], v[160:163]\n"
        "ds_read_b64_tr_b16 v[194:195], v233 offset:512\n"
        "v_mfma_f32_16x16x32_bf16 v[156:159], v[204:207], v[64:67], v[156:159]\n"
        "v_xor_b32 v234, 0x10040, v230\n"
        "ds_read_b64_tr_b16 v[196:197], v234 offset:0\n"
        "v_mfma_f32_16x16x32_bf16 v[164:167], v[204:207], v[68:71], v[164:167]\n"
        "ds_read_b64_tr_b16 v[198:199], v234 offset:512\n"
        "s_waitcnt lgkmcnt(6)\n"
        "v_mfma_f32_16x16x32_bf16 v[168:171], v[208:211], v[64:67], v[168:171]\n"
        "v_xor_b32 v235, 0x10060, v230\n"
        "ds_read_b64_tr_b16 v[200:201], v235 offset:0\n"
        "v_mfma_f32_16x16x32_bf16 v[176:179], v[208:211], v[68:71], v[176:179]\n"
        "ds_read_b64_tr_b16 v[202:203], v235 offset:512\n"
        "v_mfma_f32_16x16x32_bf16 v[172:175], v[184:187], v[64:67], v[172:175]\n"
        "ds_read_b64_tr_b16 v[204:205], v232 offset:8192\n"
        "v_mfma_f32_16x16x32_bf16 v[180:183], v[184:187], v[68:71], v[180:183]\n"
        "ds_read_b64_tr_b16 v[206:207], v232 offset:8704\n"
        "s_barrier\n"
        "s_waitcnt lgkmcnt(6)\n"
        "v_mfma_f32_16x16x32_bf16 v[120:123], v[188:191], v[72:75], v[120:123]\n"
        "ds_read_b64_tr_b16 v[208:209], v233 offset:8192\n"
        "v_mfma_f32_16x16x32_bf16 v[128:131], v[188:191], v[76:79], v[128:131]\n"
        "ds_read_b64_tr_b16 v[210:211], v233 offset:8704\n"
        "v_mfma_f32_16x16x32_bf16 v[124:127], v[192:195], v[72:75], v[124:127]\n"
        "ds_read_b64_tr_b16 v[184:185], v234 offset:8192\n"
        "v_mfma_f32_16x16x32_bf16 v[132:135], v[192:195], v[76:79], v[132:135]\n"
        "ds_read_b64_tr_b16 v[186:187], v234 offset:8704\n"
        "s_waitcnt lgkmcnt(6)\n"
        "v_mfma_f32_16x16x32_bf16 v[136:139], v[196:199], v[72:75], v[136:139]\n"
        "ds_read_b64_tr_b16 v[188:189], v235 offset:8192\n"
        "v_mfma_f32_16x16x32_bf16 v[144:147], v[196:199], v[76:79], v[144:147]\n"
        "ds_read_b64_tr_b16 v[190:191], v235 offset:8704\n"
        "v_mfma_f32_16x16x32_bf16 v[140:143], v[200:203], v[72:75], v[140:143]\n"
        "ds_read_b64_tr_b16 v[192:193], v232 offset:4096\n"
        "v_mfma_f32_16x16x32_bf16 v[148:151], v[200:203], v[76:79], v[148:151]\n"
        "ds_read_b64_tr_b16 v[194:195], v232 offset:4608\n"
        "s_waitcnt lgkmcnt(6)\n"
        "v_mfma_f32_16x16x32_bf16 v[152:155], v[204:207], v[72:75], v[152:155]\n"
        "ds_read_b64_tr_b16 v[196:197], v233 offset:4096\n"
        "v_mfma_f32_16x16x32_bf16 v[160:163], v[204:207], v[76:79], v[160:163]\n"
        "ds_read_b64_tr_b16 v[198:199], v233 offset:4608\n"
        "v_mfma_f32_16x16x32_bf16 v[156:159], v[208:211], v[72:75], v[156:159]\n"
        "ds_read_b64_tr_b16 v[200:201], v234 offset:4096\n"
        "v_mfma_f32_16x16x32_bf16 v[164:167], v[208:211], v[76:79], v[164:167]\n"
        "ds_read_b64_tr_b16 v[202:203], v234 offset:4608\n"
        "s_waitcnt lgkmcnt(6)\n"
        "v_mfma_f32_16x16x32_bf16 v[168:171], v[184:187], v[72:75], v[168:171]\n"
        "ds_read_b64_tr_b16 v[204:205], v235 offset:4096\n"
        "v_mfma_f32_16x16x32_bf16 v[176:179], v[184:187], v[76:79], v[176:179]\n"
        "ds_read_b64_tr_b16 v[206:207], v235 offset:4608\n"
        "v_mfma_f32_16x16x32_bf16 v[172:175], v[188:191], v[72:75], v[172:175]\n"
        "ds_read_b64_tr_b16 v[208:209], v232 offset:12288\n"
        "v_mfma_f32_16x16x32_bf16 v[180:183], v[188:191], v[76:79], v[180:183]\n"
        "ds_read_b64_tr_b16 v[210:211], v232 offset:12800\n"
        "s_waitcnt lgkmcnt(6)\n"
        "v_mfma_f32_16x16x32_bf16 v[120:123], v[192:195], v[80:83], v[120:123]\n"
        "ds_read_b64_tr_b16 v[184:185], v233 offset:12288\n"
        "v_mfma_f32_16x16x32_bf16 v[128:131], v[192:195], v[84:87], v[128:131]\n"
        "ds_read_b64_tr_b16 v[186:187], v233 offset:12800\n"
        "v_mfma_f32_16x16x32_bf16 v[124:127], v[196:199], v[80:83], v[124:127]\n"
        "ds_read_b64_tr_b16 v[188:189], v234 offset:12288\n"
        "v_mfma_f32_16x16x32_bf16 v[132:135], v[196:199], v[84:87], v[132:135]\n"
        "ds_read_b64_tr_b16 v[190:191], v234 offset:12800\n"
        "s_waitcnt lgkmcnt(6)\n"
        "v_mfma_f32_16x16x32_bf16 v[136:139], v[200:203], v[80:83], v[136:139]\n"
        "ds_read_b64_tr_b16 v[192:193], v235 offset:12288\n"
        "v_mfma_f32_16x16x32_bf16 v[144:147], v[200:203], v[84:87], v[144:147]\n"
        "ds_read_b64_tr_b16 v[194:195], v235 offset:12800\n"
        "v_mfma_f32_16x16x32_bf16 v[140:143], v[204:207], v[80:83], v[140:143]\n"
        "ds_read_b64_tr_b16 v[196:197], v232 offset:16384\n"
        "v_mfma_f32_16x16x32_bf16 v[148:151], v[204:207], v[84:87], v[148:151]\n"
        "ds_read_b64_tr_b16 v[198:199], v232 offset:16896\n"
        "s_waitcnt lgkmcnt(6)\n"
        "v_mfma_f32_16x16x32_bf16 v[152:155], v[208:211], v[80:83], v[152:155]\n"
        "ds_read_b64_tr_b16 v[200:201], v233 offset:16384\n"
        "v_mfma_f32_16x16x32_bf16 v[160:163], v[208:211], v[84:87], v[160:163]\n"
        "ds_read_b64_tr_b16 v[202:203], v233 offset:16896\n"
        "v_mfma_f32_16x16x32_bf16 v[156:159], v[184:187], v[80:83], v[156:159]\n"
        "ds_read_b64_tr_b16 v[204:205], v234 offset:16384\n"
        "v_mfma_f32_16x16x32_bf16 v[164:167], v[184:187], v[84:87], v[164:167]\n"
        "ds_read_b64_tr_b16 v[206:207], v234 offset:16896\n"
        "s_waitcnt lgkmcnt(6)\n"
        "v_mfma_f32_16x16x32_bf16 v[168:171], v[188:191], v[80:83], v[168:171]\n"
        "ds_read_b64_tr_b16 v[208:209], v235 offset:16384\n"
        "v_mfma_f32_16x16x32_bf16 v[176:179], v[188:191], v[84:87], v[176:179]\n"
        "ds_read_b64_tr_b16 v[210:211], v235 offset:16896\n"
        "v_mfma_f32_16x16x32_bf16 v[172:175], v[192:195], v[80:83], v[172:175]\n"
        "ds_read_b64_tr_b16 v[184:185], v232 offset:24576\n"
        "v_mfma_f32_16x16x32_bf16 v[180:183], v[192:195], v[84:87], v[180:183]\n"
        "ds_read_b64_tr_b16 v[186:187], v232 offset:25088\n"
        "s_barrier\n"
        "s_waitcnt lgkmcnt(6)\n"
        "v_mfma_f32_16x16x32_bf16 v[120:123], v[196:199], v[88:91], v[120:123]\n"
        "ds_read_b64_tr_b16 v[188:189], v233 offset:24576\n"
        "v_mfma_f32_16x16x32_bf16 v[128:131], v[196:199], v[92:95], v[128:131]\n"
        "ds_read_b64_tr_b16 v[190:191], v233 offset:25088\n"
        "v_mfma_f32_16x16x32_bf16 v[124:127], v[200:203], v[88:91], v[124:127]\n"
        "ds_read_b64_tr_b16 v[192:193], v234 offset:24576\n"
        "v_mfma_f32_16x16x32_bf16 v[132:135], v[200:203], v[92:95], v[132:135]\n"
        "ds_read_b64_tr_b16 v[194:195], v234 offset:25088\n"
        "s_waitcnt lgkmcnt(6)\n"
        "v_mfma_f32_16x16x32_bf16 v[136:139], v[204:207], v[88:91], v[136:139]\n"
        "ds_read_b64_tr_b16 v[196:197], v235 offset:24576\n"
        "v_mfma_f32_16x16x32_bf16 v[144:147], v[204:207], v[92:95], v[144:147]\n"
        "ds_read_b64_tr_b16 v[198:199], v235 offset:25088\n"
        "v_mfma_f32_16x16x32_bf16 v[140:143], v[208:211], v[88:91], v[140:143]\n"
        "ds_read_b64_tr_b16 v[200:201], v232 offset:20480\n"
        "v_mfma_f32_16x16x32_bf16 v[148:151], v[208:211], v[92:95], v[148:151]\n"
        "ds_read_b64_tr_b16 v[202:203], v232 offset:20992\n"
        "s_waitcnt lgkmcnt(6)\n"
        "v_mfma_f32_16x16x32_bf16 v[152:155], v[184:187], v[88:91], v[152:155]\n"
        "ds_read_b64_tr_b16 v[204:205], v233 offset:20480\n"
        "v_mfma_f32_16x16x32_bf16 v[160:163], v[184:187], v[92:95], v[160:163]\n"
        "ds_read_b64_tr_b16 v[206:207], v233 offset:20992\n"
        "v_mfma_f32_16x16x32_bf16 v[156:159], v[188:191], v[88:91], v[156:159]\n"
        "ds_read_b64_tr_b16 v[208:209], v234 offset:20480\n"
        "v_mfma_f32_16x16x32_bf16 v[164:167], v[188:191], v[92:95], v[164:167]\n"
        "ds_read_b64_tr_b16 v[210:211], v234 offset:20992\n"
        "s_waitcnt lgkmcnt(6)\n"
        "v_mfma_f32_16x16x32_bf16 v[168:171], v[192:195], v[88:91], v[168:171]\n"
        "ds_read_b64_tr_b16 v[184:185], v235 offset:20480\n"
        "v_mfma_f32_16x16x32_bf16 v[176:179], v[192:195], v[92:95], v[176:179]\n"
        "ds_read_b64_tr_b16 v[186:187], v235 offset:20992\n"
        "v_mfma_f32_16x16x32_bf16 v[172:175], v[196:199], v[88:91], v[172:175]\n"
        "ds_read_b64_tr_b16 v[188:189], v232 offset:28672\n"
        "v_mfma_f32_16x16x32_bf16 v[180:183], v[196:199], v[92:95], v[180:183]\n"
        "ds_read_b64_tr_b16 v[190:191], v232 offset:29184\n"
        "s_waitcnt lgkmcnt(6)\n"
        "v_mfma_f32_16x16x32_bf16 v[120:123], v[200:203], v[96:99], v[120:123]\n"
        "ds_read_b64_tr_b16 v[192:193], v233 offset:28672\n"
        "v_mfma_f32_16x16x32_bf16 v[128:131], v[200:203], v[100:103], v[128:131]\n"
        "ds_read_b64_tr_b16 v[194:195], v233 offset:29184\n"
        "v_mfma_f32_16x16x32_bf16 v[124:127], v[204:207], v[96:99], v[124:127]\n"
        "ds_read_b64_tr_b16 v[196:197], v234 offset:28672\n"
        "v_mfma_f32_16x16x32_bf16 v[132:135], v[204:207], v[100:103], v[132:135]\n"
        "ds_read_b64_tr_b16 v[198:199], v234 offset:29184\n"
        "s_waitcnt lgkmcnt(6)\n"
        "v_mfma_f32_16x16x32_bf16 v[136:139], v[208:211], v[96:99], v[136:139]\n"
        "ds_read_b64_tr_b16 v[200:201], v235 offset:28672\n"
        "v_mfma_f32_16x16x32_bf16 v[144:147], v[208:211], v[100:103], v[144:147]\n"
        "ds_read_b64_tr_b16 v[202:203], v235 offset:29184\n"
        "v_mfma_f32_16x16x32_bf16 v[140:143], v[184:187], v[96:99], v[140:143]\n"
        "v_xor_b32 v232, 0x0, v230\n"
        "ds_read_b64_tr_b16 v[204:205], v232 offset:0\n"
        "v_mfma_f32_16x16x32_bf16 v[148:151], v[184:187], v[100:103], v[148:151]\n"
        "ds_read_b64_tr_b16 v[206:207], v232 offset:512\n"
        "s_waitcnt lgkmcnt(6)\n"
        "v_mfma_f32_16x16x32_bf16 v[152:155], v[188:191], v[96:99], v[152:155]\n"
        "v_xor_b32 v233, 0x20, v230\n"
        "ds_read_b64_tr_b16 v[208:209], v233 offset:0\n"
        "v_mfma_f32_16x16x32_bf16 v[160:163], v[188:191], v[100:103], v[160:163]\n"
        "ds_read_b64_tr_b16 v[210:211], v233 offset:512\n"
        "v_mfma_f32_16x16x32_bf16 v[156:159], v[192:195], v[96:99], v[156:159]\n"
        "v_xor_b32 v234, 0x40, v230\n"
        "ds_read_b64_tr_b16 v[184:185], v234 offset:0\n"
        "v_mfma_f32_16x16x32_bf16 v[164:167], v[192:195], v[100:103], v[164:167]\n"
        "ds_read_b64_tr_b16 v[186:187], v234 offset:512\n"
        "s_waitcnt lgkmcnt(6)\n"
        "v_mfma_f32_16x16x32_bf16 v[168:171], v[196:199], v[96:99], v[168:171]\n"
        "v_xor_b32 v235, 0x60, v230\n"
        "ds_read_b64_tr_b16 v[188:189], v235 offset:0\n"
        "v_mfma_f32_16x16x32_bf16 v[176:179], v[196:199], v[100:103], v[176:179]\n"
        "ds_read_b64_tr_b16 v[190:191], v235 offset:512\n"
        "v_mfma_f32_16x16x32_bf16 v[172:175], v[200:203], v[96:99], v[172:175]\n"
        "ds_read_b64_tr_b16 v[192:193], v232 offset:8192\n"
        "v_mfma_f32_16x16x32_bf16 v[180:183], v[200:203], v[100:103], v[180:183]\n"
        "ds_read_b64_tr_b16 v[194:195], v232 offset:8704\n"
        "s_waitcnt lgkmcnt(6)\n"
        "v_mfma_f32_16x16x32_bf16 v[120:123], v[204:207], v[104:107], v[120:123]\n"
        "ds_read_b64_tr_b16 v[196:197], v233 offset:8192\n"
        "v_mfma_f32_16x16x32_bf16 v[128:131], v[204:207], v[108:111], v[128:131]\n"
        "ds_read_b64_tr_b16 v[198:199], v233 offset:8704\n"
        "v_mfma_f32_16x16x32_bf16 v[124:127], v[208:211], v[104:107], v[124:127]\n"
        "ds_read_b64_tr_b16 v[200:201], v234 offset:8192\n"
        "v_mfma_f32_16x16x32_bf16 v[132:135], v[208:211], v[108:111], v[132:135]\n"
        "ds_read_b64_tr_b16 v[202:203], v234 offset:8704\n"
        "s_waitcnt lgkmcnt(6)\n"
        "v_mfma_f32_16x16x32_bf16 v[136:139], v[184:187], v[104:107], v[136:139]\n"
        "ds_read_b64_tr_b16 v[204:205], v235 offset:8192\n"
        "v_mfma_f32_16x16x32_bf16 v[144:147], v[184:187], v[108:111], v[144:147]\n"
        "ds_read_b64_tr_b16 v[206:207], v235 offset:8704\n"
        "v_mfma_f32_16x16x32_bf16 v[140:143], v[188:191], v[104:107], v[140:143]\n"
        "ds_read_b64_tr_b16 v[208:209], v232 offset:4096\n"
        "v_mfma_f32_16x16x32_bf16 v[148:151], v[188:191], v[108:111], v[148:151]\n"
        "ds_read_b64_tr_b16 v[210:211], v232 offset:4608\n"
        "s_waitcnt lgkmcnt(6)\n"
        "v_mfma_f32_16x16x32_bf16 v[152:155], v[192:195], v[104:107], v[152:155]\n"
        "ds_read_b64_tr_b16 v[184:185], v233 offset:4096\n"
        "v_mfma_f32_16x16x32_bf16 v[160:163], v[192:195], v[108:111], v[160:163]\n"
        "ds_read_b64_tr_b16 v[186:187], v233 offset:4608\n"
        "v_mfma_f32_16x16x32_bf16 v[156:159], v[196:199], v[104:107], v[156:159]\n"
        "ds_read_b64_tr_b16 v[188:189], v234 offset:4096\n"
        "v_mfma_f32_16x16x32_bf16 v[164:167], v[196:199], v[108:111], v[164:167]\n"
        "ds_read_b64_tr_b16 v[190:191], v234 offset:4608\n"
        "s_waitcnt lgkmcnt(6)\n"
        "v_mfma_f32_16x16x32_bf16 v[168:171], v[200:203], v[104:107], v[168:171]\n"
        "ds_read_b64_tr_b16 v[192:193], v235 offset:4096\n"
        "v_mfma_f32_16x16x32_bf16 v[176:179], v[200:203], v[108:111], v[176:179]\n"
        "ds_read_b64_tr_b16 v[194:195], v235 offset:4608\n"
        "v_mfma_f32_16x16x32_bf16 v[172:175], v[204:207], v[104:107], v[172:175]\n"
        "ds_read_b64_tr_b16 v[196:197], v232 offset:12288\n"
        "v_mfma_f32_16x16x32_bf16 v[180:183], v[204:207], v[108:111], v[180:183]\n"
        "ds_read_b64_tr_b16 v[198:199], v232 offset:12800\n"
        "s_waitcnt lgkmcnt(6)\n"
        "v_mfma_f32_16x16x32_bf16 v[120:123], v[208:211], v[112:115], v[120:123]\n"
        "ds_read_b64_tr_b16 v[200:201], v233 offset:12288\n"
        "v_mfma_f32_16x16x32_bf16 v[128:131], v[208:211], v[116:119], v[128:131]\n"
        "ds_read_b64_tr_b16 v[202:203], v233 offset:12800\n"
        "v_mfma_f32_16x16x32_bf16 v[124:127], v[184:187], v[112:115], v[124:127]\n"
        "ds_read_b64_tr_b16 v[204:205], v234 offset:12288\n"
        "v_mfma_f32_16x16x32_bf16 v[132:135], v[184:187], v[116:119], v[132:135]\n"
        "ds_read_b64_tr_b16 v[206:207], v234 offset:12800\n"
        "s_waitcnt lgkmcnt(6)\n"
        "v_mfma_f32_16x16x32_bf16 v[136:139], v[188:191], v[112:115], v[136:139]\n"
        "ds_read_b64_tr_b16 v[208:209], v235 offset:12288\n"
        "v_mfma_f32_16x16x32_bf16 v[144:147], v[188:191], v[116:119], v[144:147]\n"
        "ds_read_b64_tr_b16 v[210:211], v235 offset:12800\n"
        "v_mfma_f32_16x16x32_bf16 v[140:143], v[192:195], v[112:115], v[140:143]\n"
        "v_mfma_f32_16x16x32_bf16 v[148:151], v[192:195], v[116:119], v[148:151]\n"
        "s_waitcnt lgkmcnt(4)\n"
        "v_mfma_f32_16x16x32_bf16 v[152:155], v[196:199], v[112:115], v[152:155]\n"
        "v_mfma_f32_16x16x32_bf16 v[160:163], v[196:199], v[116:119], v[160:163]\n"
        "v_mfma_f32_16x16x32_bf16 v[156:159], v[200:203], v[112:115], v[156:159]\n"
        "v_mfma_f32_16x16x32_bf16 v[164:167], v[200:203], v[116:119], v[164:167]\n"
        "s_waitcnt lgkmcnt(0)\n"
        "v_mfma_f32_16x16x32_bf16 v[168:171], v[204:207], v[112:115], v[168:171]\n"
        "v_mfma_f32_16x16x32_bf16 v[176:179], v[204:207], v[116:119], v[176:179]\n"
        "v_mfma_f32_16x16x32_bf16 v[172:175], v[208:211], v[112:115], v[172:175]\n"
        "v_mfma_f32_16x16x32_bf16 v[180:183], v[208:211], v[116:119], v[180:183]\n"
        "s_setprio 3\n"
        "v_pk_mul_f32 v[216:217], v[120:121], v[120:121]\n"
        "v_pk_mul_f32 v[218:219], v[122:123], v[122:123]\n"
        "v_pk_mul_f32 v[220:221], v[124:125], v[124:125]\n"
        "v_pk_mul_f32 v[222:223], v[126:127], v[126:127]\n"
        "v_pk_fma_f32 v[216:217], v[216:217], s[86:87], v[6:7]\n"
        "v_pk_fma_f32 v[218:219], v[218:219], s[86:87], v[6:7]\n"
        "v_pk_fma_f32 v[220:221], v[220:221], s[86:87], v[6:7]\n"
        "v_pk_fma_f32 v[222:223], v[222:223], s[86:87], v[6:7]\n"
        "v_pk_mul_f32 v[216:217], v[216:217], v[120:121]\n"
        "v_pk_mul_f32 v[218:219], v[218:219], v[122:123]\n"
        "v_pk_mul_f32 v[220:221], v[220:221], v[124:125]\n"
        "v_pk_mul_f32 v[222:223], v[222:223], v[126:127]\n"
        "v_exp_f32 v216, v216\n"
        "v_exp_f32 v217, v217\n"
        "v_pk_mul_f32 v[152:153], v[152:153], v[120:121]\n"
        "v_exp_f32 v218, v218\n"
        "v_exp_f32 v219, v219\n"
        "v_pk_mul_f32 v[154:155], v[154:155], v[122:123]\n"
        "v_exp_f32 v220, v220\n"
        "v_exp_f32 v221, v221\n"
        "v_pk_mul_f32 v[156:157], v[156:157], v[124:125]\n"
        "v_exp_f32 v222, v222\n"
        "v_exp_f32 v223, v223\n"
        "v_pk_mul_f32 v[158:159], v[158:159], v[126:127]\n"
        "v_pk_add_f32 v[216:217], v[216:217], s[88:89]\n"
        "v_pk_add_f32 v[218:219], v[218:219], s[88:89]\n"
        "s_nop 0\n"
        "s_cbranch_execz Lback\n"
        "v_pk_add_f32 v[220:221], v[220:221], s[88:89]\n"
        "v_pk_add_f32 v[222:223], v[222:223], s[88:89]\n"
        "v_rcp_f32 v216, v216\n"
        "v_rcp_f32 v217, v217\n"
        "v_rcp_f32 v218, v218\n"
        "v_rcp_f32 v219, v219\n"
        "v_rcp_f32 v220, v220\n"
        "v_rcp_f32 v221, v221\n"
        "v_rcp_f32 v222, v222\n"
        "v_rcp_f32 v223, v223\n"
        "v_pk_mul_f32 v[152:153], v[152:153], v[216:217]\n"
        "v_pk_mul_f32 v[154:155], v[154:155], v[218:219]\n"
        "v_pk_mul_f32 v[156:157], v[156:157], v[220:221]\n"
        "v_pk_mul_f32 v[158:159], v[158:159], v[222:223]\n"
        "v_cvt_pk_bf16_f32 v120, v152, v153\n"
        "v_cvt_pk_bf16_f32 v121, v154, v155\n"
        "v_cvt_pk_bf16_f32 v122, v156, v157\n"
        "v_cvt_pk_bf16_f32 v123, v158, v159\n"
        "buffer_store_dwordx4 v[120:123], v236, s[60:63], s82 offen offset:0 sc1\n"
        "v_pk_mul_f32 v[216:217], v[128:129], v[128:129]\n"
        "v_pk_mul_f32 v[218:219], v[130:131], v[130:131]\n"
        "v_pk_mul_f32 v[220:221], v[132:133], v[132:133]\n"
        "v_pk_mul_f32 v[222:223], v[134:135], v[134:135]\n"
        "v_pk_fma_f32 v[216:217], v[216:217], s[86:87], v[6:7]\n"
        "v_pk_fma_f32 v[218:219], v[218:219], s[86:87], v[6:7]\n"
        "v_pk_fma_f32 v[220:221], v[220:221], s[86:87], v[6:7]\n"
        "v_pk_fma_f32 v[222:223], v[222:223], s[86:87], v[6:7]\n"
        "v_pk_mul_f32 v[216:217], v[216:217], v[128:129]\n"
        "v_pk_mul_f32 v[218:219], v[218:219], v[130:131]\n"
        "v_pk_mul_f32 v[220:221], v[220:221], v[132:133]\n"
        "v_pk_mul_f32 v[222:223], v[222:223], v[134:135]\n"
        "v_exp_f32 v216, v216\n"
        "v_exp_f32 v217, v217\n"
        "v_pk_mul_f32 v[160:161], v[160:161], v[128:129]\n"
        "v_exp_f32 v218, v218\n"
        "v_exp_f32 v219, v219\n"
        "v_pk_mul_f32 v[162:163], v[162:163], v[130:131]\n"
        "v_exp_f32 v220, v220\n"
        "v_exp_f32 v221, v221\n"
        "v_pk_mul_f32 v[164:165], v[164:165], v[132:133]\n"
        "v_exp_f32 v222, v222\n"
        "v_exp_f32 v223, v223\n"
        "v_pk_mul_f32 v[166:167], v[166:167], v[134:135]\n"
        "v_pk_add_f32 v[216:217], v[216:217], s[88:89]\n"
        "v_pk_add_f32 v[218:219], v[218:219], s[88:89]\n"
        "v_pk_add_f32 v[220:221], v[220:221], s[88:89]\n"
        "v_pk_add_f32 v[222:223], v[222:223], s[88:89]\n"
        "v_rcp_f32 v216, v216\n"
        "v_rcp_f32 v217, v217\n"
        "v_rcp_f32 v218, v218\n"
        "v_rcp_f32 v219, v219\n"
        "v_rcp_f32 v220, v220\n"
        "v_rcp_f32 v221, v221\n"
        "v_rcp_f32 v222, v222\n"
        "v_rcp_f32 v223, v223\n"
        "v_pk_mul_f32 v[160:161], v[160:161], v[216:217]\n"
        "v_pk_mul_f32 v[162:163], v[162:163], v[218:219]\n"
        "v_pk_mul_f32 v[164:165], v[164:165], v[220:221]\n"
        "v_pk_mul_f32 v[166:167], v[166:167], v[222:223]\n"
        "v_cvt_pk_bf16_f32 v128, v160, v161\n"
        "v_cvt_pk_bf16_f32 v129, v162, v163\n"
        "v_cvt_pk_bf16_f32 v130, v164, v165\n"
        "v_cvt_pk_bf16_f32 v131, v166, v167\n"
        "buffer_store_dwordx4 v[128:131], v236, s[60:63], s82 offen offset:1024 sc1\n"
        "v_pk_mul_f32 v[216:217], v[136:137], v[136:137]\n"
        "v_pk_mul_f32 v[218:219], v[138:139], v[138:139]\n"
        "v_pk_mul_f32 v[220:221], v[140:141], v[140:141]\n"
        "v_pk_mul_f32 v[222:223], v[142:143], v[142:143]\n"
        "v_pk_fma_f32 v[216:217], v[216:217], s[86:87], v[6:7]\n"
        "v_pk_fma_f32 v[218:219], v[218:219], s[86:87], v[6:7]\n"
        "v_pk_fma_f32 v[220:221], v[220:221], s[86:87], v[6:7]\n"
        "v_pk_fma_f32 v[222:223], v[222:223], s[86:87], v[6:7]\n"
        "v_pk_mul_f32 v[216:217], v[216:217], v[136:137]\n"
        "v_pk_mul_f32 v[218:219], v[218:219], v[138:139]\n"
        "v_pk_mul_f32 v[220:221], v[220:221], v[140:141]\n"
        "v_pk_mul_f32 v[222:223], v[222:223], v[142:143]\n"
        "v_exp_f32 v216, v216\n"
        "v_exp_f32 v217, v217\n"
        "v_pk_mul_f32 v[168:169], v[168:169], v[136:137]\n"
        "v_exp_f32 v218, v218\n"
        "v_exp_f32 v219, v219\n"
        "v_pk_mul_f32 v[170:171], v[170:171], v[138:139]\n"
        "v_exp_f32 v220, v220\n"
        "v_exp_f32 v221, v221\n"
        "v_pk_mul_f32 v[172:173], v[172:173], v[140:141]\n"
        "v_exp_f32 v222, v222\n"
        "v_exp_f32 v223, v223\n"
        "v_pk_mul_f32 v[174:175], v[174:175], v[142:143]\n"
        "v_pk_add_f32 v[216:217], v[216:217], s[88:89]\n"
        "v_pk_add_f32 v[218:219], v[218:219], s[88:89]\n"
        "v_pk_add_f32 v[220:221], v[220:221], s[88:89]\n"
        "v_pk_add_f32 v[222:223], v[222:223], s[88:89]\n"
        "v_rcp_f32 v216, v216\n"
        "v_rcp_f32 v217, v217\n"
        "v_rcp_f32 v218, v218\n"
        "v_rcp_f32 v219, v219\n"
        "v_rcp_f32 v220, v220\n"
        "v_rcp_f32 v221, v221\n"
        "v_rcp_f32 v222, v222\n"
        "v_rcp_f32 v223, v223\n"
        "v_pk_mul_f32 v[168:169], v[168:169], v[216:217]\n"
        "v_pk_mul_f32 v[170:171], v[170:171], v[218:219]\n"
        "v_pk_mul_f32 v[172:173], v[172:173], v[220:221]\n"
        "v_pk_mul_f32 v[174:175], v[174:175], v[222:223]\n"
        "v_cvt_pk_bf16_f32 v136, v168, v169\n"
        "v_cvt_pk_bf16_f32 v137, v170, v171\n"
        "v_cvt_pk_bf16_f32 v138, v172, v173\n"
        "v_cvt_pk_bf16_f32 v139, v174, v175\n"
        "buffer_store_dwordx4 v[136:139], v236, s[60:63], s83 offen offset:0 sc1\n"
        "v_pk_mul_f32 v[216:217], v[144:145], v[144:145]\n"
        "v_pk_mul_f32 v[218:219], v[146:147], v[146:147]\n"
        "v_pk_mul_f32 v[220:221], v[148:149], v[148:149]\n"
        "v_pk_mul_f32 v[222:223], v[150:151], v[150:151]\n"
        "v_pk_fma_f32 v[216:217], v[216:217], s[86:87], v[6:7]\n"
        "v_pk_fma_f32 v[218:219], v[218:219], s[86:87], v[6:7]\n"
        "v_pk_fma_f32 v[220:221], v[220:221], s[86:87], v[6:7]\n"
        "v_pk_fma_f32 v[222:223], v[222:223], s[86:87], v[6:7]\n"
        "v_pk_mul_f32 v[216:217], v[216:217], v[144:145]\n"
        "v_pk_mul_f32 v[218:219], v[218:219], v[146:147]\n"
        "v_pk_mul_f32 v[220:221], v[220:221], v[148:149]\n"
        "v_pk_mul_f32 v[222:223], v[222:223], v[150:151]\n"
        "v_exp_f32 v216, v216\n"
        "v_exp_f32 v217, v217\n"
        "v_pk_mul_f32 v[176:177], v[176:177], v[144:145]\n"
        "v_exp_f32 v218, v218\n"
        "v_exp_f32 v219, v219\n"
        "v_pk_mul_f32 v[178:179], v[178:179], v[146:147]\n"
        "v_exp_f32 v220, v220\n"
        "v_exp_f32 v221, v221\n"
        "v_pk_mul_f32 v[180:181], v[180:181], v[148:149]\n"
        "v_exp_f32 v222, v222\n"
        "v_exp_f32 v223, v223\n"
        "v_pk_mul_f32 v[182:183], v[182:183], v[150:151]\n"
        "v_pk_add_f32 v[216:217], v[216:217], s[88:89]\n"
        "v_pk_add_f32 v[218:219], v[218:219], s[88:89]\n"
        "v_pk_add_f32 v[220:221], v[220:221], s[88:89]\n"
        "v_pk_add_f32 v[222:223], v[222:223], s[88:89]\n"
        "v_rcp_f32 v216, v216\n"
        "v_rcp_f32 v217, v217\n"
        "v_rcp_f32 v218, v218\n"
        "v_rcp_f32 v219, v219\n"
        "v_rcp_f32 v220, v220\n"
        "v_rcp_f32 v221, v221\n"
        "v_rcp_f32 v222, v222\n"
        "v_rcp_f32 v223, v223\n"
        "v_pk_mul_f32 v[176:177], v[176:177], v[216:217]\n"
        "v_pk_mul_f32 v[178:179], v[178:179], v[218:219]\n"
        "v_pk_mul_f32 v[180:181], v[180:181], v[220:221]\n"
        "v_pk_mul_f32 v[182:183], v[182:183], v[222:223]\n"
        "v_cvt_pk_bf16_f32 v144, v176, v177\n"
        "v_cvt_pk_bf16_f32 v145, v178, v179\n"
        "v_cvt_pk_bf16_f32 v146, v180, v181\n"
        "v_cvt_pk_bf16_f32 v147, v182, v183\n"
        "buffer_store_dwordx4 v[144:147], v236, s[60:63], s83 offen offset:1024 sc1\n"
        "s_barrier\n"
        "s_waitcnt vmcnt(0)\n"
        "s_add_u32 s66, s95, 262144\n"
        "s_mov_b64 exec, 0xffff\n"
        "buffer_store_dwordx2 v[242:243], v2, s[60:63], s66 offen sc1\n"
        "s_mov_b64 exec, -1\n"
        "s_setprio 0\n"
        "s_sleep 12\n"
        "s_lshr_b32 s67, s97, 1\n"
        "s_lshl_b32 s68, s74, 5\n"
        "s_add_u32 s67, s67, s68\n"
        "s_and_b32 s68, s97, 1\n"
        "s_lshl4_add_u32 s67, s68, s67\n"
        "s_lshl_b32 s67, s67, 7\n"
        "s_add_u32 s67, s67, 266240\n"
        "s_add_u32 s68, s60, s67\n"
        "s_addc_u32 s69, s61, 0\n"
        "s_mov_b32 s79, 2000\n"
        "Lsp5:\n"
        "s_load_dwordx2 s[32:33], s[68:69], 0x0 glc\n"
        "s_load_dwordx2 s[34:35], s[68:69], 0x100 glc\n"
        "s_load_dwordx2 s[36:37], s[68:69], 0x200 glc\n"
        "s_load_dwordx2 s[38:39], s[68:69], 0x300 glc\n"
        "s_load_dwordx2 s[40:41], s[68:69], 0x400 glc\n"
        "s_load_dwordx2 s[42:43], s[68:69], 0x500 glc\n"
        "s_load_dwordx2 s[44:45], s[68:69], 0x600 glc\n"
        "s_waitcnt lgkmcnt(0)\n"
        "Lsp5m:\n"
        "s_xor_b64 s[32:33], s[32:33], s[70:71]\n"
        "s_xor_b64 s[34:35], s[34:35], s[70:71]\n"
        "s_xor_b64 s[36:37], s[36:37], s[70:71]\n"
        "s_xor_b64 s[38:39], s[38:39], s[70:71]\n"
        "s_xor_b64 s[40:41], s[40:41], s[70:71]\n"
        "s_xor_b64 s[42:43], s[42:43], s[70:71]\n"
        "s_xor_b64 s[44:45], s[44:45], s[70:71]\n"
        "s_or_b64 s[32:33], s[32:33], s[34:35]\n"
        "s_or_b64 s[32:33], s[32:33], s[36:37]\n"
        "s_or_b64 s[32:33], s[32:33], s[38:39]\n"
        "s_or_b64 s[32:33], s[32:33], s[40:41]\n"
        "s_or_b64 s[32:33], s[32:33], s[42:43]\n"
        "s_or_b64 s[32:33], s[32:33], s[44:45]\n"
        "s_cbranch_scc0 Lso5\n"
        "s_sub_u32 s79, s79, 1\n"
        "s_cbranch_scc0 Lsp5\n"
        "s_lshl2_add_u32 s67, s74, s97\n"
        "s_mul_i32 s67, s67, 73728\n"
        "s_add_u32 s67, s67, 1048576\n"
        "v_lshl_add_u32 v237, v1, 4, s67\n"
        "s_lshr_b32 s68, s97, 1\n"
        "s_lshl_b32 s68, s68, 7\n"
        "s_add_u32 s68, s68, 4096\n"
        "s_lshl_b32 s67, s74, 12\n"
        "s_add_u32 s67, s67, s68\n"
        "v_lshrrev_b32 v216, 5, v1\n"
        "v_lshl_add_u32 v216, v216, 11, s67\n"
        "v_bfe_u32 v217, v1, 2, 3\n"
        "v_min_u32 v217, 6, v217\n"
        "v_lshl_add_u32 v240, v217, 8, v216\n"
        "s_lshl_b32 s67, s74, 11\n"
        "s_add_u32 s67, s67, s68\n"
        "v_lshl_add_u32 v231, v4, 8, s67\n"
        "s_mov_b32 s66, 262144\n"
        "buffer_load_dwordx2 v[244:245], v240, s[60:63], s66 offen sc1\n"
        "s_waitcnt vmcnt(0)\n"
        "v_cmp_ne_u64 vcc, s[70:71], v[244:245]\n"
        "s_cbranch_vccz Lret6\n"
        "s_mov_b32 s79, 400000\n"
        "s_mov_b32 s66, 262144\n"
        "Lspin6:\n"
        "s_sleep 2\n"
        "buffer_load_dwordx2 v[244:245], v240, s[60:63], s66 offen sc1\n"
        "s_waitcnt vmcnt(0)\n"
        "v_cmp_ne_u64 vcc, s[70:71], v[244:245]\n"
        "s_cbranch_vccz Lret6\n"
        "s_sub_u32 s79, s79, 1\n"
        "s_cmp_lg_u32 s79, 0\n"
        "s_cbranch_scc1 Lspin6\n"
        "Lret6:\n"
        "s_nop 0\n"
        "Lso5:\n"
        "s_barrier\n"
        "s_sleep 8\n"
        "s_lshl2_add_u32 s67, s74, 0\n"
        "s_lshl1_add_u32 s67, s67, s97\n"
        "s_lshl_b32 s67, s67, 8\n"
        "s_add_u32 s67, s67, 397312\n"
        "s_add_u32 s68, s60, s67\n"
        "s_addc_u32 s69, s61, 0\n"
        "s_mov_b32 s79, 2000\n"
        "Lsp7:\n"
        "s_load_dwordx2 s[32:33], s[68:69], 0x0 glc\n"
        "s_load_dwordx2 s[34:35], s[68:69], 0x80 glc\n"
        "s_waitcnt lgkmcnt(0)\n"
        "Lsp7m:\n"
        "s_xor_b64 s[32:33], s[32:33], s[70:71]\n"
        "s_xor_b64 s[34:35], s[34:35], s[70:71]\n"
        "s_or_b64 s[32:33], s[32:33], s[34:35]\n"
        "s_cbranch_scc0 Lso7\n"
        "s_sub_u32 s79, s79, 1\n"
        "s_cbranch_scc0 Lsp7\n"
        "s_lshl2_add_u32 s67, s74, s97\n"
        "s_mul_i32 s67, s67, 73728\n"
        "s_add_u32 s67, s67, 1048576\n"
        "v_lshl_add_u32 v237, v1, 4, s67\n"
        "s_lshr_b32 s68, s97, 1\n"
        "s_lshl_b32 s68, s68, 7\n"
        "s_add_u32 s68, s68, 4096\n"
        "s_lshl_b32 s67, s74, 12\n"
        "s_add_u32 s67, s67, s68\n"
        "v_lshrrev_b32 v216, 5, v1\n"
        "v_lshl_add_u32 v216, v216, 11, s67\n"
        "v_bfe_u32 v217, v1, 2, 3\n"
        "v_min_u32 v217, 6, v217\n"
        "v_lshl_add_u32 v240, v217, 8, v216\n"
        "s_lshl_b32 s67, s74, 11\n"
        "s_add_u32 s67, s67, s68\n"
        "v_lshl_add_u32 v231, v4, 8, s67\n"
        "s_mov_b32 s66, 393216\n"
        "buffer_load_dwordx2 v[244:245], v231, s[60:63], s66 offen sc1\n"
        "s_waitcnt vmcnt(0)\n"
        "v_cmp_ne_u64 vcc, s[70:71], v[244:245]\n"
        "s_cbranch_vccz Lret8\n"
        "s_mov_b32 s79, 400000\n"
        "s_mov_b32 s66, 393216\n"
        "Lspin8:\n"
        "s_sleep 2\n"
        "buffer_load_dwordx2 v[244:245], v231, s[60:63], s66 offen sc1\n"
        "s_waitcnt vmcnt(0)\n"
        "v_cmp_ne_u64 vcc, s[70:71], v[244:245]\n"
        "s_cbranch_vccz Lret8\n"
        "s_sub_u32 s79, s79, 1\n"
        "s_cmp_lg_u32 s79, 0\n"
        "s_cbranch_scc1 Lspin8\n"
        "Lret8:\n"
        "s_nop 0\n"
        "Lso7:\n"
        "s_barrier\n"
        "s_lshr_b32 s67, s64, 1\n"
        "s_lshl_b32 s67, s67, 3\n"
        "s_lshr_b32 s67, 0x3020100, s67\n"
        "s_and_b32 s67, s67, 0xff\n"
        "s_lshl_b32 s67, s67, 14\n"
        "s_and_b32 s68, s64, 1\n"
        "s_lshl_b32 s68, s68, 3\n"
        "v_lshrrev_b32 v216, 3, v1\n"
        "v_add_u32 v216, s68, v216\n"
        "v_lshlrev_b32 v217, 1, v5\n"
        "v_xor_b32 v216, v216, v217\n"
        "v_lshlrev_b32 v216, 4, v216\n"
        "v_lshl_add_u32 v216, v5, 9, v216\n"
        "v_add_u32 v230, s67, v216\n"
        "v_lshlrev_b32 v216, 16, v246\n"
        "v_and_b32 v217, s78, v246\n"
        "v_lshlrev_b32 v218, 16, v247\n"
        "v_and_b32 v219, s78, v247\n"
        "v_lshlrev_b32 v220, 16, v248\n"
        "v_and_b32 v221, s78, v248\n"
        "v_lshlrev_b32 v222, 16, v249\n"
        "v_and_b32 v223, s78, v249\n"
        "s_barrier\n"
        "ds_read_b128 v[8:11], v230 offset:0\n"
        "ds_read_b128 v[12:15], v230 offset:256\n"
        "ds_read_b128 v[16:19], v230 offset:4096\n"
        "ds_read_b128 v[20:23], v230 offset:4352\n"
        "ds_read_b128 v[24:27], v230 offset:8192\n"
        "ds_read_b128 v[28:31], v230 offset:8448\n"
        "ds_read_b128 v[32:35], v230 offset:12288\n"
        "ds_read_b128 v[36:39], v230 offset:12544\n"
        "s_waitcnt lgkmcnt(7)\n"
        "v_pk_add_f32 v[216:217], v[216:217], v[8:9]\n"
        "v_pk_add_f32 v[218:219], v[218:219], v[10:11]\n"
        "s_waitcnt lgkmcnt(6)\n"
        "v_pk_add_f32 v[220:221], v[220:221], v[12:13]\n"
        "v_pk_add_f32 v[222:223], v[222:223], v[14:15]\n"
        "s_waitcnt lgkmcnt(5)\n"
        "v_pk_add_f32 v[216:217], v[216:217], v[16:17]\n"
        "v_pk_add_f32 v[218:219], v[218:219], v[18:19]\n"
        "s_waitcnt lgkmcnt(4)\n"
        "v_pk_add_f32 v[220:221], v[220:221], v[20:21]\n"
        "v_pk_add_f32 v[222:223], v[222:223], v[22:23]\n"
        "s_waitcnt lgkmcnt(3)\n"
        "v_pk_add_f32 v[216:217], v[216:217], v[24:25]\n"
        "v_pk_add_f32 v[218:219], v[218:219], v[26:27]\n"
        "s_waitcnt lgkmcnt(2)\n"
        "v_pk_add_f32 v[220:221], v[220:221], v[28:29]\n"
        "v_pk_add_f32 v[222:223], v[222:223], v[30:31]\n"
        "s_waitcnt lgkmcnt(1)\n"
        "v_pk_add_f32 v[216:217], v[216:217], v[32:33]\n"
        "v_pk_add_f32 v[218:219], v[218:219], v[34:35]\n"
        "s_waitcnt lgkmcnt(0)\n"
        "v_pk_add_f32 v[220:221], v[220:221], v[36:37]\n"
        "v_pk_add_f32 v[222:223], v[222:223], v[38:39]\n"
        "v_cvt_pk_bf16_f32 v246, v216, v217\n"
        "v_cvt_pk_bf16_f32 v247, v218, v219\n"
        "v_cvt_pk_bf16_f32 v248, v220, v221\n"
        "v_cvt_pk_bf16_f32 v249, v222, v223\n"
        "buffer_store_dwordx4 v[246:249], v239, s[56:59], 0 offen\n"
        "s_branch Lend\n"
        "Lrole_V:\n"
        "s_cmp_eq_u32 s96, 0\n"
        "s_cbranch_scc1 Lrole_G\n"
        "s_waitcnt vmcnt(5) lgkmcnt(0)\n"
        "s_barrier\n"
        "ds_read_b128 v[8:11], v250 offset:0\n"
        "v_xor_b32 v232, 0x0, v230\n"
        "ds_read_b64_tr_b16 v[184:185], v232 offset:0\n"
        "ds_read_b64_tr_b16 v[186:187], v232 offset:512\n"
        "ds_read_b128 v[12:15], v252 offset:0\n"
        "v_xor_b32 v233, 0x20, v230\n"
        "ds_read_b64_tr_b16 v[188:189], v233 offset:0\n"
        "ds_read_b64_tr_b16 v[190:191], v233 offset:512\n"
        "v_xor_b32 v234, 0x40, v230\n"
        "ds_read_b64_tr_b16 v[192:193], v234 offset:0\n"
        "ds_read_b64_tr_b16 v[194:195], v234 offset:512\n"
        "v_xor_b32 v235, 0x60, v230\n"
        "ds_read_b64_tr_b16 v[196:197], v235 offset:0\n"
        "ds_read_b64_tr_b16 v[198:199], v235 offset:512\n"
        "ds_read_b64_tr_b16 v[200:201], v232 offset:4096\n"
        "ds_read_b64_tr_b16 v[202:203], v232 offset:4608\n"
        "s_setprio 0\n"
        "s_waitcnt lgkmcnt(6)\n"
        "v_mfma_f32_16x16x32_bf16 v[120:123], v[8:11], v[184:187], 0\n"
        "ds_read_b64_tr_b16 v[204:205], v233 offset:4096\n"
        "ds_read_b128 v[16:19], v251 offset:0\n"
        "v_mfma_f32_16x16x32_bf16 v[124:127], v[12:15], v[184:187], 0\n"
        "ds_read_b64_tr_b16 v[206:207], v233 offset:4608\n"
        "ds_read_b128 v[20:23], v253 offset:0\n"
        "v_mfma_f32_16x16x32_bf16 v[128:131], v[8:11], v[188:191], 0\n"
        "ds_read_b64_tr_b16 v[208:209], v234 offset:4096\n"
        "ds_read_b128 v[24:27], v250 offset:8192\n"
        "v_mfma_f32_16x16x32_bf16 v[132:135], v[12:15], v[188:191], 0\n"
        "ds_read_b64_tr_b16 v[210:211], v234 offset:4608\n"
        "ds_read_b128 v[32:35], v251 offset:8192\n"
        "s_waitcnt lgkmcnt(10)\n"
        "v_mfma_f32_16x16x32_bf16 v[136:139], v[8:11], v[192:195], 0\n"
        "ds_read_b64_tr_b16 v[184:185], v235 offset:4096\n"
        "ds_read_b128 v[40:43], v250 offset:16384\n"
        "v_mfma_f32_16x16x32_bf16 v[140:143], v[12:15], v[192:195], 0\n"
        "ds_read_b64_tr_b16 v[186:187], v235 offset:4608\n"
        "ds_read_b128 v[48:51], v251 offset:16384\n"
        "v_mfma_f32_16x16x32_bf16 v[144:147], v[8:11], v[196:199], 0\n"
        "ds_read_b64_tr_b16 v[188:189], v232 offset:8192\n"
        "s_waitcnt lgkmcnt(8)\n"
        "ds_read_b128 v[56:59], v250 offset:24576\n"
        "v_mfma_f32_16x16x32_bf16 v[148:151], v[12:15], v[196:199], 0\n"
        "ds_read_b64_tr_b16 v[190:191], v232 offset:8704\n"
        "ds_read_b128 v[64:67], v251 offset:24576\n"
        "v_mfma_f32_16x16x32_bf16 v[120:123], v[16:19], v[200:203], v[120:123]\n"
        "ds_read_b64_tr_b16 v[192:193], v233 offset:8192\n"
        "ds_read_b128 v[28:31], v252 offset:8192\n"
        "v_mfma_f32_16x16x32_bf16 v[124:127], v[20:23], v[200:203], v[124:127]\n"
        "ds_read_b64_tr_b16 v[194:195], v233 offset:8704\n"
        "ds_read_b128 v[72:75], v250 offset:32768\n"
        "v_mfma_f32_16x16x32_bf16 v[128:131], v[16:19], v[204:207], v[128:131]\n"
        "s_waitcnt lgkmcnt(8)\n"
        "ds_read_b64_tr_b16 v[196:197], v234 offset:8192\n"
        "ds_read_b128 v[80:83], v251 offset:32768\n"
        "v_mfma_f32_16x16x32_bf16 v[132:135], v[20:23], v[204:207], v[132:135]\n"
        "ds_read_b64_tr_b16 v[198:199], v234 offset:8704\n"
        "ds_read_b128 v[88:91], v250 offset:40960\n"
        "v_mfma_f32_16x16x32_bf16 v[136:139], v[16:19], v[208:211], v[136:139]\n"
        "ds_read_b64_tr_b16 v[200:201], v235 offset:8192\n"
        "ds_read_b128 v[96:99], v251 offset:40960\n"
        "v_mfma_f32_16x16x32_bf16 v[140:143], v[20:23], v[208:211], v[140:143]\n"
        "ds_read_b64_tr_b16 v[202:203], v235 offset:8704\n"
        "s_waitcnt lgkmcnt(8)\n"
        "ds_read_b128 v[104:107], v250 offset:49152\n"
        "v_mfma_f32_16x16x32_bf16 v[144:147], v[16:19], v[184:187], v[144:147]\n"
        "ds_read_b64_tr_b16 v[204:205], v232 offset:12288\n"
        "ds_read_b128 v[112:115], v251 offset:49152\n"
        "v_mfma_f32_16x16x32_bf16 v[148:151], v[20:23], v[184:187], v[148:151]\n"
        "ds_read_b64_tr_b16 v[206:207], v232 offset:12800\n"
        "ds_read_b128 v[36:39], v253 offset:8192\n"
        "s_waitcnt vmcnt(4) lgkmcnt(2)\n"
        "s_barrier\n"
        "v_mfma_f32_16x16x32_bf16 v[120:123], v[24:27], v[188:191], v[120:123]\n"
        "ds_read_b64_tr_b16 v[208:209], v233 offset:12288\n"
        "v_mfma_f32_16x16x32_bf16 v[124:127], v[28:31], v[188:191], v[124:127]\n"
        "ds_read_b64_tr_b16 v[210:211], v233 offset:12800\n"
        "ds_read_b128 v[44:47], v252 offset:16384\n"
        "v_mfma_f32_16x16x32_bf16 v[128:131], v[24:27], v[192:195], v[128:131]\n"
        "ds_read_b64_tr_b16 v[184:185], v234 offset:12288\n"
        "v_mfma_f32_16x16x32_bf16 v[132:135], v[28:31], v[192:195], v[132:135]\n"
        "ds_read_b64_tr_b16 v[186:187], v234 offset:12800\n"
        "ds_read_b128 v[52:55], v253 offset:16384\n"
        "v_mfma_f32_16x16x32_bf16 v[136:139], v[24:27], v[196:199], v[136:139]\n"
        "ds_read_b64_tr_b16 v[188:189], v235 offset:12288\n"
        "v_mfma_f32_16x16x32_bf16 v[140:143], v[28:31], v[196:199], v[140:143]\n"
        "ds_read_b64_tr_b16 v[190:191], v235 offset:12800\n"
        "ds_read_b128 v[60:63], v252 offset:24576\n"
        "v_mfma_f32_16x16x32_bf16 v[144:147], v[24:27], v[200:203], v[144:147]\n"
        "ds_read_b64_tr_b16 v[192:193], v232 offset:16384\n"
        "v_mfma_f32_16x16x32_bf16 v[148:151], v[28:31], v[200:203], v[148:151]\n"
        "ds_read_b64_tr_b16 v[194:195], v232 offset:16896\n"
        "ds_read_b128 v[68:71], v253 offset:24576\n"
        "s_waitcnt lgkmcnt(10)\n"
        "v_mfma_f32_16x16x32_bf16 v[120:123], v[32:35], v[204:207], v[120:123]\n"
        "ds_read_b64_tr_b16 v[196:197], v233 offset:16384\n"
        "v_mfma_f32_16x16x32_bf16 v[124:127], v[36:39], v[204:207], v[124:127]\n"
        "ds_read_b64_tr_b16 v[198:199], v233 offset:16896\n"
        "ds_read_b128 v[76:79], v252 offset:32768\n"
        "v_mfma_f32_16x16x32_bf16 v[128:131], v[32:35], v[208:211], v[128:131]\n"
        "ds_read_b64_tr_b16 v[200:201], v234 offset:16384\n"
        "v_mfma_f32_16x16x32_bf16 v[132:135], v[36:39], v[208:211], v[132:135]\n"
        "ds_read_b64_tr_b16 v[202:203], v234 offset:16896\n"
        "s_waitcnt lgkmcnt(8)\n"
        "ds_read_b128 v[84:87], v253 offset:32768\n"
        "v_mfma_f32_16x16x32_bf16 v[136:139], v[32:35], v[184:187], v[136:139]\n"
        "ds_read_b64_tr_b16 v[204:205], v235 offset:16384\n"
        "v_mfma_f32_16x16x32_bf16 v[140:143], v[36:39], v[184:187], v[140:143]\n"
        "ds_read_b64_tr_b16 v[206:207], v235 offset:16896\n"
        "ds_read_b128 v[92:95], v252 offset:40960\n"
        "v_mfma_f32_16x16x32_bf16 v[144:147], v[32:35], v[188:191], v[144:147]\n"
        "ds_read_b64_tr_b16 v[208:209], v232 offset:20480\n"
        "v_mfma_f32_16x16x32_bf16 v[148:151], v[36:39], v[188:191], v[148:151]\n"
        "ds_read_b64_tr_b16 v[210:211], v232 offset:20992\n"
        "ds_read_b128 v[100:103], v253 offset:40960\n"
        "s_waitcnt vmcnt(2)\n"
        "s_barrier\n"
        "s_waitcnt lgkmcnt(10)\n"
        "v_mfma_f32_16x16x32_bf16 v[120:123], v[40:43], v[192:195], v[120:123]\n"
        "ds_read_b64_tr_b16 v[184:185], v233 offset:20480\n"
        "v_mfma_f32_16x16x32_bf16 v[124:127], v[44:47], v[192:195], v[124:127]\n"
        "ds_read_b64_tr_b16 v[186:187], v233 offset:20992\n"
        "ds_read_b128 v[108:111], v252 offset:49152\n"
        "v_mfma_f32_16x16x32_bf16 v[128:131], v[40:43], v[196:199], v[128:131]\n"
        "ds_read_b64_tr_b16 v[188:189], v234 offset:20480\n"
        "v_mfma_f32_16x16x32_bf16 v[132:135], v[44:47], v[196:199], v[132:135]\n"
        "ds_read_b64_tr_b16 v[190:191], v234 offset:20992\n"
        "s_waitcnt lgkmcnt(8)\n"
        "ds_read_b128 v[116:119], v253 offset:49152\n"
        "v_mfma_f32_16x16x32_bf16 v[136:139], v[40:43], v[200:203], v[136:139]\n"
        "ds_read_b64_tr_b16 v[192:193], v235 offset:20480\n"
        "v_mfma_f32_16x16x32_bf16 v[140:143], v[44:47], v[200:203], v[140:143]\n"
        "ds_read_b64_tr_b16 v[194:195], v235 offset:20992\n"
        "v_mfma_f32_16x16x32_bf16 v[144:147], v[40:43], v[204:207], v[144:147]\n"
        "ds_read_b64_tr_b16 v[196:197], v232 offset:24576\n"
        "v_mfma_f32_16x16x32_bf16 v[148:151], v[44:47], v[204:207], v[148:151]\n"
        "ds_read_b64_tr_b16 v[198:199], v232 offset:25088\n"
        "s_waitcnt lgkmcnt(8)\n"
        "v_mfma_f32_16x16x32_bf16 v[120:123], v[48:51], v[208:211], v[120:123]\n"
        "ds_read_b64_tr_b16 v[200:201], v233 offset:24576\n"
        "v_mfma_f32_16x16x32_bf16 v[124:127], v[52:55], v[208:211], v[124:127]\n"
        "ds_read_b64_tr_b16 v[202:203], v233 offset:25088\n"
        "v_mfma_f32_16x16x32_bf16 v[128:131], v[48:51], v[184:187], v[128:131]\n"
        "ds_read_b64_tr_b16 v[204:205], v234 offset:24576\n"
        "v_mfma_f32_16x16x32_bf16 v[132:135], v[52:55], v[184:187], v[132:135]\n"
        "ds_read_b64_tr_b16 v[206:207], v234 offset:25088\n"
        "s_waitcnt lgkmcnt(6)\n"
        "v_mfma_f32_16x16x32_bf16 v[136:139], v[48:51], v[188:191], v[136:139]\n"
        "ds_read_b64_tr_b16 v[208:209], v235 offset:24576\n"
        "v_mfma_f32_16x16x32_bf16 v[140:143], v[52:55], v[188:191], v[140:143]\n"
        "ds_read_b64_tr_b16 v[210:211], v235 offset:25088\n"
        "v_mfma_f32_16x16x32_bf16 v[144:147], v[48:51], v[192:195], v[144:147]\n"
        "ds_read_b64_tr_b16 v[184:185], v232 offset:28672\n"
        "v_mfma_f32_16x16x32_bf16 v[148:151], v[52:55], v[192:195], v[148:151]\n"
        "ds_read_b64_tr_b16 v[186:187], v232 offset:29184\n"
        "s_waitcnt lgkmcnt(6)\n"
        "v_mfma_f32_16x16x32_bf16 v[120:123], v[56:59], v[196:199], v[120:123]\n"
        "ds_read_b64_tr_b16 v[188:189], v233 offset:28672\n"
        "v_mfma_f32_16x16x32_bf16 v[124:127], v[60:63], v[196:199], v[124:127]\n"
        "ds_read_b64_tr_b16 v[190:191], v233 offset:29184\n"
        "v_mfma_f32_16x16x32_bf16 v[128:131], v[56:59], v[200:203], v[128:131]\n"
        "ds_read_b64_tr_b16 v[192:193], v234 offset:28672\n"
        "v_mfma_f32_16x16x32_bf16 v[132:135], v[60:63], v[200:203], v[132:135]\n"
        "ds_read_b64_tr_b16 v[194:195], v234 offset:29184\n"
        "s_waitcnt lgkmcnt(6)\n"
        "v_mfma_f32_16x16x32_bf16 v[136:139], v[56:59], v[204:207], v[136:139]\n"
        "ds_read_b64_tr_b16 v[196:197], v235 offset:28672\n"
        "v_mfma_f32_16x16x32_bf16 v[140:143], v[60:63], v[204:207], v[140:143]\n"
        "ds_read_b64_tr_b16 v[198:199], v235 offset:29184\n"
        "v_mfma_f32_16x16x32_bf16 v[144:147], v[56:59], v[208:211], v[144:147]\n"
        "ds_read_b64_tr_b16 v[200:201], v232 offset:32768\n"
        "v_mfma_f32_16x16x32_bf16 v[148:151], v[60:63], v[208:211], v[148:151]\n"
        "ds_read_b64_tr_b16 v[202:203], v232 offset:33280\n"
        "s_waitcnt lgkmcnt(6)\n"
        "v_mfma_f32_16x16x32_bf16 v[120:123], v[64:67], v[184:187], v[120:123]\n"
        "ds_read_b64_tr_b16 v[204:205], v233 offset:32768\n"
        "v_mfma_f32_16x16x32_bf16 v[124:127], v[68:71], v[184:187], v[124:127]\n"
        "ds_read_b64_tr_b16 v[206:207], v233 offset:33280\n"
        "v_mfma_f32_16x16x32_bf16 v[128:131], v[64:67], v[188:191], v[128:131]\n"
        "ds_read_b64_tr_b16 v[208:209], v234 offset:32768\n"
        "v_mfma_f32_16x16x32_bf16 v[132:135], v[68:71], v[188:191], v[132:135]\n"
        "ds_read_b64_tr_b16 v[210:211], v234 offset:33280\n"
        "s_waitcnt lgkmcnt(6)\n"
        "v_mfma_f32_16x16x32_bf16 v[136:139], v[64:67], v[192:195], v[136:139]\n"
        "ds_read_b64_tr_b16 v[184:185], v235 offset:32768\n"
        "v_mfma_f32_16x16x32_bf16 v[140:143], v[68:71], v[192:195], v[140:143]\n"
        "ds_read_b64_tr_b16 v[186:187], v235 offset:33280\n"
        "v_mfma_f32_16x16x32_bf16 v[144:147], v[64:67], v[196:199], v[144:147]\n"
        "ds_read_b64_tr_b16 v[188:189], v232 offset:36864\n"
        "v_mfma_f32_16x16x32_bf16 v[148:151], v[68:71], v[196:199], v[148:151]\n"
        "ds_read_b64_tr_b16 v[190:191], v232 offset:37376\n"
        "s_waitcnt vmcnt(1)\n"
        "s_barrier\n"
        "s_waitcnt lgkmcnt(6)\n"
        "v_mfma_f32_16x16x32_bf16 v[120:123], v[72:75], v[200:203], v[120:123]\n"
        "ds_read_b64_tr_b16 v[192:193], v233 offset:36864\n"
        "v_mfma_f32_16x16x32_bf16 v[124:127], v[76:79], v[200:203], v[124:127]\n"
        "ds_read_b64_tr_b16 v[194:195], v233 offset:37376\n"
        "v_mfma_f32_16x16x32_bf16 v[128:131], v[72:75], v[204:207], v[128:131]\n"
        "ds_read_b64_tr_b16 v[196:197], v234 offset:36864\n"
        "v_mfma_f32_16x16x32_bf16 v[132:135], v[76:79], v[204:207], v[132:135]\n"
        "ds_read_b64_tr_b16 v[198:199], v234 offset:37376\n"
        "s_waitcnt lgkmcnt(6)\n"
        "v_mfma_f32_16x16x32_bf16 v[136:139], v[72:75], v[208:211], v[136:139]\n"
        "ds_read_b64_tr_b16 v[200:201], v235 offset:36864\n"
        "v_mfma_f32_16x16x32_bf16 v[140:143], v[76:79], v[208:211], v[140:143]\n"
        "ds_read_b64_tr_b16 v[202:203], v235 offset:37376\n"
        "v_mfma_f32_16x16x32_bf16 v[144:147], v[72:75], v[184:187], v[144:147]\n"
        "ds_read_b64_tr_b16 v[204:205], v232 offset:40960\n"
        "v_mfma_f32_16x16x32_bf16 v[148:151], v[76:79], v[184:187], v[148:151]\n"
        "ds_read_b64_tr_b16 v[206:207], v232 offset:41472\n"
        "s_waitcnt lgkmcnt(6)\n"
        "v_mfma_f32_16x16x32_bf16 v[120:123], v[80:83], v[188:191], v[120:123]\n"
        "ds_read_b64_tr_b16 v[208:209], v233 offset:40960\n"
        "v_mfma_f32_16x16x32_bf16 v[124:127], v[84:87], v[188:191], v[124:127]\n"
        "ds_read_b64_tr_b16 v[210:211], v233 offset:41472\n"
        "v_mfma_f32_16x16x32_bf16 v[128:131], v[80:83], v[192:195], v[128:131]\n"
        "ds_read_b64_tr_b16 v[184:185], v234 offset:40960\n"
        "v_mfma_f32_16x16x32_bf16 v[132:135], v[84:87], v[192:195], v[132:135]\n"
        "ds_read_b64_tr_b16 v[186:187], v234 offset:41472\n"
        "s_waitcnt lgkmcnt(6)\n"
        "v_mfma_f32_16x16x32_bf16 v[136:139], v[80:83], v[196:199], v[136:139]\n"
        "ds_read_b64_tr_b16 v[188:189], v235 offset:40960\n"
        "v_mfma_f32_16x16x32_bf16 v[140:143], v[84:87], v[196:199], v[140:143]\n"
        "ds_read_b64_tr_b16 v[190:191], v235 offset:41472\n"
        "v_mfma_f32_16x16x32_bf16 v[144:147], v[80:83], v[200:203], v[144:147]\n"
        "ds_read_b64_tr_b16 v[192:193], v232 offset:45056\n"
        "v_mfma_f32_16x16x32_bf16 v[148:151], v[84:87], v[200:203], v[148:151]\n"
        "ds_read_b64_tr_b16 v[194:195], v232 offset:45568\n"
        "s_waitcnt lgkmcnt(6)\n"
        "v_mfma_f32_16x16x32_bf16 v[120:123], v[88:91], v[204:207], v[120:123]\n"
        "ds_read_b64_tr_b16 v[196:197], v233 offset:45056\n"
        "v_mfma_f32_16x16x32_bf16 v[124:127], v[92:95], v[204:207], v[124:127]\n"
        "ds_read_b64_tr_b16 v[198:199], v233 offset:45568\n"
        "v_mfma_f32_16x16x32_bf16 v[128:131], v[88:91], v[208:211], v[128:131]\n"
        "ds_read_b64_tr_b16 v[200:201], v234 offset:45056\n"
        "v_mfma_f32_16x16x32_bf16 v[132:135], v[92:95], v[208:211], v[132:135]\n"
        "ds_read_b64_tr_b16 v[202:203], v234 offset:45568\n"
        "s_waitcnt lgkmcnt(6)\n"
        "v_mfma_f32_16x16x32_bf16 v[136:139], v[88:91], v[184:187], v[136:139]\n"
        "ds_read_b64_tr_b16 v[204:205], v235 offset:45056\n"
        "v_mfma_f32_16x16x32_bf16 v[140:143], v[92:95], v[184:187], v[140:143]\n"
        "ds_read_b64_tr_b16 v[206:207], v235 offset:45568\n"
        "v_mfma_f32_16x16x32_bf16 v[144:147], v[88:91], v[188:191], v[144:147]\n"
        "v_xor_b32 v232, 0x10000, v230\n"
        "ds_read_b64_tr_b16 v[208:209], v232 offset:32768\n"
        "v_mfma_f32_16x16x32_bf16 v[148:151], v[92:95], v[188:191], v[148:151]\n"
        "ds_read_b64_tr_b16 v[210:211], v232 offset:33280\n"
        "s_waitcnt lgkmcnt(6)\n"
        "v_mfma_f32_16x16x32_bf16 v[120:123], v[96:99], v[192:195], v[120:123]\n"
        "v_xor_b32 v233, 0x10020, v230\n"
        "ds_read_b64_tr_b16 v[184:185], v233 offset:32768\n"
        "v_mfma_f32_16x16x32_bf16 v[124:127], v[100:103], v[192:195], v[124:127]\n"
        "ds_read_b64_tr_b16 v[186:187], v233 offset:33280\n"
        "v_mfma_f32_16x16x32_bf16 v[128:131], v[96:99], v[196:199], v[128:131]\n"
        "v_xor_b32 v234, 0x10040, v230\n"
        "ds_read_b64_tr_b16 v[188:189], v234 offset:32768\n"
        "v_mfma_f32_16x16x32_bf16 v[132:135], v[100:103], v[196:199], v[132:135]\n"
        "ds_read_b64_tr_b16 v[190:191], v234 offset:33280\n"
        "s_waitcnt lgkmcnt(6)\n"
        "v_mfma_f32_16x16x32_bf16 v[136:139], v[96:99], v[200:203], v[136:139]\n"
        "v_xor_b32 v235, 0x10060, v230\n"
        "ds_read_b64_tr_b16 v[192:193], v235 offset:32768\n"
        "v_mfma_f32_16x16x32_bf16 v[140:143], v[100:103], v[200:203], v[140:143]\n"
        "ds_read_b64_tr_b16 v[194:195], v235 offset:33280\n"
        "v_mfma_f32_16x16x32_bf16 v[144:147], v[96:99], v[204:207], v[144:147]\n"
        "ds_read_b64_tr_b16 v[196:197], v232 offset:36864\n"
        "v_mfma_f32_16x16x32_bf16 v[148:151], v[100:103], v[204:207], v[148:151]\n"
        "ds_read_b64_tr_b16 v[198:199], v232 offset:37376\n"
        "s_barrier\n"
        "s_waitcnt lgkmcnt(6)\n"
        "s_cbranch_execz Lback\n"
        "v_mfma_f32_16x16x32_bf16 v[120:123], v[104:107], v[208:211], v[120:123]\n"
        "ds_read_b64_tr_b16 v[200:201], v233 offset:36864\n"
        "v_mfma_f32_16x16x32_bf16 v[124:127], v[108:111], v[208:211], v[124:127]\n"
        "ds_read_b64_tr_b16 v[202:203], v233 offset:37376\n"
        "v_mfma_f32_16x16x32_bf16 v[128:131], v[104:107], v[184:187], v[128:131]\n"
        "ds_read_b64_tr_b16 v[204:205], v234 offset:36864\n"
        "v_mfma_f32_16x16x32_bf16 v[132:135], v[108:111], v[184:187], v[132:135]\n"
        "ds_read_b64_tr_b16 v[206:207], v234 offset:37376\n"
        "s_waitcnt lgkmcnt(6)\n"
        "v_mfma_f32_16x16x32_bf16 v[136:139], v[104:107], v[188:191], v[136:139]\n"
        "ds_read_b64_tr_b16 v[208:209], v235 offset:36864\n"
        "v_mfma_f32_16x16x32_bf16 v[140:143], v[108:111], v[188:191], v[140:143]\n"
        "ds_read_b64_tr_b16 v[210:211], v235 offset:37376\n"
        "v_mfma_f32_16x16x32_bf16 v[144:147], v[104:107], v[192:195], v[144:147]\n"
        "v_mfma_f32_16x16x32_bf16 v[148:151], v[108:111], v[192:195], v[148:151]\n"
        "s_waitcnt lgkmcnt(4)\n"
        "v_mfma_f32_16x16x32_bf16 v[120:123], v[112:115], v[196:199], v[120:123]\n"
        "v_mfma_f32_16x16x32_bf16 v[124:127], v[116:119], v[196:199], v[124:127]\n"
        "v_mfma_f32_16x16x32_bf16 v[128:131], v[112:115], v[200:203], v[128:131]\n"
        "v_mfma_f32_16x16x32_bf16 v[132:135], v[116:119], v[200:203], v[132:135]\n"
        "s_waitcnt lgkmcnt(0)\n"
        "v_mfma_f32_16x16x32_bf16 v[136:139], v[112:115], v[204:207], v[136:139]\n"
        "v_mfma_f32_16x16x32_bf16 v[140:143], v[116:119], v[204:207], v[140:143]\n"
        "v_mfma_f32_16x16x32_bf16 v[144:147], v[112:115], v[208:211], v[144:147]\n"
        "v_mfma_f32_16x16x32_bf16 v[148:151], v[116:119], v[208:211], v[148:151]\n"
        "s_barrier\n"
        "s_lshl_b32 s67, s72, 2\n"
        "s_add_u32 s67, s67, s97\n"
        "s_lshl_b32 s95, s67, 4\n"
        "s_add_u32 s95, s95, s73\n"
        "s_lshl_b32 s95, s95, 7\n"
        "s_add_u32 s95, s95, 4096\n"
        "s_mul_i32 s67, s67, 28672\n"
        "s_lshl_b32 s68, s15, 10\n"
        "s_add_u32 s67, s67, s68\n"
        "s_add_u32 s67, s67, 13624320\n"
        "v_mul_u32_u24 v216, 7168, v4\n"
        "v_lshl_add_u32 v216, v3, 4, v216\n"
        "v_add_u32 v238, s67, v216\n"
        "s_sub_u32 s15, s15, s73\n"
        "s_lshl_b32 s15, s15, 7\n"
        "s_add_u32 s15, s15, s95\n"
        "v_cvt_pk_bf16_f32 v120, v120, v121\n"
        "v_cvt_pk_bf16_f32 v121, v122, v123\n"
        "v_cvt_pk_bf16_f32 v122, v124, v125\n"
        "v_cvt_pk_bf16_f32 v123, v126, v127\n"
        "buffer_store_dwordx4 v[120:123], v238, s[60:63], 0 offen offset:0 sc1\n"
        "v_cvt_pk_bf16_f32 v128, v128, v129\n"
        "v_cvt_pk_bf16_f32 v129, v130, v131\n"
        "v_cvt_pk_bf16_f32 v130, v132, v133\n"
        "v_cvt_pk_bf16_f32 v131, v134, v135\n"
        "buffer_store_dwordx4 v[128:131], v238, s[60:63], 0 offen offset:256 sc1\n"
        "v_cvt_pk_bf16_f32 v136, v136, v137\n"
        "v_cvt_pk_bf16_f32 v137, v138, v139\n"
        "v_cvt_pk_bf16_f32 v138, v140, v141\n"
        "v_cvt_pk_bf16_f32 v139, v142, v143\n"
        "buffer_store_dwordx4 v[136:139], v238, s[60:63], 0 offen offset:512 sc1\n"
        "v_cvt_pk_bf16_f32 v144, v144, v145\n"
        "v_cvt_pk_bf16_f32 v145, v146, v147\n"
        "v_cvt_pk_bf16_f32 v146, v148, v149\n"
        "v_cvt_pk_bf16_f32 v147, v150, v151\n"
        "buffer_store_dwordx4 v[144:147], v238, s[60:63], 0 offen offset:768 sc1\n"
        "s_add_u32 m0, s65, 140288\n"
        "s_mul_i32 s66, s9, 229376\n"
        "buffer_load_dwordx4 v227, s[44:47], s66 offen lds\n"
        "v_mov_b32 v6, 0xc0135761\n"
        "s_add_u32 m0, s65, 147456\n"
        "s_mul_i32 s66, s10, 229376\n"
        "buffer_load_dwordx4 v226, s[40:43], s66 offen lds\n"
        "v_mov_b32 v7, 0xc0135761\n"
        "s_mov_b32 s88, 1.0\n"
        "s_add_u32 m0, s65, 148480\n"
        "s_mul_i32 s66, s10, 229376\n"
        "buffer_load_dwordx4 v227, s[40:43], s66 offen lds\n"
        "s_mov_b32 s89, 1.0\n"
        "v_readfirstlane_b32 s67, v244\n"
        "s_add_u32 m0, s65, 155648\n"
        "s_mul_i32 s66, s10, 229376\n"
        "buffer_load_dwordx4 v226, s[44:47], s66 offen lds\n"
        "v_readfirstlane_b32 s68, v245\n"
        "s_mul_hi_u32 s71, s67, 0x4c957f2d\n"
        "s_add_u32 m0, s65, 156672\n"
        "s_mul_i32 s66, s10, 229376\n"
        "buffer_load_dwordx4 v227, s[44:47], s66 offen lds\n"
        "s_mul_i32 s69, s67, 0x5851f42d\n"
        "s_add_u32 m0, s65, 16384\n"
        "s_mul_i32 s66, s4, 229376\n"
        "s_add_u32 s66, s66, 128\n"
        "buffer_load_dwordx4 v226, s[40:43], s66 offen lds\n"
        "s_add_u32 s71, s71, s69\n"
        "s_mul_i32 s69, s68, 0x4c957f2d\n"
        "s_add_u32 m0, s65, 17408\n"
        "s_mul_i32 s66, s4, 229376\n"
        "s_add_u32 s66, s66, 128\n"
        "buffer_load_dwordx4 v227, s[40:43], s66 offen lds\n"
        "s_add_u32 s71, s71, s69\n"
        "s_mul_i32 s70, s67, 0x4c957f2d\n"
        "s_add_u32 m0, s65, 24576\n"
        "s_mul_i32 s66, s4, 229376\n"
        "s_add_u32 s66, s66, 128\n"
        "buffer_load_dwordx4 v226, s[44:47], s66 offen lds\n"
        "s_add_u32 s70, s70, 0xf767814f\n"
        "s_addc_u32 s71, s71, 0x14057b7e\n"
        "v_mov_b32 v242, s70\n"
        "s_add_u32 m0, s65, 25600\n"
        "s_mul_i32 s66, s4, 229376\n"
        "s_add_u32 s66, s66, 128\n"
        "buffer_load_dwordx4 v227, s[44:47], s66 offen lds\n"
        "v_mov_b32 v243, s71\n"
        "s_barrier\n"
        "s_waitcnt vmcnt(9)\n"
        "s_add_u32 s66, s15, 0\n"
        "s_mov_b64 exec, 0xffff\n"
        "buffer_store_dwordx2 v[242:243], v2, s[60:63], s66 offen sc1\n"
        "s_mov_b64 exec, -1\n"
        "s_add_u32 m0, s65, 32768\n"
        "s_mul_i32 s66, s5, 229376\n"
        "s_add_u32 s66, s66, 128\n"
        "buffer_load_dwordx4 v226, s[40:43], s66 offen lds\n"
        "s_mov_b32 s80, 0xbdd2d3e8\n"
        "s_mov_b32 s86, s80\n"
        "s_mov_b32 s87, s80\n"
        "s_and_b32 s68, s73, 1\n"
        "s_add_u32 m0, s65, 33792\n"
        "s_mul_i32 s66, s5, 229376\n"
        "s_add_u32 s66, s66, 128\n"
        "buffer_load_dwordx4 v227, s[40:43], s66 offen lds\n"
        "s_mul_i32 s68, s68, 36\n"
        "s_lshr_b32 s67, s73, 1\n"
        "s_add_u32 s67, s67, s68\n"
        "s_lshl_b32 s82, s67, 12\n"
        "s_add_u32 s83, s82, 73728\n"
        "s_add_u32 m0, s65, 40960\n"
        "s_mul_i32 s66, s5, 229376\n"
        "s_add_u32 s66, s66, 128\n"
        "buffer_load_dwordx4 v226, s[44:47], s66 offen lds\n"
        "s_lshr_b32 s67, s97, 1\n"
        "s_lshl1_add_u32 s67, s72, s67\n"
        "s_mul_i32 s67, s67, 294912\n"
        "s_and_b32 s68, s97, 1\n"
        "s_add_u32 m0, s65, 41984\n"
        "s_mul_i32 s66, s5, 229376\n"
        "s_add_u32 s66, s66, 128\n"
        "buffer_load_dwordx4 v227, s[44:47], s66 offen lds\n"
        "s_lshl_b32 s68, s68, 11\n"
        "s_add_u32 s67, s67, s68\n"
        "s_add_u32 s67, s67, 1077248\n"
        "v_lshl_add_u32 v236, v1, 4, s67\n"
        "s_lshl_b32 s69, s75, 7\n"
        "s_add_u32 m0, s65, 98304\n"
        "s_mul_i32 s66, s6, 229376\n"
        "s_add_u32 s66, s66, 128\n"
        "buffer_load_dwordx4 v226, s[40:43], s66 offen lds\n"
        "s_add_u32 s48, s26, s69\n"
        "s_addc_u32 s49, s27, 0\n"
        "s_and_b32 s49, s49, 0xffff\n"
        "s_sub_u32 s50, 1605632, s69\n"
        "s_mov_b32 s51, 0x20000\n"
        "s_add_u32 m0, s65, 99328\n"
        "s_mul_i32 s66, s6, 229376\n"
        "s_add_u32 s66, s66, 128\n"
        "buffer_load_dwordx4 v227, s[40:43], s66 offen lds\n"
        "s_mov_b32 s56, s28\n"
        "s_and_b32 s57, s29, 0xffff\n"
        "s_mov_b32 s58, 1835008\n"
        "s_mov_b32 s59, 0x20000\n"
        "v_lshrrev_b32 v217, 5, v1\n"
        "s_add_u32 m0, s65, 106496\n"
        "s_mul_i32 s66, s6, 229376\n"
        "s_add_u32 s66, s66, 128\n"
        "buffer_load_dwordx4 v226, s[44:47], s66 offen lds\n"
        "v_bfe_u32 v216, v1, 3, 2\n"
        "v_lshl_add_u32 v216, v217, 4, v216\n"
        "s_mov_b32 s69, 896\n"
        "v_mad_u32_u24 v228, v216, s69, v222\n"
        "s_add_u32 m0, s65, 107520\n"
        "s_mul_i32 s66, s6, 229376\n"
        "s_add_u32 s66, s66, 128\n"
        "buffer_load_dwordx4 v227, s[44:47], s66 offen lds\n"
        "v_xor_b32 v229, 64, v228\n"
        "s_lshl_b32 s84, s97, 12\n"
        "s_add_u32 s68, s75, s74\n"
        "s_and_b32 s68, s68, 0\n"
        "s_lshl_b32 s67, s68, 4\n"
        "s_add_u32 m0, s65, 49152\n"
        "s_mul_i32 s66, s7, 229376\n"
        "s_add_u32 s66, s66, 128\n"
        "buffer_load_dwordx4 v226, s[40:43], s66 offen lds\n"
        "s_lshl_b32 s68, s68, 1\n"
        "s_mov_b32 s28, 0x06543210\n"
        "s_mov_b32 s29, 0x00654321\n"
        "s_lshr_b64 s[28:29], s[28:29], s67\n"
        "s_add_u32 m0, s65, 50176\n"
        "s_mul_i32 s66, s7, 229376\n"
        "s_add_u32 s66, s66, 128\n"
        "buffer_load_dwordx4 v227, s[40:43], s66 offen lds\n"
        "s_lshr_b32 s67, s97, 1\n"
        "s_mul_i32 s67, s67, 57344\n"
        "s_mul_i32 s68, s97, 28672\n"
        "s_add_u32 s67, s67, s68\n"
        "s_bfe_u32 s20, s28, 0x40010\n"
        "s_add_u32 m0, s65, 57344\n"
        "s_mul_i32 s66, s7, 229376\n"
        "s_add_u32 s66, s66, 128\n"
        "buffer_load_dwordx4 v226, s[44:47], s66 offen lds\n"
        "s_mul_i32 s20, s20, 229376\n"
        "s_add_u32 s20, s20, s67\n"
        "s_bfe_u32 s21, s28, 0x40014\n"
        "s_mul_i32 s21, s21, 229376\n"
        "s_add_u32 s21, s21, s67\n"
        "buffer_load_dwordx4 v[246:249], v239, s[32:35], 0 offen\n"
        "s_waitcnt vmcnt(17)\n"
        "s_barrier\n"
        "s_add_u32 m0, s65, 58368\n"
        "s_mul_i32 s66, s7, 229376\n"
        "s_add_u32 s66, s66, 128\n"
        "buffer_load_dwordx4 v227, s[44:47], s66 offen lds\n"
        "s_bfe_u32 s22, s28, 0x40018\n"
        "s_mul_i32 s22, s22, 229376\n"
        "s_add_u32 s22, s22, s67\n"
        "s_add_u32 m0, s65, 65536\n"
        "s_mul_i32 s66, s8, 229376\n"
        "s_add_u32 s66, s66, 128\n"
        "buffer_load_dwordx4 v226, s[40:43], s66 offen lds\n"
        "s_add_u32 m0, s65, 66560\n"
        "s_mul_i32 s66, s8, 229376\n"
        "s_add_u32 s66, s66, 128\n"
        "buffer_load_dwordx4 v227, s[40:43], s66 offen lds\n"
        "s_add_u32 m0, s65, 73728\n"
        "s_mul_i32 s66, s8, 229376\n"
        "s_add_u32 s66, s66, 128\n"
        "buffer_load_dwordx4 v226, s[44:47], s66 offen lds\n"
        "s_add_u32 m0, s65, 74752\n"
        "s_mul_i32 s66, s8, 229376\n"
        "s_add_u32 s66, s66, 128\n"
        "buffer_load_dwordx4 v227, s[44:47], s66 offen lds\n"
        "s_add_u32 m0, s65, 81920\n"
        "s_mul_i32 s66, s9, 229376\n"
        "s_add_u32 s66, s66, 128\n"
        "buffer_load_dwordx4 v226, s[40:43], s66 offen lds\n"
        "s_add_u32 m0, s65, 82944\n"
        "s_mul_i32 s66, s9, 229376\n"
        "s_add_u32 s66, s66, 128\n"
        "buffer_load_dwordx4 v227, s[40:43], s66 offen lds\n"
        "s_add_u32 m0, s65, 90112\n"
        "s_mul_i32 s66, s9, 229376\n"
        "s_add_u32 s66, s66, 128\n"
        "buffer_load_dwordx4 v226, s[44:47], s66 offen lds\n"
        "s_add_u32 m0, s65, 91136\n"
        "s_mul_i32 s66, s9, 229376\n"
        "s_add_u32 s66, s66, 128\n"
        "buffer_load_dwordx4 v227, s[44:47], s66 offen lds\n"
        "s_add_u32 m0, s65, 0\n"
        "s_mul_i32 s66, s10, 229376\n"
        "s_add_u32 s66, s66, 128\n"
        "buffer_load_dwordx4 v226, s[40:43], s66 offen lds\n"
        "s_add_u32 m0, s65, 1024\n"
        "s_mul_i32 s66, s10, 229376\n"
        "s_add_u32 s66, s66, 128\n"
        "buffer_load_dwordx4 v227, s[40:43], s66 offen lds\n"
        "s_add_u32 m0, s65, 8192\n"
        "s_mul_i32 s66, s10, 229376\n"
        "s_add_u32 s66, s66, 128\n"
        "buffer_load_dwordx4 v226, s[44:47], s66 offen lds\n"
        "s_waitcnt vmcnt(25)\n"
        "s_barrier\n"
        "s_add_u32 m0, s65, 9216\n"
        "s_mul_i32 s66, s10, 229376\n"
        "s_add_u32 s66, s66, 128\n"
        "buffer_load_dwordx4 v227, s[44:47], s66 offen lds\n"
        "s_add_u32 m0, s84, 114688\n"
        "s_add_u32 s66, s20, 0\n"
        "buffer_load_dwordx4 v228, s[48:51], s66 offen lds\n"
        "s_add_u32 m0, s84, 115712\n"
        "s_add_u32 s66, s20, 3584\n"
        "buffer_load_dwordx4 v229, s[48:51], s66 offen lds\n"
        "s_add_u32 m0, s84, 116736\n"
        "s_add_u32 s66, s20, 7168\n"
        "buffer_load_dwordx4 v228, s[48:51], s66 offen lds\n"
        "s_add_u32 m0, s84, 117760\n"
        "s_add_u32 s66, s20, 10752\n"
        "buffer_load_dwordx4 v229, s[48:51], s66 offen lds\n"
        "s_add_u32 m0, s84, 131072\n"
        "s_add_u32 s66, s21, 0\n"
        "buffer_load_dwordx4 v228, s[48:51], s66 offen lds\n"
        "s_barrier\n"
        "s_add_u32 m0, s84, 132096\n"
        "s_add_u32 s66, s21, 3584\n"
        "buffer_load_dwordx4 v229, s[48:51], s66 offen lds\n"
        "s_add_u32 m0, s84, 133120\n"
        "s_add_u32 s66, s21, 7168\n"
        "buffer_load_dwordx4 v228, s[48:51], s66 offen lds\n"
        "s_add_u32 m0, s84, 134144\n"
        "s_add_u32 s66, s21, 10752\n"
        "buffer_load_dwordx4 v229, s[48:51], s66 offen lds\n"
        "s_add_u32 m0, s84, 147456\n"
        "s_add_u32 s66, s22, 0\n"
        "buffer_load_dwordx4 v228, s[48:51], s66 offen lds\n"
        "s_add_u32 m0, s84, 148480\n"
        "s_add_u32 s66, s22, 3584\n"
        "buffer_load_dwordx4 v229, s[48:51], s66 offen lds\n"
        "s_add_u32 m0, s84, 149504\n"
        "s_add_u32 s66, s22, 7168\n"
        "buffer_load_dwordx4 v228, s[48:51], s66 offen lds\n"
        "s_mov_b32 s66, 0x8059764\n"
        "s_lshl_b32 s79, s97, 3\n"
        "s_add_u32 s79, s79, 0x40004\n"
        "s_bfe_u32 s66, s66, s79\n"
        "s_cbranch_scc0 Lvb9\n"
        "s_lshl_b32 s66, s66, 12\n"
        "s_mov_b64 exec, 0\n"
        "s_getpc_b64 s[90:91]\n"
        "Lvp9:\n"
        "s_sub_u32 s66, s66, Lvp9-Lp0\n"
        "s_ashr_i32 s79, s66, 31\n"
        "s_add_u32 s90, s90, s66\n"
        "s_addc_u32 s91, s91, s79\n"
        "s_swappc_b64 vcc, s[90:91]\n"
        "s_mov_b64 exec, -1\n"
        "Lvb9:\n"
        "v_xor_b32 v232, 0x0, v230\n"
        "ds_read_b64_tr_b16 v[184:185], v232 offset:16384\n"
        "ds_read_b64_tr_b16 v[186:187], v232 offset:16896\n"
        "v_xor_b32 v233, 0x20, v230\n"
        "ds_read_b64_tr_b16 v[188:189], v233 offset:16384\n"
        "ds_read_b64_tr_b16 v[190:191], v233 offset:16896\n"
        "v_xor_b32 v234, 0x40, v230\n"
        "ds_read_b64_tr_b16 v[192:193], v234 offset:16384\n"
        "ds_read_b64_tr_b16 v[194:195], v234 offset:16896\n"
        "v_xor_b32 v235, 0x60, v230\n"
        "ds_read_b64_tr_b16 v[196:197], v235 offset:16384\n"
        "ds_read_b64_tr_b16 v[198:199], v235 offset:16896\n"
        "ds_read_b64_tr_b16 v[200:201], v232 offset:24576\n"
        "ds_read_b64_tr_b16 v[202:203], v232 offset:25088\n"
        "s_waitcnt vmcnt(3)\n"
        "s_barrier\n"
        "s_waitcnt lgkmcnt(6)\n"
        "v_mfma_f32_16x16x32_bf16 v[120:123], v[184:187], v[8:11], 0\n"
        "ds_read_b64_tr_b16 v[204:205], v233 offset:24576\n"
        "v_mfma_f32_16x16x32_bf16 v[128:131], v[184:187], v[12:15], 0\n"
        "ds_read_b64_tr_b16 v[206:207], v233 offset:25088\n"
        "v_mfma_f32_16x16x32_bf16 v[124:127], v[188:191], v[8:11], 0\n"
        "ds_read_b64_tr_b16 v[208:209], v234 offset:24576\n"
        "v_mfma_f32_16x16x32_bf16 v[132:135], v[188:191], v[12:15], 0\n"
        "ds_read_b64_tr_b16 v[210:211], v234 offset:25088\n"
        "s_waitcnt lgkmcnt(6)\n"
        "v_mfma_f32_16x16x32_bf16 v[136:139], v[192:195], v[8:11], 0\n"
        "ds_read_b64_tr_b16 v[184:185], v235 offset:24576\n"
        "v_mfma_f32_16x16x32_bf16 v[144:147], v[192:195], v[12:15], 0\n"
        "ds_read_b64_tr_b16 v[186:187], v235 offset:25088\n"
        "v_mfma_f32_16x16x32_bf16 v[140:143], v[196:199], v[8:11], 0\n"
        "ds_read_b64_tr_b16 v[188:189], v232 offset:20480\n"
        "v_mfma_f32_16x16x32_bf16 v[148:151], v[196:199], v[12:15], 0\n"
        "ds_read_b64_tr_b16 v[190:191], v232 offset:20992\n"
        "s_waitcnt lgkmcnt(6)\n"
        "v_mfma_f32_16x16x32_bf16 v[152:155], v[200:203], v[8:11], 0\n"
        "ds_read_b64_tr_b16 v[192:193], v233 offset:20480\n"
        "v_mfma_f32_16x16x32_bf16 v[160:163], v[200:203], v[12:15], 0\n"
        "ds_read_b64_tr_b16 v[194:195], v233 offset:20992\n"
        "v_mfma_f32_16x16x32_bf16 v[156:159], v[204:207], v[8:11], 0\n"
        "ds_read_b64_tr_b16 v[196:197], v234 offset:20480\n"
        "v_mfma_f32_16x16x32_bf16 v[164:167], v[204:207], v[12:15], 0\n"
        "ds_read_b64_tr_b16 v[198:199], v234 offset:20992\n"
        "s_waitcnt lgkmcnt(6)\n"
        "v_mfma_f32_16x16x32_bf16 v[168:171], v[208:211], v[8:11], 0\n"
        "ds_read_b64_tr_b16 v[200:201], v235 offset:20480\n"
        "v_mfma_f32_16x16x32_bf16 v[176:179], v[208:211], v[12:15], 0\n"
        "ds_read_b64_tr_b16 v[202:203], v235 offset:20992\n"
        "v_mfma_f32_16x16x32_bf16 v[172:175], v[184:187], v[8:11], 0\n"
        "ds_read_b64_tr_b16 v[204:205], v232 offset:28672\n"
        "v_mfma_f32_16x16x32_bf16 v[180:183], v[184:187], v[12:15], 0\n"
        "ds_read_b64_tr_b16 v[206:207], v232 offset:29184\n"
        "s_waitcnt lgkmcnt(6)\n"
        "v_mfma_f32_16x16x32_bf16 v[120:123], v[188:191], v[16:19], v[120:123]\n"
        "ds_read_b64_tr_b16 v[208:209], v233 offset:28672\n"
        "v_mfma_f32_16x16x32_bf16 v[128:131], v[188:191], v[20:23], v[128:131]\n"
        "ds_read_b64_tr_b16 v[210:211], v233 offset:29184\n"
        "v_mfma_f32_16x16x32_bf16 v[124:127], v[192:195], v[16:19], v[124:127]\n"
        "ds_read_b64_tr_b16 v[184:185], v234 offset:28672\n"
        "v_mfma_f32_16x16x32_bf16 v[132:135], v[192:195], v[20:23], v[132:135]\n"
        "ds_read_b64_tr_b16 v[186:187], v234 offset:29184\n"
        "s_waitcnt lgkmcnt(6)\n"
        "v_mfma_f32_16x16x32_bf16 v[136:139], v[196:199], v[16:19], v[136:139]\n"
        "ds_read_b64_tr_b16 v[188:189], v235 offset:28672\n"
        "v_mfma_f32_16x16x32_bf16 v[144:147], v[196:199], v[20:23], v[144:147]\n"
        "ds_read_b64_tr_b16 v[190:191], v235 offset:29184\n"
        "v_mfma_f32_16x16x32_bf16 v[140:143], v[200:203], v[16:19], v[140:143]\n"
        "ds_read_b64_tr_b16 v[192:193], v232 offset:32768\n"
        "v_mfma_f32_16x16x32_bf16 v[148:151], v[200:203], v[20:23], v[148:151]\n"
        "ds_read_b64_tr_b16 v[194:195], v232 offset:33280\n"
        "s_waitcnt lgkmcnt(6)\n"
        "v_mfma_f32_16x16x32_bf16 v[152:155], v[204:207], v[16:19], v[152:155]\n"
        "ds_read_b64_tr_b16 v[196:197], v233 offset:32768\n"
        "v_mfma_f32_16x16x32_bf16 v[160:163], v[204:207], v[20:23], v[160:163]\n"
        "ds_read_b64_tr_b16 v[198:199], v233 offset:33280\n"
        "v_mfma_f32_16x16x32_bf16 v[156:159], v[208:211], v[16:19], v[156:159]\n"
        "ds_read_b64_tr_b16 v[200:201], v234 offset:32768\n"
        "v_mfma_f32_16x16x32_bf16 v[164:167], v[208:211], v[20:23], v[164:167]\n"
        "ds_read_b64_tr_b16 v[202:203], v234 offset:33280\n"
        "s_waitcnt lgkmcnt(6)\n"
        "v_mfma_f32_16x16x32_bf16 v[168:171], v[184:187], v[16:19], v[168:171]\n"
        "ds_read_b64_tr_b16 v[204:205], v235 offset:32768\n"
        "v_mfma_f32_16x16x32_bf16 v[176:179], v[184:187], v[20:23], v[176:179]\n"
        "ds_read_b64_tr_b16 v[206:207], v235 offset:33280\n"
        "v_mfma_f32_16x16x32_bf16 v[172:175], v[188:191], v[16:19], v[172:175]\n"
        "ds_read_b64_tr_b16 v[208:209], v232 offset:40960\n"
        "v_mfma_f32_16x16x32_bf16 v[180:183], v[188:191], v[20:23], v[180:183]\n"
        "ds_read_b64_tr_b16 v[210:211], v232 offset:41472\n"
        "s_waitcnt lgkmcnt(6)\n"
        "v_mfma_f32_16x16x32_bf16 v[120:123], v[192:195], v[24:27], v[120:123]\n"
        "ds_read_b64_tr_b16 v[184:185], v233 offset:40960\n"
        "v_mfma_f32_16x16x32_bf16 v[128:131], v[192:195], v[28:31], v[128:131]\n"
        "ds_read_b64_tr_b16 v[186:187], v233 offset:41472\n"
        "v_mfma_f32_16x16x32_bf16 v[124:127], v[196:199], v[24:27], v[124:127]\n"
        "ds_read_b64_tr_b16 v[188:189], v234 offset:40960\n"
        "v_mfma_f32_16x16x32_bf16 v[132:135], v[196:199], v[28:31], v[132:135]\n"
        "ds_read_b64_tr_b16 v[190:191], v234 offset:41472\n"
        "s_waitcnt lgkmcnt(6)\n"
        "v_mfma_f32_16x16x32_bf16 v[136:139], v[200:203], v[24:27], v[136:139]\n"
        "ds_read_b64_tr_b16 v[192:193], v235 offset:40960\n"
        "v_mfma_f32_16x16x32_bf16 v[144:147], v[200:203], v[28:31], v[144:147]\n"
        "ds_read_b64_tr_b16 v[194:195], v235 offset:41472\n"
        "v_mfma_f32_16x16x32_bf16 v[140:143], v[204:207], v[24:27], v[140:143]\n"
        "ds_read_b64_tr_b16 v[196:197], v232 offset:36864\n"
        "v_mfma_f32_16x16x32_bf16 v[148:151], v[204:207], v[28:31], v[148:151]\n"
        "ds_read_b64_tr_b16 v[198:199], v232 offset:37376\n"
        "s_waitcnt lgkmcnt(6)\n"
        "v_mfma_f32_16x16x32_bf16 v[152:155], v[208:211], v[24:27], v[152:155]\n"
        "ds_read_b64_tr_b16 v[200:201], v233 offset:36864\n"
        "v_mfma_f32_16x16x32_bf16 v[160:163], v[208:211], v[28:31], v[160:163]\n"
        "ds_read_b64_tr_b16 v[202:203], v233 offset:37376\n"
        "v_mfma_f32_16x16x32_bf16 v[156:159], v[184:187], v[24:27], v[156:159]\n"
        "ds_read_b64_tr_b16 v[204:205], v234 offset:36864\n"
        "v_mfma_f32_16x16x32_bf16 v[164:167], v[184:187], v[28:31], v[164:167]\n"
        "ds_read_b64_tr_b16 v[206:207], v234 offset:37376\n"
        "s_waitcnt lgkmcnt(6)\n"
        "v_mfma_f32_16x16x32_bf16 v[168:171], v[188:191], v[24:27], v[168:171]\n"
        "ds_read_b64_tr_b16 v[208:209], v235 offset:36864\n"
        "v_mfma_f32_16x16x32_bf16 v[176:179], v[188:191], v[28:31], v[176:179]\n"
        "ds_read_b64_tr_b16 v[210:211], v235 offset:37376\n"
        "v_mfma_f32_16x16x32_bf16 v[172:175], v[192:195], v[24:27], v[172:175]\n"
        "ds_read_b64_tr_b16 v[184:185], v232 offset:45056\n"
        "v_mfma_f32_16x16x32_bf16 v[180:183], v[192:195], v[28:31], v[180:183]\n"
        "ds_read_b64_tr_b16 v[186:187], v232 offset:45568\n"
        "s_waitcnt lgkmcnt(6)\n"
        "v_mfma_f32_16x16x32_bf16 v[120:123], v[196:199], v[32:35], v[120:123]\n"
        "ds_read_b64_tr_b16 v[188:189], v233 offset:45056\n"
        "v_mfma_f32_16x16x32_bf16 v[128:131], v[196:199], v[36:39], v[128:131]\n"
        "ds_read_b64_tr_b16 v[190:191], v233 offset:45568\n"
        "v_mfma_f32_16x16x32_bf16 v[124:127], v[200:203], v[32:35], v[124:127]\n"
        "ds_read_b64_tr_b16 v[192:193], v234 offset:45056\n"
        "v_mfma_f32_16x16x32_bf16 v[132:135], v[200:203], v[36:39], v[132:135]\n"
        "ds_read_b64_tr_b16 v[194:195], v234 offset:45568\n"
        "s_waitcnt lgkmcnt(6)\n"
        "v_mfma_f32_16x16x32_bf16 v[136:139], v[204:207], v[32:35], v[136:139]\n"
        "ds_read_b64_tr_b16 v[196:197], v235 offset:45056\n"
        "v_mfma_f32_16x16x32_bf16 v[144:147], v[204:207], v[36:39], v[144:147]\n"
        "ds_read_b64_tr_b16 v[198:199], v235 offset:45568\n"
        "v_mfma_f32_16x16x32_bf16 v[140:143], v[208:211], v[32:35], v[140:143]\n"
        "v_xor_b32 v232, 0x10000, v230\n"
        "ds_read_b64_tr_b16 v[200:201], v232 offset:32768\n"
        "v_mfma_f32_16x16x32_bf16 v[148:151], v[208:211], v[36:39], v[148:151]\n"
        "ds_read_b64_tr_b16 v[202:203], v232 offset:33280\n"
        "s_waitcnt lgkmcnt(6)\n"
        "v_mfma_f32_16x16x32_bf16 v[152:155], v[184:187], v[32:35], v[152:155]\n"
        "v_xor_b32 v233, 0x10020, v230\n"
        "ds_read_b64_tr_b16 v[204:205], v233 offset:32768\n"
        "v_mfma_f32_16x16x32_bf16 v[160:163], v[184:187], v[36:39], v[160:163]\n"
        "ds_read_b64_tr_b16 v[206:207], v233 offset:33280\n"
        "v_mfma_f32_16x16x32_bf16 v[156:159], v[188:191], v[32:35], v[156:159]\n"
        "v_xor_b32 v234, 0x10040, v230\n"
        "ds_read_b64_tr_b16 v[208:209], v234 offset:32768\n"
        "v_mfma_f32_16x16x32_bf16 v[164:167], v[188:191], v[36:39], v[164:167]\n"
        "ds_read_b64_tr_b16 v[210:211], v234 offset:33280\n"
        "s_waitcnt lgkmcnt(6)\n"
        "v_mfma_f32_16x16x32_bf16 v[168:171], v[192:195], v[32:35], v[168:171]\n"
        "v_xor_b32 v235, 0x10060, v230\n"
        "ds_read_b64_tr_b16 v[184:185], v235 offset:32768\n"
        "v_mfma_f32_16x16x32_bf16 v[176:179], v[192:195], v[36:39], v[176:179]\n"
        "ds_read_b64_tr_b16 v[186:187], v235 offset:33280\n"
        "v_mfma_f32_16x16x32_bf16 v[172:175], v[196:199], v[32:35], v[172:175]\n"
        "ds_read_b64_tr_b16 v[188:189], v232 offset:40960\n"
        "v_mfma_f32_16x16x32_bf16 v[180:183], v[196:199], v[36:39], v[180:183]\n"
        "ds_read_b64_tr_b16 v[190:191], v232 offset:41472\n"
        "s_waitcnt vmcnt(0)\n"
        "s_barrier\n"
        "s_waitcnt lgkmcnt(6)\n"
        "v_mfma_f32_16x16x32_bf16 v[120:123], v[200:203], v[40:43], v[120:123]\n"
        "ds_read_b64_tr_b16 v[192:193], v233 offset:40960\n"
        "v_mfma_f32_16x16x32_bf16 v[128:131], v[200:203], v[44:47], v[128:131]\n"
        "ds_read_b64_tr_b16 v[194:195], v233 offset:41472\n"
        "v_mfma_f32_16x16x32_bf16 v[124:127], v[204:207], v[40:43], v[124:127]\n"
        "ds_read_b64_tr_b16 v[196:197], v234 offset:40960\n"
        "v_mfma_f32_16x16x32_bf16 v[132:135], v[204:207], v[44:47], v[132:135]\n"
        "ds_read_b64_tr_b16 v[198:199], v234 offset:41472\n"
        "s_waitcnt lgkmcnt(6)\n"
        "v_mfma_f32_16x16x32_bf16 v[136:139], v[208:211], v[40:43], v[136:139]\n"
        "ds_read_b64_tr_b16 v[200:201], v235 offset:40960\n"
        "v_mfma_f32_16x16x32_bf16 v[144:147], v[208:211], v[44:47], v[144:147]\n"
        "ds_read_b64_tr_b16 v[202:203], v235 offset:41472\n"
        "v_mfma_f32_16x16x32_bf16 v[140:143], v[184:187], v[40:43], v[140:143]\n"
        "ds_read_b64_tr_b16 v[204:205], v232 offset:36864\n"
        "v_mfma_f32_16x16x32_bf16 v[148:151], v[184:187], v[44:47], v[148:151]\n"
        "ds_read_b64_tr_b16 v[206:207], v232 offset:37376\n"
        "s_waitcnt lgkmcnt(6)\n"
        "v_mfma_f32_16x16x32_bf16 v[152:155], v[188:191], v[40:43], v[152:155]\n"
        "ds_read_b64_tr_b16 v[208:209], v233 offset:36864\n"
        "v_mfma_f32_16x16x32_bf16 v[160:163], v[188:191], v[44:47], v[160:163]\n"
        "ds_read_b64_tr_b16 v[210:211], v233 offset:37376\n"
        "v_mfma_f32_16x16x32_bf16 v[156:159], v[192:195], v[40:43], v[156:159]\n"
        "ds_read_b64_tr_b16 v[184:185], v234 offset:36864\n"
        "v_mfma_f32_16x16x32_bf16 v[164:167], v[192:195], v[44:47], v[164:167]\n"
        "ds_read_b64_tr_b16 v[186:187], v234 offset:37376\n"
        "s_waitcnt lgkmcnt(6)\n"
        "v_mfma_f32_16x16x32_bf16 v[168:171], v[196:199], v[40:43], v[168:171]\n"
        "ds_read_b64_tr_b16 v[188:189], v235 offset:36864\n"
        "v_mfma_f32_16x16x32_bf16 v[176:179], v[196:199], v[44:47], v[176:179]\n"
        "ds_read_b64_tr_b16 v[190:191], v235 offset:37376\n"
        "v_mfma_f32_16x16x32_bf16 v[172:175], v[200:203], v[40:43], v[172:175]\n"
        "ds_read_b64_tr_b16 v[192:193], v232 offset:45056\n"
        "v_mfma_f32_16x16x32_bf16 v[180:183], v[200:203], v[44:47], v[180:183]\n"
        "ds_read_b64_tr_b16 v[194:195], v232 offset:45568\n"
        "s_waitcnt lgkmcnt(6)\n"
        "v_mfma_f32_16x16x32_bf16 v[120:123], v[204:207], v[48:51], v[120:123]\n"
        "ds_read_b64_tr_b16 v[196:197], v233 offset:45056\n"
        "v_mfma_f32_16x16x32_bf16 v[128:131], v[204:207], v[52:55], v[128:131]\n"
        "ds_read_b64_tr_b16 v[198:199], v233 offset:45568\n"
        "v_mfma_f32_16x16x32_bf16 v[124:127], v[208:211], v[48:51], v[124:127]\n"
        "ds_read_b64_tr_b16 v[200:201], v234 offset:45056\n"
        "v_mfma_f32_16x16x32_bf16 v[132:135], v[208:211], v[52:55], v[132:135]\n"
        "ds_read_b64_tr_b16 v[202:203], v234 offset:45568\n"
        "s_waitcnt lgkmcnt(6)\n"
        "v_mfma_f32_16x16x32_bf16 v[136:139], v[184:187], v[48:51], v[136:139]\n"
        "ds_read_b64_tr_b16 v[204:205], v235 offset:45056\n"
        "v_mfma_f32_16x16x32_bf16 v[144:147], v[184:187], v[52:55], v[144:147]\n"
        "ds_read_b64_tr_b16 v[206:207], v235 offset:45568\n"
        "v_mfma_f32_16x16x32_bf16 v[140:143], v[188:191], v[48:51], v[140:143]\n"
        "v_xor_b32 v232, 0x0, v230\n"
        "ds_read_b64_tr_b16 v[208:209], v232 offset:49152\n"
        "v_mfma_f32_16x16x32_bf16 v[148:151], v[188:191], v[52:55], v[148:151]\n"
        "ds_read_b64_tr_b16 v[210:211], v232 offset:49664\n"
        "s_waitcnt lgkmcnt(6)\n"
        "v_mfma_f32_16x16x32_bf16 v[152:155], v[192:195], v[48:51], v[152:155]\n"
        "v_xor_b32 v233, 0x20, v230\n"
        "ds_read_b64_tr_b16 v[184:185], v233 offset:49152\n"
        "v_mfma_f32_16x16x32_bf16 v[160:163], v[192:195], v[52:55], v[160:163]\n"
        "ds_read_b64_tr_b16 v[186:187], v233 offset:49664\n"
        "v_mfma_f32_16x16x32_bf16 v[156:159], v[196:199], v[48:51], v[156:159]\n"
        "v_xor_b32 v234, 0x40, v230\n"
        "ds_read_b64_tr_b16 v[188:189], v234 offset:49152\n"
        "v_mfma_f32_16x16x32_bf16 v[164:167], v[196:199], v[52:55], v[164:167]\n"
        "ds_read_b64_tr_b16 v[190:191], v234 offset:49664\n"
        "s_waitcnt lgkmcnt(6)\n"
        "v_mfma_f32_16x16x32_bf16 v[168:171], v[200:203], v[48:51], v[168:171]\n"
        "v_xor_b32 v235, 0x60, v230\n"
        "ds_read_b64_tr_b16 v[192:193], v235 offset:49152\n"
        "v_mfma_f32_16x16x32_bf16 v[176:179], v[200:203], v[52:55], v[176:179]\n"
        "ds_read_b64_tr_b16 v[194:195], v235 offset:49664\n"
        "v_mfma_f32_16x16x32_bf16 v[172:175], v[204:207], v[48:51], v[172:175]\n"
        "ds_read_b64_tr_b16 v[196:197], v232 offset:57344\n"
        "v_mfma_f32_16x16x32_bf16 v[180:183], v[204:207], v[52:55], v[180:183]\n"
        "ds_read_b64_tr_b16 v[198:199], v232 offset:57856\n"
        "s_barrier\n"
        "s_waitcnt lgkmcnt(6)\n"
        "v_mfma_f32_16x16x32_bf16 v[120:123], v[208:211], v[56:59], v[120:123]\n"
        "ds_read_b64_tr_b16 v[200:201], v233 offset:57344\n"
        "v_mfma_f32_16x16x32_bf16 v[128:131], v[208:211], v[60:63], v[128:131]\n"
        "ds_read_b64_tr_b16 v[202:203], v233 offset:57856\n"
        "v_mfma_f32_16x16x32_bf16 v[124:127], v[184:187], v[56:59], v[124:127]\n"
        "s_nop 0\n"
        "s_cbranch_execz Lback\n"
        "ds_read_b64_tr_b16 v[204:205], v234 offset:57344\n"
        "v_mfma_f32_16x16x32_bf16 v[132:135], v[184:187], v[60:63], v[132:135]\n"
        "ds_read_b64_tr_b16 v[206:207], v234 offset:57856\n"
        "s_waitcnt lgkmcnt(6)\n"
        "v_mfma_f32_16x16x32_bf16 v[136:139], v[188:191], v[56:59], v[136:139]\n"
        "ds_read_b64_tr_b16 v[208:209], v235 offset:57344\n"
        "v_mfma_f32_16x16x32_bf16 v[144:147], v[188:191], v[60:63], v[144:147]\n"
        "ds_read_b64_tr_b16 v[210:211], v235 offset:57856\n"
        "v_mfma_f32_16x16x32_bf16 v[140:143], v[192:195], v[56:59], v[140:143]\n"
        "ds_read_b64_tr_b16 v[184:185], v232 offset:53248\n"
        "v_mfma_f32_16x16x32_bf16 v[148:151], v[192:195], v[60:63], v[148:151]\n"
        "ds_read_b64_tr_b16 v[186:187], v232 offset:53760\n"
        "s_waitcnt lgkmcnt(6)\n"
        "v_mfma_f32_16x16x32_bf16 v[152:155], v[196:199], v[56:59], v[152:155]\n"
        "ds_read_b64_tr_b16 v[188:189], v233 offset:53248\n"
        "v_mfma_f32_16x16x32_bf16 v[160:163], v[196:199], v[60:63], v[160:163]\n"
        "ds_read_b64_tr_b16 v[190:191], v233 offset:53760\n"
        "v_mfma_f32_16x16x32_bf16 v[156:159], v[200:203], v[56:59], v[156:159]\n"
        "ds_read_b64_tr_b16 v[192:193], v234 offset:53248\n"
        "v_mfma_f32_16x16x32_bf16 v[164:167], v[200:203], v[60:63], v[164:167]\n"
        "ds_read_b64_tr_b16 v[194:195], v234 offset:53760\n"
        "s_waitcnt lgkmcnt(6)\n"
        "v_mfma_f32_16x16x32_bf16 v[168:171], v[204:207], v[56:59], v[168:171]\n"
        "ds_read_b64_tr_b16 v[196:197], v235 offset:53248\n"
        "v_mfma_f32_16x16x32_bf16 v[176:179], v[204:207], v[60:63], v[176:179]\n"
        "ds_read_b64_tr_b16 v[198:199], v235 offset:53760\n"
        "v_mfma_f32_16x16x32_bf16 v[172:175], v[208:211], v[56:59], v[172:175]\n"
        "ds_read_b64_tr_b16 v[200:201], v232 offset:61440\n"
        "v_mfma_f32_16x16x32_bf16 v[180:183], v[208:211], v[60:63], v[180:183]\n"
        "ds_read_b64_tr_b16 v[202:203], v232 offset:61952\n"
        "s_waitcnt lgkmcnt(6)\n"
        "v_mfma_f32_16x16x32_bf16 v[120:123], v[184:187], v[64:67], v[120:123]\n"
        "ds_read_b64_tr_b16 v[204:205], v233 offset:61440\n"
        "v_mfma_f32_16x16x32_bf16 v[128:131], v[184:187], v[68:71], v[128:131]\n"
        "ds_read_b64_tr_b16 v[206:207], v233 offset:61952\n"
        "v_mfma_f32_16x16x32_bf16 v[124:127], v[188:191], v[64:67], v[124:127]\n"
        "ds_read_b64_tr_b16 v[208:209], v234 offset:61440\n"
        "v_mfma_f32_16x16x32_bf16 v[132:135], v[188:191], v[68:71], v[132:135]\n"
        "ds_read_b64_tr_b16 v[210:211], v234 offset:61952\n"
        "s_waitcnt lgkmcnt(6)\n"
        "v_mfma_f32_16x16x32_bf16 v[136:139], v[192:195], v[64:67], v[136:139]\n"
        "ds_read_b64_tr_b16 v[184:185], v235 offset:61440\n"
        "v_mfma_f32_16x16x32_bf16 v[144:147], v[192:195], v[68:71], v[144:147]\n"
        "ds_read_b64_tr_b16 v[186:187], v235 offset:61952\n"
        "v_mfma_f32_16x16x32_bf16 v[140:143], v[196:199], v[64:67], v[140:143]\n"
        "v_xor_b32 v232, 0x10000, v230\n"
        "ds_read_b64_tr_b16 v[188:189], v232 offset:0\n"
        "v_mfma_f32_16x16x32_bf16 v[148:151], v[196:199], v[68:71], v[148:151]\n"
        "ds_read_b64_tr_b16 v[190:191], v232 offset:512\n"
        "s_waitcnt lgkmcnt(6)\n"
        "v_mfma_f32_16x16x32_bf16 v[152:155], v[200:203], v[64:67], v[152:155]\n"
        "v_xor_b32 v233, 0x10020, v230\n"
        "ds_read_b64_tr_b16 v[192:193], v233 offset:0\n"
        "v_mfma_f32_16x16x32_bf16 v[160:163], v[200:203], v[68:71], v[160:163]\n"
        "ds_read_b64_tr_b16 v[194:195], v233 offset:512\n"
        "v_mfma_f32_16x16x32_bf16 v[156:159], v[204:207], v[64:67], v[156:159]\n"
        "v_xor_b32 v234, 0x10040, v230\n"
        "ds_read_b64_tr_b16 v[196:197], v234 offset:0\n"
        "v_mfma_f32_16x16x32_bf16 v[164:167], v[204:207], v[68:71], v[164:167]\n"
        "ds_read_b64_tr_b16 v[198:199], v234 offset:512\n"
        "s_waitcnt lgkmcnt(6)\n"
        "v_mfma_f32_16x16x32_bf16 v[168:171], v[208:211], v[64:67], v[168:171]\n"
        "v_xor_b32 v235, 0x10060, v230\n"
        "ds_read_b64_tr_b16 v[200:201], v235 offset:0\n"
        "v_mfma_f32_16x16x32_bf16 v[176:179], v[208:211], v[68:71], v[176:179]\n"
        "ds_read_b64_tr_b16 v[202:203], v235 offset:512\n"
        "v_mfma_f32_16x16x32_bf16 v[172:175], v[184:187], v[64:67], v[172:175]\n"
        "ds_read_b64_tr_b16 v[204:205], v232 offset:8192\n"
        "v_mfma_f32_16x16x32_bf16 v[180:183], v[184:187], v[68:71], v[180:183]\n"
        "ds_read_b64_tr_b16 v[206:207], v232 offset:8704\n"
        "s_barrier\n"
        "s_waitcnt lgkmcnt(6)\n"
        "v_mfma_f32_16x16x32_bf16 v[120:123], v[188:191], v[72:75], v[120:123]\n"
        "ds_read_b64_tr_b16 v[208:209], v233 offset:8192\n"
        "v_mfma_f32_16x16x32_bf16 v[128:131], v[188:191], v[76:79], v[128:131]\n"
        "ds_read_b64_tr_b16 v[210:211], v233 offset:8704\n"
        "v_mfma_f32_16x16x32_bf16 v[124:127], v[192:195], v[72:75], v[124:127]\n"
        "ds_read_b64_tr_b16 v[184:185], v234 offset:8192\n"
        "v_mfma_f32_16x16x32_bf16 v[132:135], v[192:195], v[76:79], v[132:135]\n"
        "ds_read_b64_tr_b16 v[186:187], v234 offset:8704\n"
        "s_waitcnt lgkmcnt(6)\n"
        "v_mfma_f32_16x16x32_bf16 v[136:139], v[196:199], v[72:75], v[136:139]\n"
        "ds_read_b64_tr_b16 v[188:189], v235 offset:8192\n"
        "v_mfma_f32_16x16x32_bf16 v[144:147], v[196:199], v[76:79], v[144:147]\n"
        "ds_read_b64_tr_b16 v[190:191], v235 offset:8704\n"
        "v_mfma_f32_16x16x32_bf16 v[140:143], v[200:203], v[72:75], v[140:143]\n"
        "ds_read_b64_tr_b16 v[192:193], v232 offset:4096\n"
        "v_mfma_f32_16x16x32_bf16 v[148:151], v[200:203], v[76:79], v[148:151]\n"
        "ds_read_b64_tr_b16 v[194:195], v232 offset:4608\n"
        "s_waitcnt lgkmcnt(6)\n"
        "v_mfma_f32_16x16x32_bf16 v[152:155], v[204:207], v[72:75], v[152:155]\n"
        "ds_read_b64_tr_b16 v[196:197], v233 offset:4096\n"
        "v_mfma_f32_16x16x32_bf16 v[160:163], v[204:207], v[76:79], v[160:163]\n"
        "ds_read_b64_tr_b16 v[198:199], v233 offset:4608\n"
        "v_mfma_f32_16x16x32_bf16 v[156:159], v[208:211], v[72:75], v[156:159]\n"
        "ds_read_b64_tr_b16 v[200:201], v234 offset:4096\n"
        "v_mfma_f32_16x16x32_bf16 v[164:167], v[208:211], v[76:79], v[164:167]\n"
        "ds_read_b64_tr_b16 v[202:203], v234 offset:4608\n"
        "s_waitcnt lgkmcnt(6)\n"
        "v_mfma_f32_16x16x32_bf16 v[168:171], v[184:187], v[72:75], v[168:171]\n"
        "ds_read_b64_tr_b16 v[204:205], v235 offset:4096\n"
        "v_mfma_f32_16x16x32_bf16 v[176:179], v[184:187], v[76:79], v[176:179]\n"
        "ds_read_b64_tr_b16 v[206:207], v235 offset:4608\n"
        "v_mfma_f32_16x16x32_bf16 v[172:175], v[188:191], v[72:75], v[172:175]\n"
        "ds_read_b64_tr_b16 v[208:209], v232 offset:12288\n"
        "v_mfma_f32_16x16x32_bf16 v[180:183], v[188:191], v[76:79], v[180:183]\n"
        "ds_read_b64_tr_b16 v[210:211], v232 offset:12800\n"
        "s_waitcnt lgkmcnt(6)\n"
        "v_mfma_f32_16x16x32_bf16 v[120:123], v[192:195], v[80:83], v[120:123]\n"
        "ds_read_b64_tr_b16 v[184:185], v233 offset:12288\n"
        "v_mfma_f32_16x16x32_bf16 v[128:131], v[192:195], v[84:87], v[128:131]\n"
        "ds_read_b64_tr_b16 v[186:187], v233 offset:12800\n"
        "v_mfma_f32_16x16x32_bf16 v[124:127], v[196:199], v[80:83], v[124:127]\n"
        "ds_read_b64_tr_b16 v[188:189], v234 offset:12288\n"
        "v_mfma_f32_16x16x32_bf16 v[132:135], v[196:199], v[84:87], v[132:135]\n"
        "ds_read_b64_tr_b16 v[190:191], v234 offset:12800\n"
        "s_waitcnt lgkmcnt(6)\n"
        "v_mfma_f32_16x16x32_bf16 v[136:139], v[200:203], v[80:83], v[136:139]\n"
        "ds_read_b64_tr_b16 v[192:193], v235 offset:12288\n"
        "v_mfma_f32_16x16x32_bf16 v[144:147], v[200:203], v[84:87], v[144:147]\n"
        "ds_read_b64_tr_b16 v[194:195], v235 offset:12800\n"
        "v_mfma_f32_16x16x32_bf16 v[140:143], v[204:207], v[80:83], v[140:143]\n"
        "ds_read_b64_tr_b16 v[196:197], v232 offset:16384\n"
        "v_mfma_f32_16x16x32_bf16 v[148:151], v[204:207], v[84:87], v[148:151]\n"
        "ds_read_b64_tr_b16 v[198:199], v232 offset:16896\n"
        "s_waitcnt lgkmcnt(6)\n"
        "v_mfma_f32_16x16x32_bf16 v[152:155], v[208:211], v[80:83], v[152:155]\n"
        "ds_read_b64_tr_b16 v[200:201], v233 offset:16384\n"
        "v_mfma_f32_16x16x32_bf16 v[160:163], v[208:211], v[84:87], v[160:163]\n"
        "ds_read_b64_tr_b16 v[202:203], v233 offset:16896\n"
        "v_mfma_f32_16x16x32_bf16 v[156:159], v[184:187], v[80:83], v[156:159]\n"
        "ds_read_b64_tr_b16 v[204:205], v234 offset:16384\n"
        "v_mfma_f32_16x16x32_bf16 v[164:167], v[184:187], v[84:87], v[164:167]\n"
        "ds_read_b64_tr_b16 v[206:207], v234 offset:16896\n"
        "s_waitcnt lgkmcnt(6)\n"
        "v_mfma_f32_16x16x32_bf16 v[168:171], v[188:191], v[80:83], v[168:171]\n"
        "ds_read_b64_tr_b16 v[208:209], v235 offset:16384\n"
        "v_mfma_f32_16x16x32_bf16 v[176:179], v[188:191], v[84:87], v[176:179]\n"
        "ds_read_b64_tr_b16 v[210:211], v235 offset:16896\n"
        "v_mfma_f32_16x16x32_bf16 v[172:175], v[192:195], v[80:83], v[172:175]\n"
        "ds_read_b64_tr_b16 v[184:185], v232 offset:24576\n"
        "v_mfma_f32_16x16x32_bf16 v[180:183], v[192:195], v[84:87], v[180:183]\n"
        "ds_read_b64_tr_b16 v[186:187], v232 offset:25088\n"
        "s_barrier\n"
        "s_waitcnt lgkmcnt(6)\n"
        "v_mfma_f32_16x16x32_bf16 v[120:123], v[196:199], v[88:91], v[120:123]\n"
        "ds_read_b64_tr_b16 v[188:189], v233 offset:24576\n"
        "v_mfma_f32_16x16x32_bf16 v[128:131], v[196:199], v[92:95], v[128:131]\n"
        "ds_read_b64_tr_b16 v[190:191], v233 offset:25088\n"
        "v_mfma_f32_16x16x32_bf16 v[124:127], v[200:203], v[88:91], v[124:127]\n"
        "ds_read_b64_tr_b16 v[192:193], v234 offset:24576\n"
        "v_mfma_f32_16x16x32_bf16 v[132:135], v[200:203], v[92:95], v[132:135]\n"
        "ds_read_b64_tr_b16 v[194:195], v234 offset:25088\n"
        "s_waitcnt lgkmcnt(6)\n"
        "v_mfma_f32_16x16x32_bf16 v[136:139], v[204:207], v[88:91], v[136:139]\n"
        "ds_read_b64_tr_b16 v[196:197], v235 offset:24576\n"
        "v_mfma_f32_16x16x32_bf16 v[144:147], v[204:207], v[92:95], v[144:147]\n"
        "ds_read_b64_tr_b16 v[198:199], v235 offset:25088\n"
        "v_mfma_f32_16x16x32_bf16 v[140:143], v[208:211], v[88:91], v[140:143]\n"
        "ds_read_b64_tr_b16 v[200:201], v232 offset:20480\n"
        "v_mfma_f32_16x16x32_bf16 v[148:151], v[208:211], v[92:95], v[148:151]\n"
        "ds_read_b64_tr_b16 v[202:203], v232 offset:20992\n"
        "s_waitcnt lgkmcnt(6)\n"
        "v_mfma_f32_16x16x32_bf16 v[152:155], v[184:187], v[88:91], v[152:155]\n"
        "ds_read_b64_tr_b16 v[204:205], v233 offset:20480\n"
        "v_mfma_f32_16x16x32_bf16 v[160:163], v[184:187], v[92:95], v[160:163]\n"
        "ds_read_b64_tr_b16 v[206:207], v233 offset:20992\n"
        "v_mfma_f32_16x16x32_bf16 v[156:159], v[188:191], v[88:91], v[156:159]\n"
        "ds_read_b64_tr_b16 v[208:209], v234 offset:20480\n"
        "v_mfma_f32_16x16x32_bf16 v[164:167], v[188:191], v[92:95], v[164:167]\n"
        "ds_read_b64_tr_b16 v[210:211], v234 offset:20992\n"
        "s_waitcnt lgkmcnt(6)\n"
        "v_mfma_f32_16x16x32_bf16 v[168:171], v[192:195], v[88:91], v[168:171]\n"
        "ds_read_b64_tr_b16 v[184:185], v235 offset:20480\n"
        "v_mfma_f32_16x16x32_bf16 v[176:179], v[192:195], v[92:95], v[176:179]\n"
        "ds_read_b64_tr_b16 v[186:187], v235 offset:20992\n"
        "v_mfma_f32_16x16x32_bf16 v[172:175], v[196:199], v[88:91], v[172:175]\n"
        "ds_read_b64_tr_b16 v[188:189], v232 offset:28672\n"
        "v_mfma_f32_16x16x32_bf16 v[180:183], v[196:199], v[92:95], v[180:183]\n"
        "ds_read_b64_tr_b16 v[190:191], v232 offset:29184\n"
        "s_waitcnt lgkmcnt(6)\n"
        "v_mfma_f32_16x16x32_bf16 v[120:123], v[200:203], v[96:99], v[120:123]\n"
        "ds_read_b64_tr_b16 v[192:193], v233 offset:28672\n"
        "v_mfma_f32_16x16x32_bf16 v[128:131], v[200:203], v[100:103], v[128:131]\n"
        "ds_read_b64_tr_b16 v[194:195], v233 offset:29184\n"
        "v_mfma_f32_16x16x32_bf16 v[124:127], v[204:207], v[96:99], v[124:127]\n"
        "ds_read_b64_tr_b16 v[196:197], v234 offset:28672\n"
        "v_mfma_f32_16x16x32_bf16 v[132:135], v[204:207], v[100:103], v[132:135]\n"
        "ds_read_b64_tr_b16 v[198:199], v234 offset:29184\n"
        "s_waitcnt lgkmcnt(6)\n"
        "v_mfma_f32_16x16x32_bf16 v[136:139], v[208:211], v[96:99], v[136:139]\n"
        "ds_read_b64_tr_b16 v[200:201], v235 offset:28672\n"
        "v_mfma_f32_16x16x32_bf16 v[144:147], v[208:211], v[100:103], v[144:147]\n"
        "ds_read_b64_tr_b16 v[202:203], v235 offset:29184\n"
        "v_mfma_f32_16x16x32_bf16 v[140:143], v[184:187], v[96:99], v[140:143]\n"
        "v_xor_b32 v232, 0x0, v230\n"
        "ds_read_b64_tr_b16 v[204:205], v232 offset:0\n"
        "v_mfma_f32_16x16x32_bf16 v[148:151], v[184:187], v[100:103], v[148:151]\n"
        "ds_read_b64_tr_b16 v[206:207], v232 offset:512\n"
        "s_waitcnt lgkmcnt(6)\n"
        "v_mfma_f32_16x16x32_bf16 v[152:155], v[188:191], v[96:99], v[152:155]\n"
        "v_xor_b32 v233, 0x20, v230\n"
        "ds_read_b64_tr_b16 v[208:209], v233 offset:0\n"
        "v_mfma_f32_16x16x32_bf16 v[160:163], v[188:191], v[100:103], v[160:163]\n"
        "ds_read_b64_tr_b16 v[210:211], v233 offset:512\n"
        "v_mfma_f32_16x16x32_bf16 v[156:159], v[192:195], v[96:99], v[156:159]\n"
        "v_xor_b32 v234, 0x40, v230\n"
        "ds_read_b64_tr_b16 v[184:185], v234 offset:0\n"
        "v_mfma_f32_16x16x32_bf16 v[164:167], v[192:195], v[100:103], v[164:167]\n"
        "ds_read_b64_tr_b16 v[186:187], v234 offset:512\n"
        "s_waitcnt lgkmcnt(6)\n"
        "v_mfma_f32_16x16x32_bf16 v[168:171], v[196:199], v[96:99], v[168:171]\n"
        "v_xor_b32 v235, 0x60, v230\n"
        "ds_read_b64_tr_b16 v[188:189], v235 offset:0\n"
        "v_mfma_f32_16x16x32_bf16 v[176:179], v[196:199], v[100:103], v[176:179]\n"
        "ds_read_b64_tr_b16 v[190:191], v235 offset:512\n"
        "v_mfma_f32_16x16x32_bf16 v[172:175], v[200:203], v[96:99], v[172:175]\n"
        "ds_read_b64_tr_b16 v[192:193], v232 offset:8192\n"
        "v_mfma_f32_16x16x32_bf16 v[180:183], v[200:203], v[100:103], v[180:183]\n"
        "ds_read_b64_tr_b16 v[194:195], v232 offset:8704\n"
        "s_waitcnt lgkmcnt(6)\n"
        "v_mfma_f32_16x16x32_bf16 v[120:123], v[204:207], v[104:107], v[120:123]\n"
        "ds_read_b64_tr_b16 v[196:197], v233 offset:8192\n"
        "v_mfma_f32_16x16x32_bf16 v[128:131], v[204:207], v[108:111], v[128:131]\n"
        "ds_read_b64_tr_b16 v[198:199], v233 offset:8704\n"
        "v_mfma_f32_16x16x32_bf16 v[124:127], v[208:211], v[104:107], v[124:127]\n"
        "ds_read_b64_tr_b16 v[200:201], v234 offset:8192\n"
        "v_mfma_f32_16x16x32_bf16 v[132:135], v[208:211], v[108:111], v[132:135]\n"
        "ds_read_b64_tr_b16 v[202:203], v234 offset:8704\n"
        "s_waitcnt lgkmcnt(6)\n"
        "v_mfma_f32_16x16x32_bf16 v[136:139], v[184:187], v[104:107], v[136:139]\n"
        "ds_read_b64_tr_b16 v[204:205], v235 offset:8192\n"
        "v_mfma_f32_16x16x32_bf16 v[144:147], v[184:187], v[108:111], v[144:147]\n"
        "ds_read_b64_tr_b16 v[206:207], v235 offset:8704\n"
        "v_mfma_f32_16x16x32_bf16 v[140:143], v[188:191], v[104:107], v[140:143]\n"
        "ds_read_b64_tr_b16 v[208:209], v232 offset:4096\n"
        "v_mfma_f32_16x16x32_bf16 v[148:151], v[188:191], v[108:111], v[148:151]\n"
        "ds_read_b64_tr_b16 v[210:211], v232 offset:4608\n"
        "s_waitcnt lgkmcnt(6)\n"
        "v_mfma_f32_16x16x32_bf16 v[152:155], v[192:195], v[104:107], v[152:155]\n"
        "ds_read_b64_tr_b16 v[184:185], v233 offset:4096\n"
        "v_mfma_f32_16x16x32_bf16 v[160:163], v[192:195], v[108:111], v[160:163]\n"
        "ds_read_b64_tr_b16 v[186:187], v233 offset:4608\n"
        "v_mfma_f32_16x16x32_bf16 v[156:159], v[196:199], v[104:107], v[156:159]\n"
        "ds_read_b64_tr_b16 v[188:189], v234 offset:4096\n"
        "v_mfma_f32_16x16x32_bf16 v[164:167], v[196:199], v[108:111], v[164:167]\n"
        "ds_read_b64_tr_b16 v[190:191], v234 offset:4608\n"
        "s_waitcnt lgkmcnt(6)\n"
        "v_mfma_f32_16x16x32_bf16 v[168:171], v[200:203], v[104:107], v[168:171]\n"
        "ds_read_b64_tr_b16 v[192:193], v235 offset:4096\n"
        "v_mfma_f32_16x16x32_bf16 v[176:179], v[200:203], v[108:111], v[176:179]\n"
        "ds_read_b64_tr_b16 v[194:195], v235 offset:4608\n"
        "v_mfma_f32_16x16x32_bf16 v[172:175], v[204:207], v[104:107], v[172:175]\n"
        "ds_read_b64_tr_b16 v[196:197], v232 offset:12288\n"
        "v_mfma_f32_16x16x32_bf16 v[180:183], v[204:207], v[108:111], v[180:183]\n"
        "ds_read_b64_tr_b16 v[198:199], v232 offset:12800\n"
        "s_waitcnt lgkmcnt(6)\n"
        "v_mfma_f32_16x16x32_bf16 v[120:123], v[208:211], v[112:115], v[120:123]\n"
        "ds_read_b64_tr_b16 v[200:201], v233 offset:12288\n"
        "v_mfma_f32_16x16x32_bf16 v[128:131], v[208:211], v[116:119], v[128:131]\n"
        "ds_read_b64_tr_b16 v[202:203], v233 offset:12800\n"
        "v_mfma_f32_16x16x32_bf16 v[124:127], v[184:187], v[112:115], v[124:127]\n"
        "ds_read_b64_tr_b16 v[204:205], v234 offset:12288\n"
        "v_mfma_f32_16x16x32_bf16 v[132:135], v[184:187], v[116:119], v[132:135]\n"
        "ds_read_b64_tr_b16 v[206:207], v234 offset:12800\n"
        "s_waitcnt lgkmcnt(6)\n"
        "v_mfma_f32_16x16x32_bf16 v[136:139], v[188:191], v[112:115], v[136:139]\n"
        "ds_read_b64_tr_b16 v[208:209], v235 offset:12288\n"
        "v_mfma_f32_16x16x32_bf16 v[144:147], v[188:191], v[116:119], v[144:147]\n"
        "ds_read_b64_tr_b16 v[210:211], v235 offset:12800\n"
        "v_mfma_f32_16x16x32_bf16 v[140:143], v[192:195], v[112:115], v[140:143]\n"
        "v_mfma_f32_16x16x32_bf16 v[148:151], v[192:195], v[116:119], v[148:151]\n"
        "s_waitcnt lgkmcnt(4)\n"
        "v_mfma_f32_16x16x32_bf16 v[152:155], v[196:199], v[112:115], v[152:155]\n"
        "v_mfma_f32_16x16x32_bf16 v[160:163], v[196:199], v[116:119], v[160:163]\n"
        "v_mfma_f32_16x16x32_bf16 v[156:159], v[200:203], v[112:115], v[156:159]\n"
        "v_mfma_f32_16x16x32_bf16 v[164:167], v[200:203], v[116:119], v[164:167]\n"
        "s_waitcnt lgkmcnt(0)\n"
        "v_mfma_f32_16x16x32_bf16 v[168:171], v[204:207], v[112:115], v[168:171]\n"
        "v_mfma_f32_16x16x32_bf16 v[176:179], v[204:207], v[116:119], v[176:179]\n"
        "v_mfma_f32_16x16x32_bf16 v[172:175], v[208:211], v[112:115], v[172:175]\n"
        "v_mfma_f32_16x16x32_bf16 v[180:183], v[208:211], v[116:119], v[180:183]\n"
        "s_setprio 3\n"
        "v_pk_mul_f32 v[216:217], v[120:121], v[120:121]\n"
        "v_pk_mul_f32 v[218:219], v[122:123], v[122:123]\n"
        "v_pk_mul_f32 v[220:221], v[124:125], v[124:125]\n"
        "v_pk_mul_f32 v[222:223], v[126:127], v[126:127]\n"
        "v_pk_fma_f32 v[216:217], v[216:217], s[86:87], v[6:7]\n"
        "v_pk_fma_f32 v[218:219], v[218:219], s[86:87], v[6:7]\n"
        "v_pk_fma_f32 v[220:221], v[220:221], s[86:87], v[6:7]\n"
        "v_pk_fma_f32 v[222:223], v[222:223], s[86:87], v[6:7]\n"
        "v_pk_mul_f32 v[216:217], v[216:217], v[120:121]\n"
        "v_pk_mul_f32 v[218:219], v[218:219], v[122:123]\n"
        "v_pk_mul_f32 v[220:221], v[220:221], v[124:125]\n"
        "v_pk_mul_f32 v[222:223], v[222:223], v[126:127]\n"
        "v_exp_f32 v216, v216\n"
        "v_exp_f32 v217, v217\n"
        "v_pk_mul_f32 v[152:153], v[152:153], v[120:121]\n"
        "v_exp_f32 v218, v218\n"
        "v_exp_f32 v219, v219\n"
        "v_pk_mul_f32 v[154:155], v[154:155], v[122:123]\n"
        "v_exp_f32 v220, v220\n"
        "v_exp_f32 v221, v221\n"
        "v_pk_mul_f32 v[156:157], v[156:157], v[124:125]\n"
        "v_exp_f32 v222, v222\n"
        "v_exp_f32 v223, v223\n"
        "v_pk_mul_f32 v[158:159], v[158:159], v[126:127]\n"
        "v_pk_add_f32 v[216:217], v[216:217], s[88:89]\n"
        "v_pk_add_f32 v[218:219], v[218:219], s[88:89]\n"
        "v_pk_add_f32 v[220:221], v[220:221], s[88:89]\n"
        "v_pk_add_f32 v[222:223], v[222:223], s[88:89]\n"
        "v_rcp_f32 v216, v216\n"
        "v_rcp_f32 v217, v217\n"
        "v_rcp_f32 v218, v218\n"
        "v_rcp_f32 v219, v219\n"
        "v_rcp_f32 v220, v220\n"
        "v_rcp_f32 v221, v221\n"
        "v_rcp_f32 v222, v222\n"
        "v_rcp_f32 v223, v223\n"
        "v_pk_mul_f32 v[152:153], v[152:153], v[216:217]\n"
        "v_pk_mul_f32 v[154:155], v[154:155], v[218:219]\n"
        "v_pk_mul_f32 v[156:157], v[156:157], v[220:221]\n"
        "v_pk_mul_f32 v[158:159], v[158:159], v[222:223]\n"
        "v_cvt_pk_bf16_f32 v120, v152, v153\n"
        "v_cvt_pk_bf16_f32 v121, v154, v155\n"
        "v_cvt_pk_bf16_f32 v122, v156, v157\n"
        "v_cvt_pk_bf16_f32 v123, v158, v159\n"
        "buffer_store_dwordx4 v[120:123], v236, s[60:63], s82 offen offset:0 sc1\n"
        "v_pk_mul_f32 v[216:217], v[128:129], v[128:129]\n"
        "v_pk_mul_f32 v[218:219], v[130:131], v[130:131]\n"
        "v_pk_mul_f32 v[220:221], v[132:133], v[132:133]\n"
        "v_pk_mul_f32 v[222:223], v[134:135], v[134:135]\n"
        "v_pk_fma_f32 v[216:217], v[216:217], s[86:87], v[6:7]\n"
        "v_pk_fma_f32 v[218:219], v[218:219], s[86:87], v[6:7]\n"
        "v_pk_fma_f32 v[220:221], v[220:221], s[86:87], v[6:7]\n"
        "v_pk_fma_f32 v[222:223], v[222:223], s[86:87], v[6:7]\n"
        "v_pk_mul_f32 v[216:217], v[216:217], v[128:129]\n"
        "v_pk_mul_f32 v[218:219], v[218:219], v[130:131]\n"
        "v_pk_mul_f32 v[220:221], v[220:221], v[132:133]\n"
        "v_pk_mul_f32 v[222:223], v[222:223], v[134:135]\n"
        "v_exp_f32 v216, v216\n"
        "v_exp_f32 v217, v217\n"
        "v_pk_mul_f32 v[160:161], v[160:161], v[128:129]\n"
        "v_exp_f32 v218, v218\n"
        "v_exp_f32 v219, v219\n"
        "v_pk_mul_f32 v[162:163], v[162:163], v[130:131]\n"
        "v_exp_f32 v220, v220\n"
        "v_exp_f32 v221, v221\n"
        "v_pk_mul_f32 v[164:165], v[164:165], v[132:133]\n"
        "v_exp_f32 v222, v222\n"
        "v_exp_f32 v223, v223\n"
        "v_pk_mul_f32 v[166:167], v[166:167], v[134:135]\n"
        "v_pk_add_f32 v[216:217], v[216:217], s[88:89]\n"
        "v_pk_add_f32 v[218:219], v[218:219], s[88:89]\n"
        "v_pk_add_f32 v[220:221], v[220:221], s[88:89]\n"
        "v_pk_add_f32 v[222:223], v[222:223], s[88:89]\n"
        "v_rcp_f32 v216, v216\n"
        "v_rcp_f32 v217, v217\n"
        "v_rcp_f32 v218, v218\n"
        "v_rcp_f32 v219, v219\n"
        "v_rcp_f32 v220, v220\n"
        "v_rcp_f32 v221, v221\n"
        "v_rcp_f32 v222, v222\n"
        "v_rcp_f32 v223, v223\n"
        "v_pk_mul_f32 v[160:161], v[160:161], v[216:217]\n"
        "v_pk_mul_f32 v[162:163], v[162:163], v[218:219]\n"
        "v_pk_mul_f32 v[164:165], v[164:165], v[220:221]\n"
        "v_pk_mul_f32 v[166:167], v[166:167], v[222:223]\n"
        "v_cvt_pk_bf16_f32 v128, v160, v161\n"
        "v_cvt_pk_bf16_f32 v129, v162, v163\n"
        "v_cvt_pk_bf16_f32 v130, v164, v165\n"
        "v_cvt_pk_bf16_f32 v131, v166, v167\n"
        "buffer_store_dwordx4 v[128:131], v236, s[60:63], s82 offen offset:1024 sc1\n"
        "v_pk_mul_f32 v[216:217], v[136:137], v[136:137]\n"
        "v_pk_mul_f32 v[218:219], v[138:139], v[138:139]\n"
        "v_pk_mul_f32 v[220:221], v[140:141], v[140:141]\n"
        "v_pk_mul_f32 v[222:223], v[142:143], v[142:143]\n"
        "v_pk_fma_f32 v[216:217], v[216:217], s[86:87], v[6:7]\n"
        "v_pk_fma_f32 v[218:219], v[218:219], s[86:87], v[6:7]\n"
        "v_pk_fma_f32 v[220:221], v[220:221], s[86:87], v[6:7]\n"
        "v_pk_fma_f32 v[222:223], v[222:223], s[86:87], v[6:7]\n"
        "v_pk_mul_f32 v[216:217], v[216:217], v[136:137]\n"
        "v_pk_mul_f32 v[218:219], v[218:219], v[138:139]\n"
        "v_pk_mul_f32 v[220:221], v[220:221], v[140:141]\n"
        "v_pk_mul_f32 v[222:223], v[222:223], v[142:143]\n"
        "v_exp_f32 v216, v216\n"
        "v_exp_f32 v217, v217\n"
        "v_pk_mul_f32 v[168:169], v[168:169], v[136:137]\n"
        "v_exp_f32 v218, v218\n"
        "v_exp_f32 v219, v219\n"
        "v_pk_mul_f32 v[170:171], v[170:171], v[138:139]\n"
        "v_exp_f32 v220, v220\n"
        "v_exp_f32 v221, v221\n"
        "v_pk_mul_f32 v[172:173], v[172:173], v[140:141]\n"
        "v_exp_f32 v222, v222\n"
        "v_exp_f32 v223, v223\n"
        "v_pk_mul_f32 v[174:175], v[174:175], v[142:143]\n"
        "v_pk_add_f32 v[216:217], v[216:217], s[88:89]\n"
        "v_pk_add_f32 v[218:219], v[218:219], s[88:89]\n"
        "v_pk_add_f32 v[220:221], v[220:221], s[88:89]\n"
        "v_pk_add_f32 v[222:223], v[222:223], s[88:89]\n"
        "v_rcp_f32 v216, v216\n"
        "v_rcp_f32 v217, v217\n"
        "v_rcp_f32 v218, v218\n"
        "v_rcp_f32 v219, v219\n"
        "v_rcp_f32 v220, v220\n"
        "v_rcp_f32 v221, v221\n"
        "v_rcp_f32 v222, v222\n"
        "v_rcp_f32 v223, v223\n"
        "v_pk_mul_f32 v[168:169], v[168:169], v[216:217]\n"
        "v_pk_mul_f32 v[170:171], v[170:171], v[218:219]\n"
        "v_pk_mul_f32 v[172:173], v[172:173], v[220:221]\n"
        "v_pk_mul_f32 v[174:175], v[174:175], v[222:223]\n"
        "v_cvt_pk_bf16_f32 v136, v168, v169\n"
        "v_cvt_pk_bf16_f32 v137, v170, v171\n"
        "v_cvt_pk_bf16_f32 v138, v172, v173\n"
        "v_cvt_pk_bf16_f32 v139, v174, v175\n"
        "buffer_store_dwordx4 v[136:139], v236, s[60:63], s83 offen offset:0 sc1\n"
        "v_pk_mul_f32 v[216:217], v[144:145], v[144:145]\n"
        "v_pk_mul_f32 v[218:219], v[146:147], v[146:147]\n"
        "v_pk_mul_f32 v[220:221], v[148:149], v[148:149]\n"
        "v_pk_mul_f32 v[222:223], v[150:151], v[150:151]\n"
        "v_pk_fma_f32 v[216:217], v[216:217], s[86:87], v[6:7]\n"
        "v_pk_fma_f32 v[218:219], v[218:219], s[86:87], v[6:7]\n"
        "v_pk_fma_f32 v[220:221], v[220:221], s[86:87], v[6:7]\n"
        "v_pk_fma_f32 v[222:223], v[222:223], s[86:87], v[6:7]\n"
        "v_pk_mul_f32 v[216:217], v[216:217], v[144:145]\n"
        "v_pk_mul_f32 v[218:219], v[218:219], v[146:147]\n"
        "v_pk_mul_f32 v[220:221], v[220:221], v[148:149]\n"
        "v_pk_mul_f32 v[222:223], v[222:223], v[150:151]\n"
        "v_exp_f32 v216, v216\n"
        "v_exp_f32 v217, v217\n"
        "v_pk_mul_f32 v[176:177], v[176:177], v[144:145]\n"
        "v_exp_f32 v218, v218\n"
        "v_exp_f32 v219, v219\n"
        "v_pk_mul_f32 v[178:179], v[178:179], v[146:147]\n"
        "v_exp_f32 v220, v220\n"
        "v_exp_f32 v221, v221\n"
        "v_pk_mul_f32 v[180:181], v[180:181], v[148:149]\n"
        "v_exp_f32 v222, v222\n"
        "v_exp_f32 v223, v223\n"
        "v_pk_mul_f32 v[182:183], v[182:183], v[150:151]\n"
        "v_pk_add_f32 v[216:217], v[216:217], s[88:89]\n"
        "v_pk_add_f32 v[218:219], v[218:219], s[88:89]\n"
        "v_pk_add_f32 v[220:221], v[220:221], s[88:89]\n"
        "v_pk_add_f32 v[222:223], v[222:223], s[88:89]\n"
        "v_rcp_f32 v216, v216\n"
        "v_rcp_f32 v217, v217\n"
        "v_rcp_f32 v218, v218\n"
        "v_rcp_f32 v219, v219\n"
        "v_rcp_f32 v220, v220\n"
        "v_rcp_f32 v221, v221\n"
        "v_rcp_f32 v222, v222\n"
        "v_rcp_f32 v223, v223\n"
        "v_pk_mul_f32 v[176:177], v[176:177], v[216:217]\n"
        "v_pk_mul_f32 v[178:179], v[178:179], v[218:219]\n"
        "v_pk_mul_f32 v[180:181], v[180:181], v[220:221]\n"
        "v_pk_mul_f32 v[182:183], v[182:183], v[222:223]\n"
        "v_cvt_pk_bf16_f32 v144, v176, v177\n"
        "v_cvt_pk_bf16_f32 v145, v178, v179\n"
        "v_cvt_pk_bf16_f32 v146, v180, v181\n"
        "v_cvt_pk_bf16_f32 v147, v182, v183\n"
        "buffer_store_dwordx4 v[144:147], v236, s[60:63], s83 offen offset:1024 sc1\n"
        "s_barrier\n"
        "s_waitcnt vmcnt(0)\n"
        "s_add_u32 s66, s95, 262144\n"
        "s_mov_b64 exec, 0xffff\n"
        "buffer_store_dwordx2 v[242:243], v2, s[60:63], s66 offen sc1\n"
        "s_mov_b64 exec, -1\n"
        "s_setprio 0\n"
        "s_sleep 12\n"
        "s_lshr_b32 s67, s97, 1\n"
        "s_lshl_b32 s68, s74, 5\n"
        "s_add_u32 s67, s67, s68\n"
        "s_and_b32 s68, s97, 1\n"
        "s_lshl4_add_u32 s67, s68, s67\n"
        "s_lshl_b32 s67, s67, 7\n"
        "s_add_u32 s67, s67, 266240\n"
        "s_add_u32 s68, s60, s67\n"
        "s_addc_u32 s69, s61, 0\n"
        "s_mov_b32 s79, 2000\n"
        "Lsp10:\n"
        "s_load_dwordx2 s[32:33], s[68:69], 0x0 glc\n"
        "s_load_dwordx2 s[34:35], s[68:69], 0x100 glc\n"
        "s_load_dwordx2 s[36:37], s[68:69], 0x200 glc\n"
        "s_load_dwordx2 s[38:39], s[68:69], 0x300 glc\n"
        "s_load_dwordx2 s[40:41], s[68:69], 0x400 glc\n"
        "s_load_dwordx2 s[42:43], s[68:69], 0x500 glc\n"
        "s_load_dwordx2 s[44:45], s[68:69], 0x600 glc\n"
        "s_waitcnt lgkmcnt(0)\n"
        "Lsp10m:\n"
        "s_xor_b64 s[32:33], s[32:33], s[70:71]\n"
        "s_xor_b64 s[34:35], s[34:35], s[70:71]\n"
        "s_xor_b64 s[36:37], s[36:37], s[70:71]\n"
        "s_xor_b64 s[38:39], s[38:39], s[70:71]\n"
        "s_xor_b64 s[40:41], s[40:41], s[70:71]\n"
        "s_xor_b64 s[42:43], s[42:43], s[70:71]\n"
        "s_xor_b64 s[44:45], s[44:45], s[70:71]\n"
        "s_or_b64 s[32:33], s[32:33], s[34:35]\n"
        "s_or_b64 s[32:33], s[32:33], s[36:37]\n"
        "s_or_b64 s[32:33], s[32:33], s[38:39]\n"
        "s_or_b64 s[32:33], s[32:33], s[40:41]\n"
        "s_or_b64 s[32:33], s[32:33], s[42:43]\n"
        "s_or_b64 s[32:33], s[32:33], s[44:45]\n"
        "s_cbranch_scc0 Lso10\n"
        "s_sub_u32 s79, s79, 1\n"
        "s_cbranch_scc0 Lsp10\n"
        "s_lshl2_add_u32 s67, s74, s97\n"
        "s_mul_i32 s67, s67, 73728\n"
        "s_add_u32 s67, s67, 1048576\n"
        "v_lshl_add_u32 v237, v1, 4, s67\n"
        "s_lshr_b32 s68, s97, 1\n"
        "s_lshl_b32 s68, s68, 7\n"
        "s_add_u32 s68, s68, 4096\n"
        "s_lshl_b32 s67, s74, 12\n"
        "s_add_u32 s67, s67, s68\n"
        "v_lshrrev_b32 v216, 5, v1\n"
        "v_lshl_add_u32 v216, v216, 11, s67\n"
        "v_bfe_u32 v217, v1, 2, 3\n"
        "v_min_u32 v217, 6, v217\n"
        "v_lshl_add_u32 v240, v217, 8, v216\n"
        "s_lshl_b32 s67, s74, 11\n"
        "s_add_u32 s67, s67, s68\n"
        "v_lshl_add_u32 v231, v4, 8, s67\n"
        "s_mov_b32 s66, 262144\n"
        "buffer_load_dwordx2 v[244:245], v240, s[60:63], s66 offen sc1\n"
        "s_waitcnt vmcnt(0)\n"
        "v_cmp_ne_u64 vcc, s[70:71], v[244:245]\n"
        "s_cbranch_vccz Lret11\n"
        "s_mov_b32 s79, 400000\n"
        "s_mov_b32 s66, 262144\n"
        "Lspin11:\n"
        "s_sleep 2\n"
        "buffer_load_dwordx2 v[244:245], v240, s[60:63], s66 offen sc1\n"
        "s_waitcnt vmcnt(0)\n"
        "v_cmp_ne_u64 vcc, s[70:71], v[244:245]\n"
        "s_cbranch_vccz Lret11\n"
        "s_sub_u32 s79, s79, 1\n"
        "s_cmp_lg_u32 s79, 0\n"
        "s_cbranch_scc1 Lspin11\n"
        "Lret11:\n"
        "s_nop 0\n"
        "Lso10:\n"
        "s_barrier\n"
        "s_sleep 8\n"
        "s_lshl2_add_u32 s67, s74, 0\n"
        "s_lshl1_add_u32 s67, s67, s97\n"
        "s_lshl_b32 s67, s67, 8\n"
        "s_add_u32 s67, s67, 397312\n"
        "s_add_u32 s68, s60, s67\n"
        "s_addc_u32 s69, s61, 0\n"
        "s_mov_b32 s79, 2000\n"
        "Lsp12:\n"
        "s_load_dwordx2 s[32:33], s[68:69], 0x0 glc\n"
        "s_load_dwordx2 s[34:35], s[68:69], 0x80 glc\n"
        "s_waitcnt lgkmcnt(0)\n"
        "Lsp12m:\n"
        "s_xor_b64 s[32:33], s[32:33], s[70:71]\n"
        "s_xor_b64 s[34:35], s[34:35], s[70:71]\n"
        "s_or_b64 s[32:33], s[32:33], s[34:35]\n"
        "s_cbranch_scc0 Lso12\n"
        "s_sub_u32 s79, s79, 1\n"
        "s_cbranch_scc0 Lsp12\n"
        "s_lshl2_add_u32 s67, s74, s97\n"
        "s_mul_i32 s67, s67, 73728\n"
        "s_add_u32 s67, s67, 1048576\n"
        "v_lshl_add_u32 v237, v1, 4, s67\n"
        "s_lshr_b32 s68, s97, 1\n"
        "s_lshl_b32 s68, s68, 7\n"
        "s_add_u32 s68, s68, 4096\n"
        "s_lshl_b32 s67, s74, 12\n"
        "s_add_u32 s67, s67, s68\n"
        "v_lshrrev_b32 v216, 5, v1\n"
        "v_lshl_add_u32 v216, v216, 11, s67\n"
        "v_bfe_u32 v217, v1, 2, 3\n"
        "v_min_u32 v217, 6, v217\n"
        "v_lshl_add_u32 v240, v217, 8, v216\n"
        "s_lshl_b32 s67, s74, 11\n"
        "s_add_u32 s67, s67, s68\n"
        "v_lshl_add_u32 v231, v4, 8, s67\n"
        "s_mov_b32 s66, 393216\n"
        "buffer_load_dwordx2 v[244:245], v231, s[60:63], s66 offen sc1\n"
        "s_waitcnt vmcnt(0)\n"
        "v_cmp_ne_u64 vcc, s[70:71], v[244:245]\n"
        "s_cbranch_vccz Lret13\n"
        "s_mov_b32 s79, 400000\n"
        "s_mov_b32 s66, 393216\n"
        "Lspin13:\n"
        "s_sleep 2\n"
        "buffer_load_dwordx2 v[244:245], v231, s[60:63], s66 offen sc1\n"
        "s_waitcnt vmcnt(0)\n"
        "v_cmp_ne_u64 vcc, s[70:71], v[244:245]\n"
        "s_cbranch_vccz Lret13\n"
        "s_sub_u32 s79, s79, 1\n"
        "s_cmp_lg_u32 s79, 0\n"
        "s_cbranch_scc1 Lspin13\n"
        "Lret13:\n"
        "s_nop 0\n"
        "Lso12:\n"
        "s_barrier\n"
        "s_lshr_b32 s67, s64, 1\n"
        "s_lshl_b32 s67, s67, 3\n"
        "s_lshr_b32 s67, 0x3020100, s67\n"
        "s_and_b32 s67, s67, 0xff\n"
        "s_lshl_b32 s67, s67, 14\n"
        "s_and_b32 s68, s64, 1\n"
        "s_lshl_b32 s68, s68, 3\n"
        "v_lshrrev_b32 v216, 3, v1\n"
        "v_add_u32 v216, s68, v216\n"
        "v_lshlrev_b32 v217, 1, v5\n"
        "v_xor_b32 v216, v216, v217\n"
        "v_lshlrev_b32 v216, 4, v216\n"
        "v_lshl_add_u32 v216, v5, 9, v216\n"
        "v_add_u32 v230, s67, v216\n"
        "v_lshlrev_b32 v216, 16, v246\n"
        "v_and_b32 v217, s78, v246\n"
        "v_lshlrev_b32 v218, 16, v247\n"
        "v_and_b32 v219, s78, v247\n"
        "v_lshlrev_b32 v220, 16, v248\n"
        "v_and_b32 v221, s78, v248\n"
        "v_lshlrev_b32 v222, 16, v249\n"
        "v_and_b32 v223, s78, v249\n"
        "s_barrier\n"
        "ds_read_b128 v[8:11], v230 offset:0\n"
        "s_nop 0\n"
        "s_cbranch_execz Lback\n"
        "ds_read_b128 v[12:15], v230 offset:256\n"
        "ds_read_b128 v[16:19], v230 offset:4096\n"
        "ds_read_b128 v[20:23], v230 offset:4352\n"
        "ds_read_b128 v[24:27], v230 offset:8192\n"
        "ds_read_b128 v[28:31], v230 offset:8448\n"
        "ds_read_b128 v[32:35], v230 offset:12288\n"
        "ds_read_b128 v[36:39], v230 offset:12544\n"
        "s_waitcnt lgkmcnt(7)\n"
        "v_pk_add_f32 v[216:217], v[216:217], v[8:9]\n"
        "v_pk_add_f32 v[218:219], v[218:219], v[10:11]\n"
        "s_waitcnt lgkmcnt(6)\n"
        "v_pk_add_f32 v[220:221], v[220:221], v[12:13]\n"
        "v_pk_add_f32 v[222:223], v[222:223], v[14:15]\n"
        "s_waitcnt lgkmcnt(5)\n"
        "v_pk_add_f32 v[216:217], v[216:217], v[16:17]\n"
        "v_pk_add_f32 v[218:219], v[218:219], v[18:19]\n"
        "s_waitcnt lgkmcnt(4)\n"
        "v_pk_add_f32 v[220:221], v[220:221], v[20:21]\n"
        "v_pk_add_f32 v[222:223], v[222:223], v[22:23]\n"
        "s_waitcnt lgkmcnt(3)\n"
        "v_pk_add_f32 v[216:217], v[216:217], v[24:25]\n"
        "v_pk_add_f32 v[218:219], v[218:219], v[26:27]\n"
        "s_waitcnt lgkmcnt(2)\n"
        "v_pk_add_f32 v[220:221], v[220:221], v[28:29]\n"
        "v_pk_add_f32 v[222:223], v[222:223], v[30:31]\n"
        "s_waitcnt lgkmcnt(1)\n"
        "v_pk_add_f32 v[216:217], v[216:217], v[32:33]\n"
        "v_pk_add_f32 v[218:219], v[218:219], v[34:35]\n"
        "s_waitcnt lgkmcnt(0)\n"
        "v_pk_add_f32 v[220:221], v[220:221], v[36:37]\n"
        "v_pk_add_f32 v[222:223], v[222:223], v[38:39]\n"
        "v_cvt_pk_bf16_f32 v246, v216, v217\n"
        "v_cvt_pk_bf16_f32 v247, v218, v219\n"
        "v_cvt_pk_bf16_f32 v248, v220, v221\n"
        "v_cvt_pk_bf16_f32 v249, v222, v223\n"
        "buffer_store_dwordx4 v[246:249], v239, s[56:59], 0 offen\n"
        "s_branch Lend\n"
        "Lrole_G:\n"
        "s_setprio 0\n"
        "ds_read_b128 v[8:11], v250 offset:0\n"
        "s_waitcnt lgkmcnt(8)\n"
        "ds_read_b128 v[16:19], v251 offset:0\n"
        "ds_read_b128 v[24:27], v250 offset:8192\n"
        "ds_read_b128 v[32:35], v251 offset:8192\n"
        "ds_read_b128 v[40:43], v250 offset:16384\n"
        "ds_read_b128 v[48:51], v251 offset:16384\n"
        "ds_read_b128 v[56:59], v250 offset:24576\n"
        "ds_read_b128 v[64:67], v251 offset:24576\n"
        "s_waitcnt lgkmcnt(8)\n"
        "ds_read_b128 v[72:75], v250 offset:32768\n"
        "ds_read_b128 v[80:83], v251 offset:32768\n"
        "ds_read_b128 v[88:91], v250 offset:40960\n"
        "ds_read_b128 v[96:99], v251 offset:40960\n"
        "ds_read_b128 v[104:107], v250 offset:49152\n"
        "ds_read_b128 v[112:115], v251 offset:49152\n"
        "s_mov_b32 s66, 0x8029731\n"
        "s_cmp_ge_u32 s15, 7\n"
        "s_cselect_b32 s66, 0x8059764, s66\n"
        "s_lshl_b32 s79, s97, 3\n"
        "s_add_u32 s79, s79, 0x40000\n"
        "s_bfe_u32 s66, s66, s79\n"
        "s_cbranch_scc0 Lvb2\n"
        "s_lshl_b32 s66, s66, 12\n"
        "s_mov_b64 exec, 0\n"
        "s_getpc_b64 s[90:91]\n"
        "Lvp2:\n"
        "s_sub_u32 s66, s66, Lvp2-Lp0\n"
        "s_ashr_i32 s79, s66, 31\n"
        "s_add_u32 s90, s90, s66\n"
        "s_addc_u32 s91, s91, s79\n"
        "s_swappc_b64 vcc, s[90:91]\n"
        "s_mov_b64 exec, -1\n"
        "Lvb2:\n"
        "s_waitcnt vmcnt(5) lgkmcnt(0)\n"
        "s_barrier\n"
        "s_waitcnt vmcnt(4)\n"
        "s_barrier\n"
        "s_add_u32 m0, s65, 98304\n"
        "s_mul_i32 s66, s10, 172032\n"
        "buffer_load_dwordx4 v224, s[36:39], s66 offen lds\n"
        "ds_read_b128 v[12:15], v252 offset:0\n"
        "ds_read_b128 v[20:23], v253 offset:0\n"
        "v_mov_b32 v6, 0xc0135761\n"
        "v_mov_b32 v7, 0xc0135761\n"
        "s_mov_b32 s88, 1.0\n"
        "s_add_u32 m0, s65, 99328\n"
        "s_mul_i32 s66, s10, 172032\n"
        "buffer_load_dwordx4 v225, s[36:39], s66 offen lds\n"
        "ds_read_b128 v[28:31], v252 offset:8192\n"
        "ds_read_b128 v[36:39], v253 offset:8192\n"
        "s_mov_b32 s89, 1.0\n"
        "v_readfirstlane_b32 s67, v244\n"
        "v_readfirstlane_b32 s68, v245\n"
        "s_add_u32 m0, s65, 49152\n"
        "s_mul_i32 s66, s4, 229376\n"
        "buffer_load_dwordx4 v226, s[40:43], s66 offen lds\n"
        "s_mul_hi_u32 s71, s67, 0x4c957f2d\n"
        "s_mul_i32 s69, s67, 0x5851f42d\n"
        "s_add_u32 s71, s71, s69\n"
        "s_add_u32 m0, s65, 50176\n"
        "s_mul_i32 s66, s4, 229376\n"
        "buffer_load_dwordx4 v227, s[40:43], s66 offen lds\n"
        "s_mul_i32 s69, s68, 0x4c957f2d\n"
        "s_add_u32 s71, s71, s69\n"
        "s_mul_i32 s70, s67, 0x4c957f2d\n"
        "s_add_u32 m0, s65, 57344\n"
        "s_mul_i32 s66, s4, 229376\n"
        "buffer_load_dwordx4 v226, s[44:47], s66 offen lds\n"
        "s_add_u32 s70, s70, 0xf767814f\n"
        "s_addc_u32 s71, s71, 0x14057b7e\n"
        "v_mov_b32 v242, s70\n"
        "v_mov_b32 v243, s71\n"
        "s_waitcnt vmcnt(7) lgkmcnt(2)\n"
        "s_barrier\n"
        "s_add_u32 m0, s65, 58368\n"
        "s_mul_i32 s66, s4, 229376\n"
        "buffer_load_dwordx4 v227, s[44:47], s66 offen lds\n"
        "ds_read_b128 v[44:47], v252 offset:16384\n"
        "ds_read_b128 v[52:55], v253 offset:16384\n"
        "s_mov_b32 s80, 0xbdd2d3e8\n"
        "s_mov_b32 s86, s80\n"
        "s_mov_b32 s87, s80\n"
        "s_and_b32 s68, s73, 1\n"
        "s_mul_i32 s68, s68, 36\n"
        "s_add_u32 m0, s65, 65536\n"
        "s_mul_i32 s66, s5, 229376\n"
        "buffer_load_dwordx4 v226, s[40:43], s66 offen lds\n"
        "ds_read_b128 v[60:63], v252 offset:24576\n"
        "ds_read_b128 v[68:71], v253 offset:24576\n"
        "s_lshr_b32 s67, s73, 1\n"
        "s_add_u32 s67, s67, s68\n"
        "s_lshl_b32 s82, s67, 12\n"
        "s_add_u32 s83, s82, 73728\n"
        "s_lshr_b32 s67, s97, 1\n"
        "s_add_u32 m0, s65, 66560\n"
        "s_mul_i32 s66, s5, 229376\n"
        "buffer_load_dwordx4 v227, s[40:43], s66 offen lds\n"
        "ds_read_b128 v[76:79], v252 offset:32768\n"
        "ds_read_b128 v[84:87], v253 offset:32768\n"
        "s_lshl1_add_u32 s67, s72, s67\n"
        "s_mul_i32 s67, s67, 294912\n"
        "s_and_b32 s68, s97, 1\n"
        "s_lshl_b32 s68, s68, 11\n"
        "s_add_u32 s67, s67, s68\n"
        "s_add_u32 m0, s65, 73728\n"
        "s_mul_i32 s66, s5, 229376\n"
        "buffer_load_dwordx4 v226, s[44:47], s66 offen lds\n"
        "ds_read_b128 v[92:95], v252 offset:40960\n"
        "ds_read_b128 v[100:103], v253 offset:40960\n"
        "s_add_u32 s67, s67, 1048576\n"
        "v_lshl_add_u32 v236, v1, 4, s67\n"
        "s_lshl_b32 s69, s75, 7\n"
        "s_add_u32 s48, s26, s69\n"
        "s_addc_u32 s49, s27, 0\n"
        "s_and_b32 s49, s49, 0xffff\n"
        "s_add_u32 m0, s65, 74752\n"
        "s_mul_i32 s66, s5, 229376\n"
        "buffer_load_dwordx4 v227, s[44:47], s66 offen lds\n"
        "ds_read_b128 v[108:111], v252 offset:49152\n"
        "ds_read_b128 v[116:119], v253 offset:49152\n"
        "s_sub_u32 s50, 1605632, s69\n"
        "s_mov_b32 s51, 0x20000\n"
        "s_mov_b64 s[12:13], s[20:21]\n"
        "s_mov_b32 s56, s28\n"
        "s_and_b32 s57, s29, 0xffff\n"
        "s_add_u32 m0, s65, 81920\n"
        "s_mul_i32 s66, s6, 229376\n"
        "buffer_load_dwordx4 v226, s[40:43], s66 offen lds\n"
        "s_mov_b32 s58, 1835008\n"
        "s_mov_b32 s59, 0x20000\n"
        "v_lshrrev_b32 v217, 5, v1\n"
        "v_bfe_u32 v216, v1, 3, 2\n"
        "v_lshl_add_u32 v216, v217, 4, v216\n"
        "s_add_u32 m0, s65, 82944\n"
        "s_mul_i32 s66, s6, 229376\n"
        "buffer_load_dwordx4 v227, s[40:43], s66 offen lds\n"
        "s_mov_b32 s69, 896\n"
        "v_mad_u32_u24 v228, v216, s69, v222\n"
        "v_xor_b32 v229, 64, v228\n"
        "s_lshl_b32 s84, s97, 12\n"
        "s_add_u32 s68, s75, s74\n"
        "s_add_u32 m0, s65, 90112\n"
        "s_mul_i32 s66, s6, 229376\n"
        "buffer_load_dwordx4 v226, s[44:47], s66 offen lds\n"
        "s_and_b32 s68, s68, 0\n"
        "s_lshl_b32 s67, s68, 4\n"
        "s_lshl_b32 s68, s68, 1\n"
        "s_mov_b32 s28, 0x06543210\n"
        "s_mov_b32 s29, 0x00654321\n"
        "s_waitcnt vmcnt(11) lgkmcnt(0)\n"
        "s_barrier\n"
        "s_add_u32 m0, s65, 91136\n"
        "s_mul_i32 s66, s6, 229376\n"
        "buffer_load_dwordx4 v227, s[44:47], s66 offen lds\n"
        "s_lshr_b64 s[28:29], s[28:29], s67\n"
        "s_mov_b32 s11, s68\n"
        "s_lshr_b32 s67, s97, 1\n"
        "s_mul_i32 s67, s67, 57344\n"
        "s_mul_i32 s68, s97, 28672\n"
        "s_add_u32 m0, s65, 0\n"
        "s_mul_i32 s66, s7, 229376\n"
        "buffer_load_dwordx4 v226, s[40:43], s66 offen lds\n"
        "s_add_u32 s67, s67, s68\n"
        "s_bfe_u32 s16, s28, 0x40000\n"
        "s_mul_i32 s16, s16, 229376\n"
        "s_add_u32 s16, s16, s67\n"
        "s_bfe_u32 s17, s28, 0x40004\n"
        "s_add_u32 m0, s65, 1024\n"
        "s_mul_i32 s66, s7, 229376\n"
        "buffer_load_dwordx4 v227, s[40:43], s66 offen lds\n"
        "s_mul_i32 s17, s17, 229376\n"
        "s_add_u32 s17, s17, s67\n"
        "s_bfe_u32 s18, s28, 0x40008\n"
        "s_mul_i32 s18, s18, 229376\n"
        "s_add_u32 s18, s18, s67\n"
        "s_add_u32 m0, s65, 8192\n"
        "s_mul_i32 s66, s7, 229376\n"
        "buffer_load_dwordx4 v226, s[44:47], s66 offen lds\n"
        "s_bfe_u32 s19, s28, 0x4000c\n"
        "s_mul_i32 s19, s19, 229376\n"
        "s_add_u32 s19, s19, s67\n"
        "s_bfe_u32 s20, s28, 0x40010\n"
        "s_mul_i32 s20, s20, 229376\n"
        "s_add_u32 m0, s65, 9216\n"
        "s_mul_i32 s66, s7, 229376\n"
        "buffer_load_dwordx4 v227, s[44:47], s66 offen lds\n"
        "s_add_u32 s20, s20, s67\n"
        "s_bfe_u32 s21, s28, 0x40014\n"
        "s_mul_i32 s21, s21, 229376\n"
        "s_add_u32 s21, s21, s67\n"
        "s_bfe_u32 s22, s28, 0x40018\n"
        "s_add_u32 m0, s65, 114688\n"
        "s_mul_i32 s66, s8, 229376\n"
        "buffer_load_dwordx4 v226, s[40:43], s66 offen lds\n"
        "s_mul_i32 s22, s22, 229376\n"
        "s_add_u32 s22, s22, s67\n"
        "v_add_u32 v231, s84, v230\n"
        "s_lshl_b32 s67, s72, 2\n"
        "s_add_u32 s67, s67, s97\n"
        "s_add_u32 m0, s65, 115712\n"
        "s_mul_i32 s66, s8, 229376\n"
        "buffer_load_dwordx4 v227, s[40:43], s66 offen lds\n"
        "s_lshl_b32 s95, s67, 4\n"
        "s_add_u32 s95, s95, s73\n"
        "s_lshl_b32 s95, s95, 7\n"
        "s_add_u32 s95, s95, 4096\n"
        "s_add_u32 m0, s65, 122880\n"
        "s_mul_i32 s66, s8, 229376\n"
        "buffer_load_dwordx4 v226, s[44:47], s66 offen lds\n"
        "s_waitcnt vmcnt(15)\n"
        "s_barrier\n"
        "s_add_u32 m0, s65, 123904\n"
        "s_mul_i32 s66, s8, 229376\n"
        "buffer_load_dwordx4 v227, s[44:47], s66 offen lds\n"
        "s_add_u32 m0, s65, 131072\n"
        "s_mul_i32 s66, s9, 229376\n"
        "buffer_load_dwordx4 v226, s[40:43], s66 offen lds\n"
        "s_add_u32 m0, s65, 132096\n"
        "s_mul_i32 s66, s9, 229376\n"
        "buffer_load_dwordx4 v227, s[40:43], s66 offen lds\n"
        "s_add_u32 m0, s65, 139264\n"
        "s_mul_i32 s66, s9, 229376\n"
        "buffer_load_dwordx4 v226, s[44:47], s66 offen lds\n"
        "v_xor_b32 v232, 0x0, v230\n"
        "ds_read_b64_tr_b16 v[184:185], v232 offset:49152\n"
        "ds_read_b64_tr_b16 v[186:187], v232 offset:49664\n"
        "v_xor_b32 v233, 0x20, v230\n"
        "ds_read_b64_tr_b16 v[188:189], v233 offset:49152\n"
        "ds_read_b64_tr_b16 v[190:191], v233 offset:49664\n"
        "v_xor_b32 v234, 0x40, v230\n"
        "ds_read_b64_tr_b16 v[192:193], v234 offset:49152\n"
        "ds_read_b64_tr_b16 v[194:195], v234 offset:49664\n"
        "v_xor_b32 v235, 0x60, v230\n"
        "ds_read_b64_tr_b16 v[196:197], v235 offset:49152\n"
        "ds_read_b64_tr_b16 v[198:199], v235 offset:49664\n"
        "ds_read_b64_tr_b16 v[200:201], v232 offset:57344\n"
        "ds_read_b64_tr_b16 v[202:203], v232 offset:57856\n"
        "s_waitcnt vmcnt(11)\n"
        "s_barrier\n"
        "s_waitcnt lgkmcnt(6)\n"
        "v_mfma_f32_16x16x32_bf16 v[120:123], v[184:187], v[8:11], 0\n"
        "ds_read_b64_tr_b16 v[204:205], v233 offset:57344\n"
        "v_mfma_f32_16x16x32_bf16 v[128:131], v[184:187], v[12:15], 0\n"
        "ds_read_b64_tr_b16 v[206:207], v233 offset:57856\n"
        "v_mfma_f32_16x16x32_bf16 v[124:127], v[188:191], v[8:11], 0\n"
        "ds_read_b64_tr_b16 v[208:209], v234 offset:57344\n"
        "v_mfma_f32_16x16x32_bf16 v[132:135], v[188:191], v[12:15], 0\n"
        "ds_read_b64_tr_b16 v[210:211], v234 offset:57856\n"
        "s_waitcnt lgkmcnt(6)\n"
        "v_mfma_f32_16x16x32_bf16 v[136:139], v[192:195], v[8:11], 0\n"
        "ds_read_b64_tr_b16 v[184:185], v235 offset:57344\n"
        "v_mfma_f32_16x16x32_bf16 v[144:147], v[192:195], v[12:15], 0\n"
        "ds_read_b64_tr_b16 v[186:187], v235 offset:57856\n"
        "v_mfma_f32_16x16x32_bf16 v[140:143], v[196:199], v[8:11], 0\n"
        "ds_read_b64_tr_b16 v[188:189], v232 offset:53248\n"
        "v_mfma_f32_16x16x32_bf16 v[148:151], v[196:199], v[12:15], 0\n"
        "ds_read_b64_tr_b16 v[190:191], v232 offset:53760\n"
        "s_waitcnt lgkmcnt(6)\n"
        "v_mfma_f32_16x16x32_bf16 v[152:155], v[200:203], v[8:11], 0\n"
        "ds_read_b64_tr_b16 v[192:193], v233 offset:53248\n"
        "v_mfma_f32_16x16x32_bf16 v[160:163], v[200:203], v[12:15], 0\n"
        "ds_read_b64_tr_b16 v[194:195], v233 offset:53760\n"
        "v_mfma_f32_16x16x32_bf16 v[156:159], v[204:207], v[8:11], 0\n"
        "ds_read_b64_tr_b16 v[196:197], v234 offset:53248\n"
        "v_mfma_f32_16x16x32_bf16 v[164:167], v[204:207], v[12:15], 0\n"
        "ds_read_b64_tr_b16 v[198:199], v234 offset:53760\n"
        "s_waitcnt lgkmcnt(6)\n"
        "v_mfma_f32_16x16x32_bf16 v[168:171], v[208:211], v[8:11], 0\n"
        "ds_read_b64_tr_b16 v[200:201], v235 offset:53248\n"
        "v_mfma_f32_16x16x32_bf16 v[176:179], v[208:211], v[12:15], 0\n"
        "ds_read_b64_tr_b16 v[202:203], v235 offset:53760\n"
        "v_mfma_f32_16x16x32_bf16 v[172:175], v[184:187], v[8:11], 0\n"
        "ds_read_b64_tr_b16 v[204:205], v232 offset:61440\n"
        "v_mfma_f32_16x16x32_bf16 v[180:183], v[184:187], v[12:15], 0\n"
        "ds_read_b64_tr_b16 v[206:207], v232 offset:61952\n"
        "s_waitcnt lgkmcnt(6)\n"
        "v_mfma_f32_16x16x32_bf16 v[120:123], v[188:191], v[16:19], v[120:123]\n"
        "ds_read_b64_tr_b16 v[208:209], v233 offset:61440\n"
        "v_mfma_f32_16x16x32_bf16 v[128:131], v[188:191], v[20:23], v[128:131]\n"
        "ds_read_b64_tr_b16 v[210:211], v233 offset:61952\n"
        "v_mfma_f32_16x16x32_bf16 v[124:127], v[192:195], v[16:19], v[124:127]\n"
        "ds_read_b64_tr_b16 v[184:185], v234 offset:61440\n"
        "v_mfma_f32_16x16x32_bf16 v[132:135], v[192:195], v[20:23], v[132:135]\n"
        "ds_read_b64_tr_b16 v[186:187], v234 offset:61952\n"
        "s_waitcnt lgkmcnt(6)\n"
        "v_mfma_f32_16x16x32_bf16 v[136:139], v[196:199], v[16:19], v[136:139]\n"
        "ds_read_b64_tr_b16 v[188:189], v235 offset:61440\n"
        "v_mfma_f32_16x16x32_bf16 v[144:147], v[196:199], v[20:23], v[144:147]\n"
        "ds_read_b64_tr_b16 v[190:191], v235 offset:61952\n"
        "v_mfma_f32_16x16x32_bf16 v[140:143], v[200:203], v[16:19], v[140:143]\n"
        "v_xor_b32 v232, 0x10000, v230\n"
        "ds_read_b64_tr_b16 v[192:193], v232 offset:0\n"
        "v_mfma_f32_16x16x32_bf16 v[148:151], v[200:203], v[20:23], v[148:151]\n"
        "ds_read_b64_tr_b16 v[194:195], v232 offset:512\n"
        "s_waitcnt lgkmcnt(6)\n"
        "v_mfma_f32_16x16x32_bf16 v[152:155], v[204:207], v[16:19], v[152:155]\n"
        "v_xor_b32 v233, 0x10020, v230\n"
        "ds_read_b64_tr_b16 v[196:197], v233 offset:0\n"
        "v_mfma_f32_16x16x32_bf16 v[160:163], v[204:207], v[20:23], v[160:163]\n"
        "ds_read_b64_tr_b16 v[198:199], v233 offset:512\n"
        "v_mfma_f32_16x16x32_bf16 v[156:159], v[208:211], v[16:19], v[156:159]\n"
        "v_xor_b32 v234, 0x10040, v230\n"
        "ds_read_b64_tr_b16 v[200:201], v234 offset:0\n"
        "v_mfma_f32_16x16x32_bf16 v[164:167], v[208:211], v[20:23], v[164:167]\n"
        "ds_read_b64_tr_b16 v[202:203], v234 offset:512\n"
        "s_waitcnt lgkmcnt(6)\n"
        "v_mfma_f32_16x16x32_bf16 v[168:171], v[184:187], v[16:19], v[168:171]\n"
        "v_xor_b32 v235, 0x10060, v230\n"
        "ds_read_b64_tr_b16 v[204:205], v235 offset:0\n"
        "v_mfma_f32_16x16x32_bf16 v[176:179], v[184:187], v[20:23], v[176:179]\n"
        "ds_read_b64_tr_b16 v[206:207], v235 offset:512\n"
        "v_mfma_f32_16x16x32_bf16 v[172:175], v[188:191], v[16:19], v[172:175]\n"
        "ds_read_b64_tr_b16 v[208:209], v232 offset:8192\n"
        "v_mfma_f32_16x16x32_bf16 v[180:183], v[188:191], v[20:23], v[180:183]\n"
        "ds_read_b64_tr_b16 v[210:211], v232 offset:8704\n"
        "s_waitcnt lgkmcnt(6)\n"
        "v_mfma_f32_16x16x32_bf16 v[120:123], v[192:195], v[24:27], v[120:123]\n"
        "ds_read_b64_tr_b16 v[184:185], v233 offset:8192\n"
        "v_mfma_f32_16x16x32_bf16 v[128:131], v[192:195], v[28:31], v[128:131]\n"
        "ds_read_b64_tr_b16 v[186:187], v233 offset:8704\n"
        "v_mfma_f32_16x16x32_bf16 v[124:127], v[196:199], v[24:27], v[124:127]\n"
        "ds_read_b64_tr_b16 v[188:189], v234 offset:8192\n"
        "v_mfma_f32_16x16x32_bf16 v[132:135], v[196:199], v[28:31], v[132:135]\n"
        "ds_read_b64_tr_b16 v[190:191], v234 offset:8704\n"
        "s_waitcnt lgkmcnt(6)\n"
        "v_mfma_f32_16x16x32_bf16 v[136:139], v[200:203], v[24:27], v[136:139]\n"
        "ds_read_b64_tr_b16 v[192:193], v235 offset:8192\n"
        "v_mfma_f32_16x16x32_bf16 v[144:147], v[200:203], v[28:31], v[144:147]\n"
        "ds_read_b64_tr_b16 v[194:195], v235 offset:8704\n"
        "v_mfma_f32_16x16x32_bf16 v[140:143], v[204:207], v[24:27], v[140:143]\n"
        "ds_read_b64_tr_b16 v[196:197], v232 offset:4096\n"
        "v_mfma_f32_16x16x32_bf16 v[148:151], v[204:207], v[28:31], v[148:151]\n"
        "ds_read_b64_tr_b16 v[198:199], v232 offset:4608\n"
        "s_waitcnt lgkmcnt(6)\n"
        "v_mfma_f32_16x16x32_bf16 v[152:155], v[208:211], v[24:27], v[152:155]\n"
        "ds_read_b64_tr_b16 v[200:201], v233 offset:4096\n"
        "v_mfma_f32_16x16x32_bf16 v[160:163], v[208:211], v[28:31], v[160:163]\n"
        "ds_read_b64_tr_b16 v[202:203], v233 offset:4608\n"
        "v_mfma_f32_16x16x32_bf16 v[156:159], v[184:187], v[24:27], v[156:159]\n"
        "ds_read_b64_tr_b16 v[204:205], v234 offset:4096\n"
        "v_mfma_f32_16x16x32_bf16 v[164:167], v[184:187], v[28:31], v[164:167]\n"
        "ds_read_b64_tr_b16 v[206:207], v234 offset:4608\n"
        "s_waitcnt lgkmcnt(6)\n"
        "v_mfma_f32_16x16x32_bf16 v[168:171], v[188:191], v[24:27], v[168:171]\n"
        "ds_read_b64_tr_b16 v[208:209], v235 offset:4096\n"
        "v_mfma_f32_16x16x32_bf16 v[176:179], v[188:191], v[28:31], v[176:179]\n"
        "ds_read_b64_tr_b16 v[210:211], v235 offset:4608\n"
        "v_mfma_f32_16x16x32_bf16 v[172:175], v[192:195], v[24:27], v[172:175]\n"
        "ds_read_b64_tr_b16 v[184:185], v232 offset:12288\n"
        "v_mfma_f32_16x16x32_bf16 v[180:183], v[192:195], v[28:31], v[180:183]\n"
        "ds_read_b64_tr_b16 v[186:187], v232 offset:12800\n"
        "s_waitcnt lgkmcnt(6)\n"
        "v_mfma_f32_16x16x32_bf16 v[120:123], v[196:199], v[32:35], v[120:123]\n"
        "ds_read_b64_tr_b16 v[188:189], v233 offset:12288\n"
        "v_mfma_f32_16x16x32_bf16 v[128:131], v[196:199], v[36:39], v[128:131]\n"
        "ds_read_b64_tr_b16 v[190:191], v233 offset:12800\n"
        "v_mfma_f32_16x16x32_bf16 v[124:127], v[200:203], v[32:35], v[124:127]\n"
        "ds_read_b64_tr_b16 v[192:193], v234 offset:12288\n"
        "v_mfma_f32_16x16x32_bf16 v[132:135], v[200:203], v[36:39], v[132:135]\n"
        "ds_read_b64_tr_b16 v[194:195], v234 offset:12800\n"
        "s_waitcnt lgkmcnt(6)\n"
        "v_mfma_f32_16x16x32_bf16 v[136:139], v[204:207], v[32:35], v[136:139]\n"
        "ds_read_b64_tr_b16 v[196:197], v235 offset:12288\n"
        "v_mfma_f32_16x16x32_bf16 v[144:147], v[204:207], v[36:39], v[144:147]\n"
        "ds_read_b64_tr_b16 v[198:199], v235 offset:12800\n"
        "v_mfma_f32_16x16x32_bf16 v[140:143], v[208:211], v[32:35], v[140:143]\n"
        "ds_read_b64_tr_b16 v[200:201], v232 offset:16384\n"
        "v_mfma_f32_16x16x32_bf16 v[148:151], v[208:211], v[36:39], v[148:151]\n"
        "ds_read_b64_tr_b16 v[202:203], v232 offset:16896\n"
        "s_waitcnt lgkmcnt(6)\n"
        "v_mfma_f32_16x16x32_bf16 v[152:155], v[184:187], v[32:35], v[152:155]\n"
        "ds_read_b64_tr_b16 v[204:205], v233 offset:16384\n"
        "v_mfma_f32_16x16x32_bf16 v[160:163], v[184:187], v[36:39], v[160:163]\n"
        "ds_read_b64_tr_b16 v[206:207], v233 offset:16896\n"
        "v_mfma_f32_16x16x32_bf16 v[156:159], v[188:191], v[32:35], v[156:159]\n"
        "ds_read_b64_tr_b16 v[208:209], v234 offset:16384\n"
        "v_mfma_f32_16x16x32_bf16 v[164:167], v[188:191], v[36:39], v[164:167]\n"
        "ds_read_b64_tr_b16 v[210:211], v234 offset:16896\n"
        "s_waitcnt lgkmcnt(6)\n"
        "v_mfma_f32_16x16x32_bf16 v[168:171], v[192:195], v[32:35], v[168:171]\n"
        "ds_read_b64_tr_b16 v[184:185], v235 offset:16384\n"
        "v_mfma_f32_16x16x32_bf16 v[176:179], v[192:195], v[36:39], v[176:179]\n"
        "ds_read_b64_tr_b16 v[186:187], v235 offset:16896\n"
        "v_mfma_f32_16x16x32_bf16 v[172:175], v[196:199], v[32:35], v[172:175]\n"
        "ds_read_b64_tr_b16 v[188:189], v232 offset:24576\n"
        "v_mfma_f32_16x16x32_bf16 v[180:183], v[196:199], v[36:39], v[180:183]\n"
        "ds_read_b64_tr_b16 v[190:191], v232 offset:25088\n"
        "s_waitcnt vmcnt(0)\n"
        "s_barrier\n"
        "s_waitcnt lgkmcnt(6)\n"
        "v_mfma_f32_16x16x32_bf16 v[120:123], v[200:203], v[40:43], v[120:123]\n"
        "ds_read_b64_tr_b16 v[192:193], v233 offset:24576\n"
        "v_mfma_f32_16x16x32_bf16 v[128:131], v[200:203], v[44:47], v[128:131]\n"
        "ds_read_b64_tr_b16 v[194:195], v233 offset:25088\n"
        "v_mfma_f32_16x16x32_bf16 v[124:127], v[204:207], v[40:43], v[124:127]\n"
        "ds_read_b64_tr_b16 v[196:197], v234 offset:24576\n"
        "v_mfma_f32_16x16x32_bf16 v[132:135], v[204:207], v[44:47], v[132:135]\n"
        "ds_read_b64_tr_b16 v[198:199], v234 offset:25088\n"
        "s_waitcnt lgkmcnt(6)\n"
        "v_mfma_f32_16x16x32_bf16 v[136:139], v[208:211], v[40:43], v[136:139]\n"
        "ds_read_b64_tr_b16 v[200:201], v235 offset:24576\n"
        "v_mfma_f32_16x16x32_bf16 v[144:147], v[208:211], v[44:47], v[144:147]\n"
        "ds_read_b64_tr_b16 v[202:203], v235 offset:25088\n"
        "v_mfma_f32_16x16x32_bf16 v[140:143], v[184:187], v[40:43], v[140:143]\n"
        "ds_read_b64_tr_b16 v[204:205], v232 offset:20480\n"
        "v_mfma_f32_16x16x32_bf16 v[148:151], v[184:187], v[44:47], v[148:151]\n"
        "ds_read_b64_tr_b16 v[206:207], v232 offset:20992\n"
        "s_waitcnt lgkmcnt(6)\n"
        "v_mfma_f32_16x16x32_bf16 v[152:155], v[188:191], v[40:43], v[152:155]\n"
        "ds_read_b64_tr_b16 v[208:209], v233 offset:20480\n"
        "v_mfma_f32_16x16x32_bf16 v[160:163], v[188:191], v[44:47], v[160:163]\n"
        "ds_read_b64_tr_b16 v[210:211], v233 offset:20992\n"
        "v_mfma_f32_16x16x32_bf16 v[156:159], v[192:195], v[40:43], v[156:159]\n"
        "ds_read_b64_tr_b16 v[184:185], v234 offset:20480\n"
        "v_mfma_f32_16x16x32_bf16 v[164:167], v[192:195], v[44:47], v[164:167]\n"
        "ds_read_b64_tr_b16 v[186:187], v234 offset:20992\n"
        "s_waitcnt lgkmcnt(6)\n"
        "v_mfma_f32_16x16x32_bf16 v[168:171], v[196:199], v[40:43], v[168:171]\n"
        "ds_read_b64_tr_b16 v[188:189], v235 offset:20480\n"
        "v_mfma_f32_16x16x32_bf16 v[176:179], v[196:199], v[44:47], v[176:179]\n"
        "ds_read_b64_tr_b16 v[190:191], v235 offset:20992\n"
        "v_mfma_f32_16x16x32_bf16 v[172:175], v[200:203], v[40:43], v[172:175]\n"
        "ds_read_b64_tr_b16 v[192:193], v232 offset:28672\n"
        "v_mfma_f32_16x16x32_bf16 v[180:183], v[200:203], v[44:47], v[180:183]\n"
        "ds_read_b64_tr_b16 v[194:195], v232 offset:29184\n"
        "s_waitcnt lgkmcnt(6)\n"
        "v_mfma_f32_16x16x32_bf16 v[120:123], v[204:207], v[48:51], v[120:123]\n"
        "ds_read_b64_tr_b16 v[196:197], v233 offset:28672\n"
        "v_mfma_f32_16x16x32_bf16 v[128:131], v[204:207], v[52:55], v[128:131]\n"
        "ds_read_b64_tr_b16 v[198:199], v233 offset:29184\n"
        "v_mfma_f32_16x16x32_bf16 v[124:127], v[208:211], v[48:51], v[124:127]\n"
        "ds_read_b64_tr_b16 v[200:201], v234 offset:28672\n"
        "v_mfma_f32_16x16x32_bf16 v[132:135], v[208:211], v[52:55], v[132:135]\n"
        "ds_read_b64_tr_b16 v[202:203], v234 offset:29184\n"
        "s_waitcnt lgkmcnt(6)\n"
        "v_mfma_f32_16x16x32_bf16 v[136:139], v[184:187], v[48:51], v[136:139]\n"
        "ds_read_b64_tr_b16 v[204:205], v235 offset:28672\n"
        "v_mfma_f32_16x16x32_bf16 v[144:147], v[184:187], v[52:55], v[144:147]\n"
        "ds_read_b64_tr_b16 v[206:207], v235 offset:29184\n"
        "v_mfma_f32_16x16x32_bf16 v[140:143], v[188:191], v[48:51], v[140:143]\n"
        "v_xor_b32 v232, 0x0, v230\n"
        "ds_read_b64_tr_b16 v[208:209], v232 offset:0\n"
        "v_mfma_f32_16x16x32_bf16 v[148:151], v[188:191], v[52:55], v[148:151]\n"
        "ds_read_b64_tr_b16 v[210:211], v232 offset:512\n"
        "s_waitcnt lgkmcnt(6)\n"
        "v_mfma_f32_16x16x32_bf16 v[152:155], v[192:195], v[48:51], v[152:155]\n"
        "v_xor_b32 v233, 0x20, v230\n"
        "ds_read_b64_tr_b16 v[184:185], v233 offset:0\n"
        "v_mfma_f32_16x16x32_bf16 v[160:163], v[192:195], v[52:55], v[160:163]\n"
        "ds_read_b64_tr_b16 v[186:187], v233 offset:512\n"
        "v_mfma_f32_16x16x32_bf16 v[156:159], v[196:199], v[48:51], v[156:159]\n"
        "v_xor_b32 v234, 0x40, v230\n"
        "ds_read_b64_tr_b16 v[188:189], v234 offset:0\n"
        "v_mfma_f32_16x16x32_bf16 v[164:167], v[196:199], v[52:55], v[164:167]\n"
        "ds_read_b64_tr_b16 v[190:191], v234 offset:512\n"
        "s_waitcnt lgkmcnt(6)\n"
        "v_mfma_f32_16x16x32_bf16 v[168:171], v[200:203], v[48:51], v[168:171]\n"
        "v_xor_b32 v235, 0x60, v230\n"
        "ds_read_b64_tr_b16 v[192:193], v235 offset:0\n"
        "v_mfma_f32_16x16x32_bf16 v[176:179], v[200:203], v[52:55], v[176:179]\n"
        "ds_read_b64_tr_b16 v[194:195], v235 offset:512\n"
        "v_mfma_f32_16x16x32_bf16 v[172:175], v[204:207], v[48:51], v[172:175]\n"
        "ds_read_b64_tr_b16 v[196:197], v232 offset:8192\n"
        "v_mfma_f32_16x16x32_bf16 v[180:183], v[204:207], v[52:55], v[180:183]\n"
        "ds_read_b64_tr_b16 v[198:199], v232 offset:8704\n"
        "s_waitcnt lgkmcnt(6)\n"
        "v_mfma_f32_16x16x32_bf16 v[120:123], v[208:211], v[56:59], v[120:123]\n"
        "ds_read_b64_tr_b16 v[200:201], v233 offset:8192\n"
        "v_mfma_f32_16x16x32_bf16 v[128:131], v[208:211], v[60:63], v[128:131]\n"
        "ds_read_b64_tr_b16 v[202:203], v233 offset:8704\n"
        "v_mfma_f32_16x16x32_bf16 v[124:127], v[184:187], v[56:59], v[124:127]\n"
        "ds_read_b64_tr_b16 v[204:205], v234 offset:8192\n"
        "v_mfma_f32_16x16x32_bf16 v[132:135], v[184:187], v[60:63], v[132:135]\n"
        "ds_read_b64_tr_b16 v[206:207], v234 offset:8704\n"
        "s_waitcnt lgkmcnt(6)\n"
        "v_mfma_f32_16x16x32_bf16 v[136:139], v[188:191], v[56:59], v[136:139]\n"
        "ds_read_b64_tr_b16 v[208:209], v235 offset:8192\n"
        "v_mfma_f32_16x16x32_bf16 v[144:147], v[188:191], v[60:63], v[144:147]\n"
        "ds_read_b64_tr_b16 v[210:211], v235 offset:8704\n"
        "v_mfma_f32_16x16x32_bf16 v[140:143], v[192:195], v[56:59], v[140:143]\n"
        "ds_read_b64_tr_b16 v[184:185], v232 offset:4096\n"
        "v_mfma_f32_16x16x32_bf16 v[148:151], v[192:195], v[60:63], v[148:151]\n"
        "ds_read_b64_tr_b16 v[186:187], v232 offset:4608\n"
        "s_waitcnt lgkmcnt(6)\n"
        "v_mfma_f32_16x16x32_bf16 v[152:155], v[196:199], v[56:59], v[152:155]\n"
        "ds_read_b64_tr_b16 v[188:189], v233 offset:4096\n"
        "v_mfma_f32_16x16x32_bf16 v[160:163], v[196:199], v[60:63], v[160:163]\n"
        "ds_read_b64_tr_b16 v[190:191], v233 offset:4608\n"
        "v_mfma_f32_16x16x32_bf16 v[156:159], v[200:203], v[56:59], v[156:159]\n"
        "ds_read_b64_tr_b16 v[192:193], v234 offset:4096\n"
        "v_mfma_f32_16x16x32_bf16 v[164:167], v[200:203], v[60:63], v[164:167]\n"
        "ds_read_b64_tr_b16 v[194:195], v234 offset:4608\n"
        "s_waitcnt lgkmcnt(6)\n"
        "v_mfma_f32_16x16x32_bf16 v[168:171], v[204:207], v[56:59], v[168:171]\n"
        "ds_read_b64_tr_b16 v[196:197], v235 offset:4096\n"
        "v_mfma_f32_16x16x32_bf16 v[176:179], v[204:207], v[60:63], v[176:179]\n"
        "ds_read_b64_tr_b16 v[198:199], v235 offset:4608\n"
        "v_mfma_f32_16x16x32_bf16 v[172:175], v[208:211], v[56:59], v[172:175]\n"
        "ds_read_b64_tr_b16 v[200:201], v232 offset:12288\n"
        "v_mfma_f32_16x16x32_bf16 v[180:183], v[208:211], v[60:63], v[180:183]\n"
        "ds_read_b64_tr_b16 v[202:203], v232 offset:12800\n"
        "s_waitcnt lgkmcnt(6)\n"
        "v_mfma_f32_16x16x32_bf16 v[120:123], v[184:187], v[64:67], v[120:123]\n"
        "ds_read_b64_tr_b16 v[204:205], v233 offset:12288\n"
        "v_mfma_f32_16x16x32_bf16 v[128:131], v[184:187], v[68:71], v[128:131]\n"
        "ds_read_b64_tr_b16 v[206:207], v233 offset:12800\n"
        "v_mfma_f32_16x16x32_bf16 v[124:127], v[188:191], v[64:67], v[124:127]\n"
        "ds_read_b64_tr_b16 v[208:209], v234 offset:12288\n"
        "v_mfma_f32_16x16x32_bf16 v[132:135], v[188:191], v[68:71], v[132:135]\n"
        "ds_read_b64_tr_b16 v[210:211], v234 offset:12800\n"
        "s_waitcnt lgkmcnt(6)\n"
        "v_mfma_f32_16x16x32_bf16 v[136:139], v[192:195], v[64:67], v[136:139]\n"
        "ds_read_b64_tr_b16 v[184:185], v235 offset:12288\n"
        "v_mfma_f32_16x16x32_bf16 v[144:147], v[192:195], v[68:71], v[144:147]\n"
        "ds_read_b64_tr_b16 v[186:187], v235 offset:12800\n"
        "v_mfma_f32_16x16x32_bf16 v[140:143], v[196:199], v[64:67], v[140:143]\n"
        "v_xor_b32 v232, 0x10000, v230\n"
        "ds_read_b64_tr_b16 v[188:189], v232 offset:49152\n"
        "v_mfma_f32_16x16x32_bf16 v[148:151], v[196:199], v[68:71], v[148:151]\n"
        "ds_read_b64_tr_b16 v[190:191], v232 offset:49664\n"
        "s_waitcnt lgkmcnt(6)\n"
        "v_mfma_f32_16x16x32_bf16 v[152:155], v[200:203], v[64:67], v[152:155]\n"
        "v_xor_b32 v233, 0x10020, v230\n"
        "ds_read_b64_tr_b16 v[192:193], v233 offset:49152\n"
        "v_mfma_f32_16x16x32_bf16 v[160:163], v[200:203], v[68:71], v[160:163]\n"
        "ds_read_b64_tr_b16 v[194:195], v233 offset:49664\n"
        "v_mfma_f32_16x16x32_bf16 v[156:159], v[204:207], v[64:67], v[156:159]\n"
        "v_xor_b32 v234, 0x10040, v230\n"
        "ds_read_b64_tr_b16 v[196:197], v234 offset:49152\n"
        "v_mfma_f32_16x16x32_bf16 v[164:167], v[204:207], v[68:71], v[164:167]\n"
        "ds_read_b64_tr_b16 v[198:199], v234 offset:49664\n"
        "s_waitcnt lgkmcnt(6)\n"
        "v_mfma_f32_16x16x32_bf16 v[168:171], v[208:211], v[64:67], v[168:171]\n"
        "v_xor_b32 v235, 0x10060, v230\n"
        "ds_read_b64_tr_b16 v[200:201], v235 offset:49152\n"
        "v_mfma_f32_16x16x32_bf16 v[176:179], v[208:211], v[68:71], v[176:179]\n"
        "ds_read_b64_tr_b16 v[202:203], v235 offset:49664\n"
        "s_nop 0\n"
        "s_cbranch_execz Lback\n"
        "v_mfma_f32_16x16x32_bf16 v[172:175], v[184:187], v[64:67], v[172:175]\n"
        "ds_read_b64_tr_b16 v[204:205], v232 offset:57344\n"
        "v_mfma_f32_16x16x32_bf16 v[180:183], v[184:187], v[68:71], v[180:183]\n"
        "ds_read_b64_tr_b16 v[206:207], v232 offset:57856\n"
        "s_barrier\n"
        "s_waitcnt lgkmcnt(6)\n"
        "v_mfma_f32_16x16x32_bf16 v[120:123], v[188:191], v[72:75], v[120:123]\n"
        "ds_read_b64_tr_b16 v[208:209], v233 offset:57344\n"
        "v_mfma_f32_16x16x32_bf16 v[128:131], v[188:191], v[76:79], v[128:131]\n"
        "ds_read_b64_tr_b16 v[210:211], v233 offset:57856\n"
        "v_mfma_f32_16x16x32_bf16 v[124:127], v[192:195], v[72:75], v[124:127]\n"
        "ds_read_b64_tr_b16 v[184:185], v234 offset:57344\n"
        "v_mfma_f32_16x16x32_bf16 v[132:135], v[192:195], v[76:79], v[132:135]\n"
        "ds_read_b64_tr_b16 v[186:187], v234 offset:57856\n"
        "s_waitcnt lgkmcnt(6)\n"
        "v_mfma_f32_16x16x32_bf16 v[136:139], v[196:199], v[72:75], v[136:139]\n"
        "ds_read_b64_tr_b16 v[188:189], v235 offset:57344\n"
        "v_mfma_f32_16x16x32_bf16 v[144:147], v[196:199], v[76:79], v[144:147]\n"
        "ds_read_b64_tr_b16 v[190:191], v235 offset:57856\n"
        "v_mfma_f32_16x16x32_bf16 v[140:143], v[200:203], v[72:75], v[140:143]\n"
        "ds_read_b64_tr_b16 v[192:193], v232 offset:53248\n"
        "v_mfma_f32_16x16x32_bf16 v[148:151], v[200:203], v[76:79], v[148:151]\n"
        "ds_read_b64_tr_b16 v[194:195], v232 offset:53760\n"
        "s_waitcnt lgkmcnt(6)\n"
        "v_mfma_f32_16x16x32_bf16 v[152:155], v[204:207], v[72:75], v[152:155]\n"
        "ds_read_b64_tr_b16 v[196:197], v233 offset:53248\n"
        "v_mfma_f32_16x16x32_bf16 v[160:163], v[204:207], v[76:79], v[160:163]\n"
        "ds_read_b64_tr_b16 v[198:199], v233 offset:53760\n"
        "v_mfma_f32_16x16x32_bf16 v[156:159], v[208:211], v[72:75], v[156:159]\n"
        "ds_read_b64_tr_b16 v[200:201], v234 offset:53248\n"
        "v_mfma_f32_16x16x32_bf16 v[164:167], v[208:211], v[76:79], v[164:167]\n"
        "ds_read_b64_tr_b16 v[202:203], v234 offset:53760\n"
        "s_waitcnt lgkmcnt(6)\n"
        "v_mfma_f32_16x16x32_bf16 v[168:171], v[184:187], v[72:75], v[168:171]\n"
        "ds_read_b64_tr_b16 v[204:205], v235 offset:53248\n"
        "v_mfma_f32_16x16x32_bf16 v[176:179], v[184:187], v[76:79], v[176:179]\n"
        "ds_read_b64_tr_b16 v[206:207], v235 offset:53760\n"
        "v_mfma_f32_16x16x32_bf16 v[172:175], v[188:191], v[72:75], v[172:175]\n"
        "ds_read_b64_tr_b16 v[208:209], v232 offset:61440\n"
        "v_mfma_f32_16x16x32_bf16 v[180:183], v[188:191], v[76:79], v[180:183]\n"
        "ds_read_b64_tr_b16 v[210:211], v232 offset:61952\n"
        "s_waitcnt lgkmcnt(6)\n"
        "v_mfma_f32_16x16x32_bf16 v[120:123], v[192:195], v[80:83], v[120:123]\n"
        "ds_read_b64_tr_b16 v[184:185], v233 offset:61440\n"
        "v_mfma_f32_16x16x32_bf16 v[128:131], v[192:195], v[84:87], v[128:131]\n"
        "ds_read_b64_tr_b16 v[186:187], v233 offset:61952\n"
        "v_mfma_f32_16x16x32_bf16 v[124:127], v[196:199], v[80:83], v[124:127]\n"
        "ds_read_b64_tr_b16 v[188:189], v234 offset:61440\n"
        "v_mfma_f32_16x16x32_bf16 v[132:135], v[196:199], v[84:87], v[132:135]\n"
        "ds_read_b64_tr_b16 v[190:191], v234 offset:61952\n"
        "s_waitcnt lgkmcnt(6)\n"
        "v_mfma_f32_16x16x32_bf16 v[136:139], v[200:203], v[80:83], v[136:139]\n"
        "ds_read_b64_tr_b16 v[192:193], v235 offset:61440\n"
        "v_mfma_f32_16x16x32_bf16 v[144:147], v[200:203], v[84:87], v[144:147]\n"
        "ds_read_b64_tr_b16 v[194:195], v235 offset:61952\n"
        "v_mfma_f32_16x16x32_bf16 v[140:143], v[204:207], v[80:83], v[140:143]\n"
        "v_xor_b32 v232, 0x20000, v230\n"
        "ds_read_b64_tr_b16 v[196:197], v232 offset:0\n"
        "v_mfma_f32_16x16x32_bf16 v[148:151], v[204:207], v[84:87], v[148:151]\n"
        "ds_read_b64_tr_b16 v[198:199], v232 offset:512\n"
        "s_waitcnt lgkmcnt(6)\n"
        "v_mfma_f32_16x16x32_bf16 v[152:155], v[208:211], v[80:83], v[152:155]\n"
        "v_xor_b32 v233, 0x20020, v230\n"
        "ds_read_b64_tr_b16 v[200:201], v233 offset:0\n"
        "v_mfma_f32_16x16x32_bf16 v[160:163], v[208:211], v[84:87], v[160:163]\n"
        "ds_read_b64_tr_b16 v[202:203], v233 offset:512\n"
        "v_mfma_f32_16x16x32_bf16 v[156:159], v[184:187], v[80:83], v[156:159]\n"
        "v_xor_b32 v234, 0x20040, v230\n"
        "ds_read_b64_tr_b16 v[204:205], v234 offset:0\n"
        "v_mfma_f32_16x16x32_bf16 v[164:167], v[184:187], v[84:87], v[164:167]\n"
        "ds_read_b64_tr_b16 v[206:207], v234 offset:512\n"
        "s_waitcnt lgkmcnt(6)\n"
        "v_mfma_f32_16x16x32_bf16 v[168:171], v[188:191], v[80:83], v[168:171]\n"
        "v_xor_b32 v235, 0x20060, v230\n"
        "ds_read_b64_tr_b16 v[208:209], v235 offset:0\n"
        "v_mfma_f32_16x16x32_bf16 v[176:179], v[188:191], v[84:87], v[176:179]\n"
        "ds_read_b64_tr_b16 v[210:211], v235 offset:512\n"
        "v_mfma_f32_16x16x32_bf16 v[172:175], v[192:195], v[80:83], v[172:175]\n"
        "ds_read_b64_tr_b16 v[184:185], v232 offset:8192\n"
        "v_mfma_f32_16x16x32_bf16 v[180:183], v[192:195], v[84:87], v[180:183]\n"
        "ds_read_b64_tr_b16 v[186:187], v232 offset:8704\n"
        "s_waitcnt lgkmcnt(6)\n"
        "v_mfma_f32_16x16x32_bf16 v[120:123], v[196:199], v[88:91], v[120:123]\n"
        "ds_read_b64_tr_b16 v[188:189], v233 offset:8192\n"
        "v_mfma_f32_16x16x32_bf16 v[128:131], v[196:199], v[92:95], v[128:131]\n"
        "ds_read_b64_tr_b16 v[190:191], v233 offset:8704\n"
        "v_mfma_f32_16x16x32_bf16 v[124:127], v[200:203], v[88:91], v[124:127]\n"
        "ds_read_b64_tr_b16 v[192:193], v234 offset:8192\n"
        "v_mfma_f32_16x16x32_bf16 v[132:135], v[200:203], v[92:95], v[132:135]\n"
        "ds_read_b64_tr_b16 v[194:195], v234 offset:8704\n"
        "s_waitcnt lgkmcnt(6)\n"
        "v_mfma_f32_16x16x32_bf16 v[136:139], v[204:207], v[88:91], v[136:139]\n"
        "ds_read_b64_tr_b16 v[196:197], v235 offset:8192\n"
        "v_mfma_f32_16x16x32_bf16 v[144:147], v[204:207], v[92:95], v[144:147]\n"
        "ds_read_b64_tr_b16 v[198:199], v235 offset:8704\n"
        "v_mfma_f32_16x16x32_bf16 v[140:143], v[208:211], v[88:91], v[140:143]\n"
        "ds_read_b64_tr_b16 v[200:201], v232 offset:4096\n"
        "v_mfma_f32_16x16x32_bf16 v[148:151], v[208:211], v[92:95], v[148:151]\n"
        "ds_read_b64_tr_b16 v[202:203], v232 offset:4608\n"
        "s_waitcnt lgkmcnt(6)\n"
        "v_mfma_f32_16x16x32_bf16 v[152:155], v[184:187], v[88:91], v[152:155]\n"
        "ds_read_b64_tr_b16 v[204:205], v233 offset:4096\n"
        "v_mfma_f32_16x16x32_bf16 v[160:163], v[184:187], v[92:95], v[160:163]\n"
        "ds_read_b64_tr_b16 v[206:207], v233 offset:4608\n"
        "v_mfma_f32_16x16x32_bf16 v[156:159], v[188:191], v[88:91], v[156:159]\n"
        "ds_read_b64_tr_b16 v[208:209], v234 offset:4096\n"
        "v_mfma_f32_16x16x32_bf16 v[164:167], v[188:191], v[92:95], v[164:167]\n"
        "ds_read_b64_tr_b16 v[210:211], v234 offset:4608\n"
        "s_waitcnt lgkmcnt(6)\n"
        "v_mfma_f32_16x16x32_bf16 v[168:171], v[192:195], v[88:91], v[168:171]\n"
        "ds_read_b64_tr_b16 v[184:185], v235 offset:4096\n"
        "v_mfma_f32_16x16x32_bf16 v[176:179], v[192:195], v[92:95], v[176:179]\n"
        "ds_read_b64_tr_b16 v[186:187], v235 offset:4608\n"
        "v_mfma_f32_16x16x32_bf16 v[172:175], v[196:199], v[88:91], v[172:175]\n"
        "ds_read_b64_tr_b16 v[188:189], v232 offset:12288\n"
        "v_mfma_f32_16x16x32_bf16 v[180:183], v[196:199], v[92:95], v[180:183]\n"
        "ds_read_b64_tr_b16 v[190:191], v232 offset:12800\n"
        "s_waitcnt lgkmcnt(6)\n"
        "v_mfma_f32_16x16x32_bf16 v[120:123], v[200:203], v[96:99], v[120:123]\n"
        "ds_read_b64_tr_b16 v[192:193], v233 offset:12288\n"
        "v_mfma_f32_16x16x32_bf16 v[128:131], v[200:203], v[100:103], v[128:131]\n"
        "ds_read_b64_tr_b16 v[194:195], v233 offset:12800\n"
        "v_mfma_f32_16x16x32_bf16 v[124:127], v[204:207], v[96:99], v[124:127]\n"
        "ds_read_b64_tr_b16 v[196:197], v234 offset:12288\n"
        "v_mfma_f32_16x16x32_bf16 v[132:135], v[204:207], v[100:103], v[132:135]\n"
        "ds_read_b64_tr_b16 v[198:199], v234 offset:12800\n"
        "s_waitcnt lgkmcnt(6)\n"
        "v_mfma_f32_16x16x32_bf16 v[136:139], v[208:211], v[96:99], v[136:139]\n"
        "ds_read_b64_tr_b16 v[200:201], v235 offset:12288\n"
        "v_mfma_f32_16x16x32_bf16 v[144:147], v[208:211], v[100:103], v[144:147]\n"
        "ds_read_b64_tr_b16 v[202:203], v235 offset:12800\n"
        "v_mfma_f32_16x16x32_bf16 v[140:143], v[184:187], v[96:99], v[140:143]\n"
        "ds_read_b64_tr_b16 v[204:205], v232 offset:16384\n"
        "v_mfma_f32_16x16x32_bf16 v[148:151], v[184:187], v[100:103], v[148:151]\n"
        "ds_read_b64_tr_b16 v[206:207], v232 offset:16896\n"
        "s_waitcnt lgkmcnt(6)\n"
        "v_mfma_f32_16x16x32_bf16 v[152:155], v[188:191], v[96:99], v[152:155]\n"
        "ds_read_b64_tr_b16 v[208:209], v233 offset:16384\n"
        "v_mfma_f32_16x16x32_bf16 v[160:163], v[188:191], v[100:103], v[160:163]\n"
        "ds_read_b64_tr_b16 v[210:211], v233 offset:16896\n"
        "v_mfma_f32_16x16x32_bf16 v[156:159], v[192:195], v[96:99], v[156:159]\n"
        "ds_read_b64_tr_b16 v[184:185], v234 offset:16384\n"
        "v_mfma_f32_16x16x32_bf16 v[164:167], v[192:195], v[100:103], v[164:167]\n"
        "ds_read_b64_tr_b16 v[186:187], v234 offset:16896\n"
        "s_waitcnt lgkmcnt(6)\n"
        "v_mfma_f32_16x16x32_bf16 v[168:171], v[196:199], v[96:99], v[168:171]\n"
        "ds_read_b64_tr_b16 v[188:189], v235 offset:16384\n"
        "v_mfma_f32_16x16x32_bf16 v[176:179], v[196:199], v[100:103], v[176:179]\n"
        "ds_read_b64_tr_b16 v[190:191], v235 offset:16896\n"
        "v_mfma_f32_16x16x32_bf16 v[172:175], v[200:203], v[96:99], v[172:175]\n"
        "ds_read_b64_tr_b16 v[192:193], v232 offset:24576\n"
        "v_mfma_f32_16x16x32_bf16 v[180:183], v[200:203], v[100:103], v[180:183]\n"
        "ds_read_b64_tr_b16 v[194:195], v232 offset:25088\n"
        "s_barrier\n"
        "s_waitcnt lgkmcnt(6)\n"
        "v_mfma_f32_16x16x32_bf16 v[120:123], v[204:207], v[104:107], v[120:123]\n"
        "ds_read_b64_tr_b16 v[196:197], v233 offset:24576\n"
        "v_mfma_f32_16x16x32_bf16 v[128:131], v[204:207], v[108:111], v[128:131]\n"
        "ds_read_b64_tr_b16 v[198:199], v233 offset:25088\n"
        "v_mfma_f32_16x16x32_bf16 v[124:127], v[208:211], v[104:107], v[124:127]\n"
        "ds_read_b64_tr_b16 v[200:201], v234 offset:24576\n"
        "v_mfma_f32_16x16x32_bf16 v[132:135], v[208:211], v[108:111], v[132:135]\n"
        "ds_read_b64_tr_b16 v[202:203], v234 offset:25088\n"
        "s_waitcnt lgkmcnt(6)\n"
        "v_mfma_f32_16x16x32_bf16 v[136:139], v[184:187], v[104:107], v[136:139]\n"
        "ds_read_b64_tr_b16 v[204:205], v235 offset:24576\n"
        "v_mfma_f32_16x16x32_bf16 v[144:147], v[184:187], v[108:111], v[144:147]\n"
        "ds_read_b64_tr_b16 v[206:207], v235 offset:25088\n"
        "v_mfma_f32_16x16x32_bf16 v[140:143], v[188:191], v[104:107], v[140:143]\n"
        "ds_read_b64_tr_b16 v[208:209], v232 offset:20480\n"
        "v_mfma_f32_16x16x32_bf16 v[148:151], v[188:191], v[108:111], v[148:151]\n"
        "ds_read_b64_tr_b16 v[210:211], v232 offset:20992\n"
        "s_waitcnt lgkmcnt(6)\n"
        "v_mfma_f32_16x16x32_bf16 v[152:155], v[192:195], v[104:107], v[152:155]\n"
        "ds_read_b64_tr_b16 v[184:185], v233 offset:20480\n"
        "v_mfma_f32_16x16x32_bf16 v[160:163], v[192:195], v[108:111], v[160:163]\n"
        "ds_read_b64_tr_b16 v[186:187], v233 offset:20992\n"
        "v_mfma_f32_16x16x32_bf16 v[156:159], v[196:199], v[104:107], v[156:159]\n"
        "ds_read_b64_tr_b16 v[188:189], v234 offset:20480\n"
        "v_mfma_f32_16x16x32_bf16 v[164:167], v[196:199], v[108:111], v[164:167]\n"
        "ds_read_b64_tr_b16 v[190:191], v234 offset:20992\n"
        "s_waitcnt lgkmcnt(6)\n"
        "v_mfma_f32_16x16x32_bf16 v[168:171], v[200:203], v[104:107], v[168:171]\n"
        "ds_read_b64_tr_b16 v[192:193], v235 offset:20480\n"
        "v_mfma_f32_16x16x32_bf16 v[176:179], v[200:203], v[108:111], v[176:179]\n"
        "ds_read_b64_tr_b16 v[194:195], v235 offset:20992\n"
        "v_mfma_f32_16x16x32_bf16 v[172:175], v[204:207], v[104:107], v[172:175]\n"
        "ds_read_b64_tr_b16 v[196:197], v232 offset:28672\n"
        "v_mfma_f32_16x16x32_bf16 v[180:183], v[204:207], v[108:111], v[180:183]\n"
        "ds_read_b64_tr_b16 v[198:199], v232 offset:29184\n"
        "s_waitcnt lgkmcnt(6)\n"
        "v_mfma_f32_16x16x32_bf16 v[120:123], v[208:211], v[112:115], v[120:123]\n"
        "ds_read_b64_tr_b16 v[200:201], v233 offset:28672\n"
        "v_mfma_f32_16x16x32_bf16 v[128:131], v[208:211], v[116:119], v[128:131]\n"
        "ds_read_b64_tr_b16 v[202:203], v233 offset:29184\n"
        "v_mfma_f32_16x16x32_bf16 v[124:127], v[184:187], v[112:115], v[124:127]\n"
        "ds_read_b64_tr_b16 v[204:205], v234 offset:28672\n"
        "v_mfma_f32_16x16x32_bf16 v[132:135], v[184:187], v[116:119], v[132:135]\n"
        "ds_read_b64_tr_b16 v[206:207], v234 offset:29184\n"
        "s_waitcnt lgkmcnt(6)\n"
        "v_mfma_f32_16x16x32_bf16 v[136:139], v[188:191], v[112:115], v[136:139]\n"
        "ds_read_b64_tr_b16 v[208:209], v235 offset:28672\n"
        "v_mfma_f32_16x16x32_bf16 v[144:147], v[188:191], v[116:119], v[144:147]\n"
        "ds_read_b64_tr_b16 v[210:211], v235 offset:29184\n"
        "v_mfma_f32_16x16x32_bf16 v[140:143], v[192:195], v[112:115], v[140:143]\n"
        "v_mfma_f32_16x16x32_bf16 v[148:151], v[192:195], v[116:119], v[148:151]\n"
        "s_waitcnt lgkmcnt(4)\n"
        "v_mfma_f32_16x16x32_bf16 v[152:155], v[196:199], v[112:115], v[152:155]\n"
        "v_mfma_f32_16x16x32_bf16 v[160:163], v[196:199], v[116:119], v[160:163]\n"
        "v_mfma_f32_16x16x32_bf16 v[156:159], v[200:203], v[112:115], v[156:159]\n"
        "v_mfma_f32_16x16x32_bf16 v[164:167], v[200:203], v[116:119], v[164:167]\n"
        "s_waitcnt lgkmcnt(0)\n"
        "v_mfma_f32_16x16x32_bf16 v[168:171], v[204:207], v[112:115], v[168:171]\n"
        "v_mfma_f32_16x16x32_bf16 v[176:179], v[204:207], v[116:119], v[176:179]\n"
        "v_mfma_f32_16x16x32_bf16 v[172:175], v[208:211], v[112:115], v[172:175]\n"
        "v_mfma_f32_16x16x32_bf16 v[180:183], v[208:211], v[116:119], v[180:183]\n"
        "s_barrier\n"
        "v_pk_mul_f32 v[216:217], v[120:121], v[120:121]\n"
        "v_pk_mul_f32 v[218:219], v[122:123], v[122:123]\n"
        "v_pk_mul_f32 v[220:221], v[124:125], v[124:125]\n"
        "v_pk_mul_f32 v[222:223], v[126:127], v[126:127]\n"
        "v_pk_fma_f32 v[216:217], v[216:217], s[86:87], v[6:7]\n"
        "v_pk_fma_f32 v[218:219], v[218:219], s[86:87], v[6:7]\n"
        "v_pk_fma_f32 v[220:221], v[220:221], s[86:87], v[6:7]\n"
        "v_pk_fma_f32 v[222:223], v[222:223], s[86:87], v[6:7]\n"
        "v_pk_mul_f32 v[216:217], v[216:217], v[120:121]\n"
        "v_pk_mul_f32 v[218:219], v[218:219], v[122:123]\n"
        "v_pk_mul_f32 v[220:221], v[220:221], v[124:125]\n"
        "v_pk_mul_f32 v[222:223], v[222:223], v[126:127]\n"
        "v_exp_f32 v216, v216\n"
        "v_exp_f32 v217, v217\n"
        "v_pk_mul_f32 v[152:153], v[152:153], v[120:121]\n"
        "v_exp_f32 v218, v218\n"
        "v_exp_f32 v219, v219\n"
        "v_pk_mul_f32 v[154:155], v[154:155], v[122:123]\n"
        "v_exp_f32 v220, v220\n"
        "v_exp_f32 v221, v221\n"
        "v_pk_mul_f32 v[156:157], v[156:157], v[124:125]\n"
        "v_exp_f32 v222, v222\n"
        "v_exp_f32 v223, v223\n"
        "v_pk_mul_f32 v[158:159], v[158:159], v[126:127]\n"
        "v_pk_add_f32 v[216:217], v[216:217], s[88:89]\n"
        "v_pk_add_f32 v[218:219], v[218:219], s[88:89]\n"
        "v_pk_add_f32 v[220:221], v[220:221], s[88:89]\n"
        "v_pk_add_f32 v[222:223], v[222:223], s[88:89]\n"
        "v_rcp_f32 v216, v216\n"
        "v_rcp_f32 v217, v217\n"
        "v_rcp_f32 v218, v218\n"
        "v_rcp_f32 v219, v219\n"
        "v_rcp_f32 v220, v220\n"
        "v_rcp_f32 v221, v221\n"
        "v_rcp_f32 v222, v222\n"
        "v_rcp_f32 v223, v223\n"
        "v_pk_mul_f32 v[152:153], v[152:153], v[216:217]\n"
        "v_pk_mul_f32 v[154:155], v[154:155], v[218:219]\n"
        "v_pk_mul_f32 v[156:157], v[156:157], v[220:221]\n"
        "v_pk_mul_f32 v[158:159], v[158:159], v[222:223]\n"
        "v_cvt_pk_bf16_f32 v120, v152, v153\n"
        "v_cvt_pk_bf16_f32 v121, v154, v155\n"
        "v_cvt_pk_bf16_f32 v122, v156, v157\n"
        "v_cvt_pk_bf16_f32 v123, v158, v159\n"
        "buffer_store_dwordx4 v[120:123], v236, s[60:63], s82 offen offset:0 sc1\n"
        "v_pk_mul_f32 v[216:217], v[128:129], v[128:129]\n"
        "v_pk_mul_f32 v[218:219], v[130:131], v[130:131]\n"
        "v_pk_mul_f32 v[220:221], v[132:133], v[132:133]\n"
        "v_pk_mul_f32 v[222:223], v[134:135], v[134:135]\n"
        "v_pk_fma_f32 v[216:217], v[216:217], s[86:87], v[6:7]\n"
        "v_pk_fma_f32 v[218:219], v[218:219], s[86:87], v[6:7]\n"
        "v_pk_fma_f32 v[220:221], v[220:221], s[86:87], v[6:7]\n"
        "v_pk_fma_f32 v[222:223], v[222:223], s[86:87], v[6:7]\n"
        "v_pk_mul_f32 v[216:217], v[216:217], v[128:129]\n"
        "v_pk_mul_f32 v[218:219], v[218:219], v[130:131]\n"
        "v_pk_mul_f32 v[220:221], v[220:221], v[132:133]\n"
        "v_pk_mul_f32 v[222:223], v[222:223], v[134:135]\n"
        "v_exp_f32 v216, v216\n"
        "v_exp_f32 v217, v217\n"
        "v_pk_mul_f32 v[160:161], v[160:161], v[128:129]\n"
        "v_exp_f32 v218, v218\n"
        "v_exp_f32 v219, v219\n"
        "v_pk_mul_f32 v[162:163], v[162:163], v[130:131]\n"
        "v_exp_f32 v220, v220\n"
        "v_exp_f32 v221, v221\n"
        "v_pk_mul_f32 v[164:165], v[164:165], v[132:133]\n"
        "v_exp_f32 v222, v222\n"
        "v_exp_f32 v223, v223\n"
        "v_pk_mul_f32 v[166:167], v[166:167], v[134:135]\n"
        "v_pk_add_f32 v[216:217], v[216:217], s[88:89]\n"
        "v_pk_add_f32 v[218:219], v[218:219], s[88:89]\n"
        "v_pk_add_f32 v[220:221], v[220:221], s[88:89]\n"
        "v_pk_add_f32 v[222:223], v[222:223], s[88:89]\n"
        "v_rcp_f32 v216, v216\n"
        "v_rcp_f32 v217, v217\n"
        "v_rcp_f32 v218, v218\n"
        "v_rcp_f32 v219, v219\n"
        "v_rcp_f32 v220, v220\n"
        "v_rcp_f32 v221, v221\n"
        "v_rcp_f32 v222, v222\n"
        "v_rcp_f32 v223, v223\n"
        "v_pk_mul_f32 v[160:161], v[160:161], v[216:217]\n"
        "v_pk_mul_f32 v[162:163], v[162:163], v[218:219]\n"
        "v_pk_mul_f32 v[164:165], v[164:165], v[220:221]\n"
        "v_pk_mul_f32 v[166:167], v[166:167], v[222:223]\n"
        "v_cvt_pk_bf16_f32 v128, v160, v161\n"
        "v_cvt_pk_bf16_f32 v129, v162, v163\n"
        "v_cvt_pk_bf16_f32 v130, v164, v165\n"
        "v_cvt_pk_bf16_f32 v131, v166, v167\n"
        "buffer_store_dwordx4 v[128:131], v236, s[60:63], s82 offen offset:1024 sc1\n"
        "v_pk_mul_f32 v[216:217], v[136:137], v[136:137]\n"
        "v_pk_mul_f32 v[218:219], v[138:139], v[138:139]\n"
        "v_pk_mul_f32 v[220:221], v[140:141], v[140:141]\n"
        "v_pk_mul_f32 v[222:223], v[142:143], v[142:143]\n"
        "v_pk_fma_f32 v[216:217], v[216:217], s[86:87], v[6:7]\n"
        "v_pk_fma_f32 v[218:219], v[218:219], s[86:87], v[6:7]\n"
        "v_pk_fma_f32 v[220:221], v[220:221], s[86:87], v[6:7]\n"
        "v_pk_fma_f32 v[222:223], v[222:223], s[86:87], v[6:7]\n"
        "v_pk_mul_f32 v[216:217], v[216:217], v[136:137]\n"
        "v_pk_mul_f32 v[218:219], v[218:219], v[138:139]\n"
        "v_pk_mul_f32 v[220:221], v[220:221], v[140:141]\n"
        "v_pk_mul_f32 v[222:223], v[222:223], v[142:143]\n"
        "v_exp_f32 v216, v216\n"
        "v_exp_f32 v217, v217\n"
        "v_pk_mul_f32 v[168:169], v[168:169], v[136:137]\n"
        "v_exp_f32 v218, v218\n"
        "v_exp_f32 v219, v219\n"
        "v_pk_mul_f32 v[170:171], v[170:171], v[138:139]\n"
        "v_exp_f32 v220, v220\n"
        "v_exp_f32 v221, v221\n"
        "v_pk_mul_f32 v[172:173], v[172:173], v[140:141]\n"
        "v_exp_f32 v222, v222\n"
        "v_exp_f32 v223, v223\n"
        "v_pk_mul_f32 v[174:175], v[174:175], v[142:143]\n"
        "v_pk_add_f32 v[216:217], v[216:217], s[88:89]\n"
        "v_pk_add_f32 v[218:219], v[218:219], s[88:89]\n"
        "v_pk_add_f32 v[220:221], v[220:221], s[88:89]\n"
        "v_pk_add_f32 v[222:223], v[222:223], s[88:89]\n"
        "v_rcp_f32 v216, v216\n"
        "v_rcp_f32 v217, v217\n"
        "v_rcp_f32 v218, v218\n"
        "v_rcp_f32 v219, v219\n"
        "v_rcp_f32 v220, v220\n"
        "v_rcp_f32 v221, v221\n"
        "v_rcp_f32 v222, v222\n"
        "v_rcp_f32 v223, v223\n"
        "v_pk_mul_f32 v[168:169], v[168:169], v[216:217]\n"
        "v_pk_mul_f32 v[170:171], v[170:171], v[218:219]\n"
        "v_pk_mul_f32 v[172:173], v[172:173], v[220:221]\n"
        "v_pk_mul_f32 v[174:175], v[174:175], v[222:223]\n"
        "v_cvt_pk_bf16_f32 v136, v168, v169\n"
        "v_cvt_pk_bf16_f32 v137, v170, v171\n"
        "v_cvt_pk_bf16_f32 v138, v172, v173\n"
        "v_cvt_pk_bf16_f32 v139, v174, v175\n"
        "buffer_store_dwordx4 v[136:139], v236, s[60:63], s83 offen offset:0 sc1\n"
        "v_pk_mul_f32 v[216:217], v[144:145], v[144:145]\n"
        "v_pk_mul_f32 v[218:219], v[146:147], v[146:147]\n"
        "v_pk_mul_f32 v[220:221], v[148:149], v[148:149]\n"
        "v_pk_mul_f32 v[222:223], v[150:151], v[150:151]\n"
        "v_pk_fma_f32 v[216:217], v[216:217], s[86:87], v[6:7]\n"
        "v_pk_fma_f32 v[218:219], v[218:219], s[86:87], v[6:7]\n"
        "v_pk_fma_f32 v[220:221], v[220:221], s[86:87], v[6:7]\n"
        "v_pk_fma_f32 v[222:223], v[222:223], s[86:87], v[6:7]\n"
        "v_pk_mul_f32 v[216:217], v[216:217], v[144:145]\n"
        "v_pk_mul_f32 v[218:219], v[218:219], v[146:147]\n"
        "v_pk_mul_f32 v[220:221], v[220:221], v[148:149]\n"
        "v_pk_mul_f32 v[222:223], v[222:223], v[150:151]\n"
        "v_exp_f32 v216, v216\n"
        "v_exp_f32 v217, v217\n"
        "v_pk_mul_f32 v[176:177], v[176:177], v[144:145]\n"
        "v_exp_f32 v218, v218\n"
        "v_exp_f32 v219, v219\n"
        "v_pk_mul_f32 v[178:179], v[178:179], v[146:147]\n"
        "v_exp_f32 v220, v220\n"
        "v_exp_f32 v221, v221\n"
        "v_pk_mul_f32 v[180:181], v[180:181], v[148:149]\n"
        "v_exp_f32 v222, v222\n"
        "v_exp_f32 v223, v223\n"
        "v_pk_mul_f32 v[182:183], v[182:183], v[150:151]\n"
        "v_pk_add_f32 v[216:217], v[216:217], s[88:89]\n"
        "v_pk_add_f32 v[218:219], v[218:219], s[88:89]\n"
        "v_pk_add_f32 v[220:221], v[220:221], s[88:89]\n"
        "v_pk_add_f32 v[222:223], v[222:223], s[88:89]\n"
        "v_rcp_f32 v216, v216\n"
        "v_rcp_f32 v217, v217\n"
        "v_rcp_f32 v218, v218\n"
        "v_rcp_f32 v219, v219\n"
        "v_rcp_f32 v220, v220\n"
        "v_rcp_f32 v221, v221\n"
        "v_rcp_f32 v222, v222\n"
        "v_rcp_f32 v223, v223\n"
        "v_pk_mul_f32 v[176:177], v[176:177], v[216:217]\n"
        "v_pk_mul_f32 v[178:179], v[178:179], v[218:219]\n"
        "v_pk_mul_f32 v[180:181], v[180:181], v[220:221]\n"
        "v_pk_mul_f32 v[182:183], v[182:183], v[222:223]\n"
        "v_cvt_pk_bf16_f32 v144, v176, v177\n"
        "v_cvt_pk_bf16_f32 v145, v178, v179\n"
        "v_cvt_pk_bf16_f32 v146, v180, v181\n"
        "v_cvt_pk_bf16_f32 v147, v182, v183\n"
        "buffer_store_dwordx4 v[144:147], v236, s[60:63], s83 offen offset:1024 sc1\n"
        "s_barrier\n"
        "s_add_u32 m0, s84, 150528\n"
        "s_add_u32 s66, s22, 10752\n"
        "buffer_load_dwordx4 v229, s[48:51], s66 offen lds\n"
        "v_xor_b32 v232, 0x10000, v231\n"
        "ds_read_b64_tr_b16 v[8:9], v232 offset:49152\n"
        "ds_read_b64_tr_b16 v[10:11], v232 offset:49664\n"
        "s_lshl_b32 s67, s75, 7\n"
        "s_add_u32 s52, s12, s67\n"
        "s_addc_u32 s53, s13, 0\n"
        "s_and_b32 s53, s53, 0xffff\n"
        "s_sub_u32 s54, 401408, s67\n"
        "s_mov_b32 s55, 0x20000\n"
        "v_xor_b32 v233, 0x10020, v231\n"
        "ds_read_b64_tr_b16 v[12:13], v233 offset:49152\n"
        "ds_read_b64_tr_b16 v[14:15], v233 offset:49664\n"
        "s_sleep 1\n"
        "v_xor_b32 v234, 0x10040, v231\n"
        "ds_read_b64_tr_b16 v[16:17], v234 offset:49152\n"
        "ds_read_b64_tr_b16 v[18:19], v234 offset:49664\n"
        "s_sleep 1\n"
        "v_xor_b32 v235, 0x10060, v231\n"
        "ds_read_b64_tr_b16 v[20:21], v235 offset:49152\n"
        "ds_read_b64_tr_b16 v[22:23], v235 offset:49664\n"
        "s_sleep 1\n"
        "s_waitcnt lgkmcnt(0)\n"
        "s_add_u32 m0, s84, 114688\n"
        "s_add_u32 s66, s16, 57344\n"
        "buffer_load_dwordx4 v228, s[48:51], s66 offen lds\n"
        "v_xor_b32 v232, 0x20000, v231\n"
        "ds_read_b64_tr_b16 v[24:25], v232 offset:0\n"
        "ds_read_b64_tr_b16 v[26:27], v232 offset:512\n"
        "s_add_u32 s14, s11, 3\n"
        "s_and_b32 s14, s14, 3\n"
        "s_add_u32 s13, s11, 2\n"
        "s_and_b32 s13, s13, 3\n"
        "s_add_u32 s12, s11, 1\n"
        "s_and_b32 s12, s12, 3\n"
        "s_add_u32 m0, s84, 115712\n"
        "s_add_u32 s66, s16, 60928\n"
        "buffer_load_dwordx4 v229, s[48:51], s66 offen lds\n"
        "v_xor_b32 v233, 0x20020, v231\n"
        "ds_read_b64_tr_b16 v[28:29], v233 offset:0\n"
        "ds_read_b64_tr_b16 v[30:31], v233 offset:512\n"
        "s_add_u32 s11, s11, 0\n"
        "s_and_b32 s11, s11, 3\n"
        "s_lshr_b32 s67, s97, 1\n"
        "s_mul_i32 s67, s67, 7168\n"
        "s_mul_i32 s68, s97, 21504\n"
        "s_add_u32 m0, s84, 116736\n"
        "s_add_u32 s66, s16, 64512\n"
        "buffer_load_dwordx4 v228, s[48:51], s66 offen lds\n"
        "v_xor_b32 v234, 0x20040, v231\n"
        "ds_read_b64_tr_b16 v[32:33], v234 offset:0\n"
        "ds_read_b64_tr_b16 v[34:35], v234 offset:512\n"
        "s_add_u32 s67, s67, s68\n"
        "s_mul_i32 s23, s11, 100352\n"
        "s_add_u32 s23, s23, s67\n"
        "s_mul_i32 s24, s12, 100352\n"
        "s_add_u32 s24, s24, s67\n"
        "s_mul_i32 s25, s13, 100352\n"
        "s_add_u32 m0, s84, 117760\n"
        "s_add_u32 s66, s16, 68096\n"
        "buffer_load_dwordx4 v229, s[48:51], s66 offen lds\n"
        "v_xor_b32 v235, 0x20060, v231\n"
        "ds_read_b64_tr_b16 v[36:37], v235 offset:0\n"
        "ds_read_b64_tr_b16 v[38:39], v235 offset:512\n"
        "s_add_u32 s25, s25, s67\n"
        "s_mul_i32 s26, s14, 100352\n"
        "s_add_u32 s26, s26, s67\n"
        "s_lshl2_add_u32 s67, s74, s97\n"
        "s_waitcnt lgkmcnt(0)\n"
        "s_add_u32 m0, s84, 131072\n"
        "s_add_u32 s66, s16, 0\n"
        "buffer_load_dwordx4 v228, s[48:51], s66 offen lds\n"
        "s_mul_i32 s67, s67, 73728\n"
        "s_add_u32 s67, s67, 1048576\n"
        "v_lshl_add_u32 v237, v1, 4, s67\n"
        "s_lshr_b32 s68, s97, 1\n"
        "s_lshl_b32 s68, s68, 7\n"
        "s_add_u32 s68, s68, 4096\n"
        "s_add_u32 m0, s84, 132096\n"
        "s_add_u32 s66, s16, 3584\n"
        "buffer_load_dwordx4 v229, s[48:51], s66 offen lds\n"
        "s_lshl_b32 s67, s74, 12\n"
        "s_add_u32 s67, s67, s68\n"
        "v_lshrrev_b32 v216, 5, v1\n"
        "v_lshl_add_u32 v216, v216, 11, s67\n"
        "v_bfe_u32 v217, v1, 2, 3\n"
        "s_add_u32 m0, s84, 133120\n"
        "s_add_u32 s66, s16, 7168\n"
        "buffer_load_dwordx4 v228, s[48:51], s66 offen lds\n"
        "s_waitcnt vmcnt(8)\n"
        "s_add_u32 s66, s95, 131072\n"
        "s_mov_b64 exec, 0xffff\n"
        "buffer_store_dwordx2 v[242:243], v2, s[60:63], s66 offen sc1\n"
        "s_mov_b64 exec, -1\n"
        "v_min_u32 v217, 6, v217\n"
        "v_lshl_add_u32 v240, v217, 8, v216\n"
        "s_lshl_b32 s67, s74, 11\n"
        "s_add_u32 s67, s67, s68\n"
        "v_lshl_add_u32 v238, v4, 8, s67\n"
        "s_add_u32 m0, s84, 134144\n"
        "s_add_u32 s66, s16, 10752\n"
        "buffer_load_dwordx4 v229, s[48:51], s66 offen lds\n"
        "buffer_load_dwordx4 v[246:249], v239, s[32:35], 0 offen\n"
        "s_waitcnt vmcnt(10)\n"
        "s_barrier\n"
        "s_add_u32 m0, s84, 16384\n"
        "s_add_u32 s66, s17, 0\n"
        "buffer_load_dwordx4 v228, s[48:51], s66 offen lds\n"
        "ds_read_b64_tr_b16 v[40:41], v232 offset:16384\n"
        "ds_read_b64_tr_b16 v[42:43], v232 offset:16896\n"
        "s_add_u32 m0, s84, 17408\n"
        "s_add_u32 s66, s17, 3584\n"
        "buffer_load_dwordx4 v229, s[48:51], s66 offen lds\n"
        "ds_read_b64_tr_b16 v[44:45], v233 offset:16384\n"
        "ds_read_b64_tr_b16 v[46:47], v233 offset:16896\n"
        "s_add_u32 m0, s84, 18432\n"
        "s_add_u32 s66, s17, 7168\n"
        "buffer_load_dwordx4 v228, s[48:51], s66 offen lds\n"
        "ds_read_b64_tr_b16 v[48:49], v234 offset:16384\n"
        "ds_read_b64_tr_b16 v[50:51], v234 offset:16896\n"
        "s_add_u32 m0, s84, 19456\n"
        "s_add_u32 s66, s17, 10752\n"
        "buffer_load_dwordx4 v229, s[48:51], s66 offen lds\n"
        "ds_read_b64_tr_b16 v[52:53], v235 offset:16384\n"
        "ds_read_b64_tr_b16 v[54:55], v235 offset:16896\n"
        "s_add_u32 m0, s84, 32768\n"
        "s_add_u32 s66, s18, 0\n"
        "buffer_load_dwordx4 v228, s[48:51], s66 offen lds\n"
        "s_add_u32 m0, s84, 33792\n"
        "s_add_u32 s66, s18, 3584\n"
        "buffer_load_dwordx4 v229, s[48:51], s66 offen lds\n"
        "s_add_u32 m0, s84, 34816\n"
        "s_add_u32 s66, s18, 7168\n"
        "buffer_load_dwordx4 v228, s[48:51], s66 offen lds\n"
        "s_waitcnt vmcnt(13)\n"
        "s_barrier\n"
        "s_add_u32 m0, s84, 35840\n"
        "s_add_u32 s66, s18, 10752\n"
        "buffer_load_dwordx4 v229, s[48:51], s66 offen lds\n"
        "v_xor_b32 v232, 0x10000, v231\n"
        "ds_read_b64_tr_b16 v[56:57], v232 offset:49152\n"
        "ds_read_b64_tr_b16 v[58:59], v232 offset:49664\n"
        "s_waitcnt lgkmcnt(2)\n"
        "s_add_u32 m0, s84, 147456\n"
        "s_add_u32 s66, s19, 0\n"
        "buffer_load_dwordx4 v228, s[48:51], s66 offen lds\n"
        "v_xor_b32 v233, 0x10020, v231\n"
        "ds_read_b64_tr_b16 v[60:61], v233 offset:49152\n"
        "ds_read_b64_tr_b16 v[62:63], v233 offset:49664\n"
        "s_add_u32 m0, s84, 148480\n"
        "s_add_u32 s66, s19, 3584\n"
        "buffer_load_dwordx4 v229, s[48:51], s66 offen lds\n"
        "v_xor_b32 v234, 0x10040, v231\n"
        "ds_read_b64_tr_b16 v[64:65], v234 offset:49152\n"
        "ds_read_b64_tr_b16 v[66:67], v234 offset:49664\n"
        "s_add_u32 m0, s84, 149504\n"
        "s_add_u32 s66, s19, 7168\n"
        "buffer_load_dwordx4 v228, s[48:51], s66 offen lds\n"
        "v_xor_b32 v235, 0x10060, v231\n"
        "ds_read_b64_tr_b16 v[68:69], v235 offset:49152\n"
        "ds_read_b64_tr_b16 v[70:71], v235 offset:49664\n"
        "s_add_u32 m0, s84, 150528\n"
        "s_add_u32 s66, s19, 10752\n"
        "buffer_load_dwordx4 v229, s[48:51], s66 offen lds\n"
        "s_add_u32 m0, s84, 98304\n"
        "s_add_u32 s66, s17, 57344\n"
        "buffer_load_dwordx4 v228, s[48:51], s66 offen lds\n"
        "s_add_u32 m0, s84, 99328\n"
        "s_add_u32 s66, s17, 60928\n"
        "buffer_load_dwordx4 v229, s[48:51], s66 offen lds\n"
        "s_mov_b32 s66, 131072\n"
        "buffer_load_dwordx2 v[244:245], v240, s[60:63], s66 offen sc1\n"
        "s_barrier\n"
        "s_add_u32 m0, s84, 100352\n"
        "s_add_u32 s66, s17, 64512\n"
        "buffer_load_dwordx4 v228, s[48:51], s66 offen lds\n"
        "s_add_u32 m0, s84, 101376\n"
        "s_add_u32 s66, s17, 68096\n"
        "buffer_load_dwordx4 v229, s[48:51], s66 offen lds\n"
        "s_cbranch_execz Lback\n"
        "s_waitcnt lgkmcnt(0)\n"
        "s_add_u32 m0, s84, 114688\n"
        "s_add_u32 s66, s18, 57344\n"
        "buffer_load_dwordx4 v228, s[48:51], s66 offen lds\n"
        "s_add_u32 m0, s84, 115712\n"
        "s_add_u32 s66, s18, 60928\n"
        "buffer_load_dwordx4 v229, s[48:51], s66 offen lds\n"
        "s_add_u32 m0, s84, 116736\n"
        "s_add_u32 s66, s18, 64512\n"
        "buffer_load_dwordx4 v228, s[48:51], s66 offen lds\n"
        "s_add_u32 m0, s84, 117760\n"
        "s_add_u32 s66, s18, 68096\n"
        "buffer_load_dwordx4 v229, s[48:51], s66 offen lds\n"
        "s_add_u32 m0, s84, 49152\n"
        "s_add_u32 s66, s19, 57344\n"
        "buffer_load_dwordx4 v228, s[48:51], s66 offen lds\n"
        "s_barrier\n"
        "s_add_u32 m0, s84, 50176\n"
        "s_add_u32 s66, s19, 60928\n"
        "buffer_load_dwordx4 v229, s[48:51], s66 offen lds\n"
        "s_add_u32 m0, s84, 51200\n"
        "s_add_u32 s66, s19, 64512\n"
        "buffer_load_dwordx4 v228, s[48:51], s66 offen lds\n"
        "s_add_u32 m0, s84, 52224\n"
        "s_add_u32 s66, s19, 68096\n"
        "buffer_load_dwordx4 v229, s[48:51], s66 offen lds\n"
        "s_add_u32 m0, s84, 65536\n"
        "s_add_u32 s66, s20, 57344\n"
        "buffer_load_dwordx4 v228, s[48:51], s66 offen lds\n"
        "s_waitcnt vmcnt(11)\n"
        "v_cmp_ne_u64 vcc, s[70:71], v[244:245]\n"
        "s_cbranch_vccz Lret3\n"
        "s_mov_b32 s79, 400000\n"
        "s_mov_b32 s66, 131072\n"
        "Lspin3:\n"
        "s_sleep 2\n"
        "buffer_load_dwordx2 v[244:245], v240, s[60:63], s66 offen sc1\n"
        "s_waitcnt vmcnt(0)\n"
        "v_cmp_ne_u64 vcc, s[70:71], v[244:245]\n"
        "s_cbranch_vccz Lret3\n"
        "s_sub_u32 s79, s79, 1\n"
        "s_cmp_lg_u32 s79, 0\n"
        "s_cbranch_scc1 Lspin3\n"
        "Lret3:\n"
        "s_bfe_u32 s27, s28, 0x40000\n"
        "s_lshl_b32 s69, s27, 12\n"
        "buffer_load_dwordx4 v[72:75], v237, s[60:63], s69 offen offset:0 sc1\n"
        "buffer_load_dwordx4 v[76:79], v237, s[60:63], s69 offen offset:1024 sc1\n"
        "buffer_load_dwordx4 v[80:83], v237, s[60:63], s69 offen offset:2048 sc1\n"
        "buffer_load_dwordx4 v[84:87], v237, s[60:63], s69 offen offset:3072 sc1\n"
        "s_bfe_u32 s27, s28, 0x40004\n"
        "s_lshl_b32 s69, s27, 12\n"
        "buffer_load_dwordx4 v[88:91], v237, s[60:63], s69 offen offset:0 sc1\n"
        "buffer_load_dwordx4 v[92:95], v237, s[60:63], s69 offen offset:1024 sc1\n"
        "buffer_load_dwordx4 v[96:99], v237, s[60:63], s69 offen offset:2048 sc1\n"
        "buffer_load_dwordx4 v[100:103], v237, s[60:63], s69 offen offset:3072 sc1\n"
        "s_bfe_u32 s27, s28, 0x40008\n"
        "s_lshl_b32 s69, s27, 12\n"
        "buffer_load_dwordx4 v[104:107], v237, s[60:63], s69 offen offset:0 sc1\n"
        "buffer_load_dwordx4 v[108:111], v237, s[60:63], s69 offen offset:1024 sc1\n"
        "buffer_load_dwordx4 v[112:115], v237, s[60:63], s69 offen offset:2048 sc1\n"
        "buffer_load_dwordx4 v[116:119], v237, s[60:63], s69 offen offset:3072 sc1\n"
        "s_bfe_u32 s27, s28, 0x4000c\n"
        "s_lshl_b32 s69, s27, 12\n"
        "buffer_load_dwordx4 v[200:203], v237, s[60:63], s69 offen offset:0 sc1\n"
        "buffer_load_dwordx4 v[204:207], v237, s[60:63], s69 offen offset:1024 sc1\n"
        "buffer_load_dwordx4 v[208:211], v237, s[60:63], s69 offen offset:2048 sc1\n"
        "buffer_load_dwordx4 v[212:215], v237, s[60:63], s69 offen offset:3072 sc1\n"
        "s_waitcnt vmcnt(15)\n"
        "s_add_u32 m0, s84, 66560\n"
        "s_add_u32 s66, s20, 60928\n"
        "buffer_load_dwordx4 v229, s[48:51], s66 offen lds\n"
        "s_waitcnt vmcnt(15)\n"
        "s_add_u32 m0, s84, 67584\n"
        "s_add_u32 s66, s20, 64512\n"
        "buffer_load_dwordx4 v228, s[48:51], s66 offen lds\n"
        "s_waitcnt vmcnt(15)\n"
        "s_add_u32 m0, s84, 68608\n"
        "s_add_u32 s66, s20, 68096\n"
        "buffer_load_dwordx4 v229, s[48:51], s66 offen lds\n"
        "s_waitcnt vmcnt(15)\n"
        "v_and_b32 v216, 2, v4\n"
        "v_xor_b32 v216, v216, v3\n"
        "v_lshlrev_b32 v216, 4, v216\n"
        "v_lshl_add_u32 v216, v4, 8, v216\n"
        "v_add_u32 v250, s84, v216\n"
        "v_xor_b32 v218, 0x0, v231\n"
        "v_xor_b32 v219, 0x20, v231\n"
        "v_xor_b32 v220, 0x40, v231\n"
        "v_xor_b32 v221, 0x60, v231\n"
        "v_xor_b32 v222, 0x10000, v231\n"
        "v_xor_b32 v223, 0x10020, v231\n"
        "v_xor_b32 v224, 0x10040, v231\n"
        "v_xor_b32 v225, 0x10060, v231\n"
        "v_xor_b32 v226, 0x20000, v231\n"
        "v_xor_b32 v227, 0x20020, v231\n"
        "v_xor_b32 v232, 0x20040, v231\n"
        "v_xor_b32 v233, 0x20060, v231\n"
        "ds_read_b64_tr_b16 v[184:185], v226 offset:0\n"
        "ds_read_b64_tr_b16 v[186:187], v226 offset:512\n"
        "ds_read_b64_tr_b16 v[188:189], v227 offset:0\n"
        "ds_read_b64_tr_b16 v[190:191], v227 offset:512\n"
        "ds_read_b64_tr_b16 v[192:193], v232 offset:0\n"
        "ds_read_b64_tr_b16 v[194:195], v232 offset:512\n"
        "s_barrier\n"
        "s_waitcnt lgkmcnt(4)\n"
        "v_mfma_f32_16x16x32_bf16 v[120:123], v[184:187], v[72:75], 0\n"
        "ds_read_b64_tr_b16 v[196:197], v233 offset:0\n"
        "v_mfma_f32_16x16x32_bf16 v[124:127], v[184:187], v[76:79], 0\n"
        "s_add_u32 m0, s84, 81920\n"
        "s_add_u32 s66, s21, 57344\n"
        "buffer_load_dwordx4 v228, s[48:51], s66 offen lds\n"
        "v_mfma_f32_16x16x32_bf16 v[128:131], v[184:187], v[80:83], 0\n"
        "ds_read_b64_tr_b16 v[198:199], v233 offset:512\n"
        "v_mfma_f32_16x16x32_bf16 v[132:135], v[184:187], v[84:87], 0\n"
        "s_add_u32 m0, s84, 82944\n"
        "s_add_u32 s66, s21, 60928\n"
        "buffer_load_dwordx4 v229, s[48:51], s66 offen lds\n"
        "s_waitcnt lgkmcnt(4)\n"
        "v_mfma_f32_16x16x32_bf16 v[136:139], v[188:191], v[72:75], 0\n"
        "ds_read_b64_tr_b16 v[184:185], v218 offset:16384\n"
        "v_mfma_f32_16x16x32_bf16 v[140:143], v[188:191], v[76:79], 0\n"
        "s_add_u32 m0, s84, 83968\n"
        "s_add_u32 s66, s21, 64512\n"
        "buffer_load_dwordx4 v228, s[48:51], s66 offen lds\n"
        "v_mfma_f32_16x16x32_bf16 v[144:147], v[188:191], v[80:83], 0\n"
        "ds_read_b64_tr_b16 v[186:187], v218 offset:16896\n"
        "v_mfma_f32_16x16x32_bf16 v[148:151], v[188:191], v[84:87], 0\n"
        "s_add_u32 m0, s84, 84992\n"
        "s_add_u32 s66, s21, 68096\n"
        "buffer_load_dwordx4 v229, s[48:51], s66 offen lds\n"
        "s_waitcnt lgkmcnt(4)\n"
        "v_mfma_f32_16x16x32_bf16 v[152:155], v[192:195], v[72:75], 0\n"
        "ds_read_b64_tr_b16 v[188:189], v219 offset:16384\n"
        "v_mfma_f32_16x16x32_bf16 v[156:159], v[192:195], v[76:79], 0\n"
        "s_add_u32 m0, s84, 0\n"
        "s_add_u32 s66, s22, 57344\n"
        "buffer_load_dwordx4 v228, s[48:51], s66 offen lds\n"
        "v_mfma_f32_16x16x32_bf16 v[160:163], v[192:195], v[80:83], 0\n"
        "ds_read_b64_tr_b16 v[190:191], v219 offset:16896\n"
        "v_mfma_f32_16x16x32_bf16 v[164:167], v[192:195], v[84:87], 0\n"
        "s_add_u32 m0, s84, 1024\n"
        "s_add_u32 s66, s22, 60928\n"
        "buffer_load_dwordx4 v229, s[48:51], s66 offen lds\n"
        "s_waitcnt lgkmcnt(4)\n"
        "v_mfma_f32_16x16x32_bf16 v[168:171], v[196:199], v[72:75], 0\n"
        "ds_read_b64_tr_b16 v[192:193], v220 offset:16384\n"
        "v_mfma_f32_16x16x32_bf16 v[172:175], v[196:199], v[76:79], 0\n"
        "s_add_u32 m0, s84, 2048\n"
        "s_add_u32 s66, s22, 64512\n"
        "buffer_load_dwordx4 v228, s[48:51], s66 offen lds\n"
        "v_mfma_f32_16x16x32_bf16 v[176:179], v[196:199], v[80:83], 0\n"
        "ds_read_b64_tr_b16 v[194:195], v220 offset:16896\n"
        "v_mfma_f32_16x16x32_bf16 v[180:183], v[196:199], v[84:87], 0\n"
        "s_add_u32 m0, s84, 3072\n"
        "s_add_u32 s66, s22, 68096\n"
        "buffer_load_dwordx4 v229, s[48:51], s66 offen lds\n"
        "s_waitcnt vmcnt(22) lgkmcnt(4)\n"
        "v_mfma_f32_16x16x32_bf16 v[120:123], v[184:187], v[88:91], v[120:123]\n"
        "ds_read_b64_tr_b16 v[196:197], v221 offset:16384\n"
        "s_waitcnt vmcnt(21)\n"
        "v_mfma_f32_16x16x32_bf16 v[124:127], v[184:187], v[92:95], v[124:127]\n"
        "s_bfe_u32 s27, s28, 0x40010\n"
        "s_lshl_b32 s69, s27, 12\n"
        "buffer_load_dwordx4 v[72:75], v237, s[60:63], s69 offen offset:0 sc1\n"
        "s_waitcnt vmcnt(21)\n"
        "v_mfma_f32_16x16x32_bf16 v[128:131], v[184:187], v[96:99], v[128:131]\n"
        "ds_read_b64_tr_b16 v[198:199], v221 offset:16896\n"
        "s_waitcnt vmcnt(20)\n"
        "v_mfma_f32_16x16x32_bf16 v[132:135], v[184:187], v[100:103], v[132:135]\n"
        "buffer_load_dwordx4 v[76:79], v237, s[60:63], s69 offen offset:1024 sc1\n"
        "s_waitcnt lgkmcnt(4)\n"
        "v_mfma_f32_16x16x32_bf16 v[136:139], v[188:191], v[88:91], v[136:139]\n"
        "ds_read_b64_tr_b16 v[184:185], v218 offset:32768\n"
        "v_mfma_f32_16x16x32_bf16 v[140:143], v[188:191], v[92:95], v[140:143]\n"
        "buffer_load_dwordx4 v[80:83], v237, s[60:63], s69 offen offset:2048 sc1\n"
        "v_mfma_f32_16x16x32_bf16 v[144:147], v[188:191], v[96:99], v[144:147]\n"
        "ds_read_b64_tr_b16 v[186:187], v218 offset:33280\n"
        "v_mfma_f32_16x16x32_bf16 v[148:151], v[188:191], v[100:103], v[148:151]\n"
        "buffer_load_dwordx4 v[84:87], v237, s[60:63], s69 offen offset:3072 sc1\n"
        "s_waitcnt lgkmcnt(4)\n"
        "v_mfma_f32_16x16x32_bf16 v[152:155], v[192:195], v[88:91], v[152:155]\n"
        "ds_read_b64_tr_b16 v[188:189], v219 offset:32768\n"
        "v_mfma_f32_16x16x32_bf16 v[156:159], v[192:195], v[92:95], v[156:159]\n"
        "s_add_u32 m0, s84, 131072\n"
        "s_add_u32 s66, s23, 0\n"
        "buffer_load_dwordx4 v228, s[52:55], s66 offen lds\n"
        "v_mfma_f32_16x16x32_bf16 v[160:163], v[192:195], v[96:99], v[160:163]\n"
        "ds_read_b64_tr_b16 v[190:191], v219 offset:33280\n"
        "v_mfma_f32_16x16x32_bf16 v[164:167], v[192:195], v[100:103], v[164:167]\n"
        "s_add_u32 m0, s84, 132096\n"
        "s_add_u32 s66, s23, 3584\n"
        "buffer_load_dwordx4 v229, s[52:55], s66 offen lds\n"
        "s_waitcnt lgkmcnt(4)\n"
        "v_mfma_f32_16x16x32_bf16 v[168:171], v[196:199], v[88:91], v[168:171]\n"
        "ds_read_b64_tr_b16 v[192:193], v220 offset:32768\n"
        "v_mfma_f32_16x16x32_bf16 v[172:175], v[196:199], v[92:95], v[172:175]\n"
        "s_add_u32 m0, s84, 133120\n"
        "s_add_u32 s66, s23, 7168\n"
        "buffer_load_dwordx4 v228, s[52:55], s66 offen lds\n"
        "v_mfma_f32_16x16x32_bf16 v[176:179], v[196:199], v[96:99], v[176:179]\n"
        "ds_read_b64_tr_b16 v[194:195], v220 offset:33280\n"
        "v_mfma_f32_16x16x32_bf16 v[180:183], v[196:199], v[100:103], v[180:183]\n"
        "s_add_u32 m0, s84, 134144\n"
        "s_add_u32 s66, s23, 10752\n"
        "buffer_load_dwordx4 v229, s[52:55], s66 offen lds\n"
        "s_waitcnt vmcnt(26) lgkmcnt(4)\n"
        "v_mfma_f32_16x16x32_bf16 v[120:123], v[184:187], v[104:107], v[120:123]\n"
        "ds_read_b64_tr_b16 v[196:197], v221 offset:32768\n"
        "s_waitcnt vmcnt(25)\n"
        "v_mfma_f32_16x16x32_bf16 v[124:127], v[184:187], v[108:111], v[124:127]\n"
        "s_bfe_u32 s27, s28, 0x40014\n"
        "s_lshl_b32 s69, s27, 12\n"
        "buffer_load_dwordx4 v[88:91], v237, s[60:63], s69 offen offset:0 sc1\n"
        "s_waitcnt vmcnt(25)\n"
        "v_mfma_f32_16x16x32_bf16 v[128:131], v[184:187], v[112:115], v[128:131]\n"
        "ds_read_b64_tr_b16 v[198:199], v221 offset:33280\n"
        "s_waitcnt vmcnt(24)\n"
        "v_mfma_f32_16x16x32_bf16 v[132:135], v[184:187], v[116:119], v[132:135]\n"
        "buffer_load_dwordx4 v[92:95], v237, s[60:63], s69 offen offset:1024 sc1\n"
        "s_waitcnt lgkmcnt(4)\n"
        "v_mfma_f32_16x16x32_bf16 v[136:139], v[188:191], v[104:107], v[136:139]\n"
        "ds_read_b64_tr_b16 v[184:185], v226 offset:16384\n"
        "v_mfma_f32_16x16x32_bf16 v[140:143], v[188:191], v[108:111], v[140:143]\n"
        "buffer_load_dwordx4 v[96:99], v237, s[60:63], s69 offen offset:2048 sc1\n"
        "v_mfma_f32_16x16x32_bf16 v[144:147], v[188:191], v[112:115], v[144:147]\n"
        "ds_read_b64_tr_b16 v[186:187], v226 offset:16896\n"
        "v_mfma_f32_16x16x32_bf16 v[148:151], v[188:191], v[116:119], v[148:151]\n"
        "buffer_load_dwordx4 v[100:103], v237, s[60:63], s69 offen offset:3072 sc1\n"
        "s_waitcnt lgkmcnt(4)\n"
        "v_mfma_f32_16x16x32_bf16 v[152:155], v[192:195], v[104:107], v[152:155]\n"
        "ds_read_b64_tr_b16 v[188:189], v227 offset:16384\n"
        "v_mfma_f32_16x16x32_bf16 v[156:159], v[192:195], v[108:111], v[156:159]\n"
        "s_add_u32 m0, s84, 16384\n"
        "s_add_u32 s66, s24, 0\n"
        "buffer_load_dwordx4 v228, s[52:55], s66 offen lds\n"
        "v_mfma_f32_16x16x32_bf16 v[160:163], v[192:195], v[112:115], v[160:163]\n"
        "ds_read_b64_tr_b16 v[190:191], v227 offset:16896\n"
        "v_mfma_f32_16x16x32_bf16 v[164:167], v[192:195], v[116:119], v[164:167]\n"
        "s_add_u32 m0, s84, 17408\n"
        "s_add_u32 s66, s24, 3584\n"
        "buffer_load_dwordx4 v229, s[52:55], s66 offen lds\n"
        "s_waitcnt lgkmcnt(4)\n"
        "v_mfma_f32_16x16x32_bf16 v[168:171], v[196:199], v[104:107], v[168:171]\n"
        "ds_read_b64_tr_b16 v[192:193], v232 offset:16384\n"
        "v_mfma_f32_16x16x32_bf16 v[172:175], v[196:199], v[108:111], v[172:175]\n"
        "s_add_u32 m0, s84, 18432\n"
        "s_add_u32 s66, s24, 7168\n"
        "buffer_load_dwordx4 v228, s[52:55], s66 offen lds\n"
        "v_mfma_f32_16x16x32_bf16 v[176:179], v[196:199], v[112:115], v[176:179]\n"
        "ds_read_b64_tr_b16 v[194:195], v232 offset:16896\n"
        "v_mfma_f32_16x16x32_bf16 v[180:183], v[196:199], v[116:119], v[180:183]\n"
        "s_add_u32 m0, s84, 19456\n"
        "s_add_u32 s66, s24, 10752\n"
        "buffer_load_dwordx4 v229, s[52:55], s66 offen lds\n"
        "s_waitcnt vmcnt(30) lgkmcnt(4)\n"
        "v_mfma_f32_16x16x32_bf16 v[120:123], v[184:187], v[200:203], v[120:123]\n"
        "ds_read_b64_tr_b16 v[196:197], v233 offset:16384\n"
        "s_waitcnt vmcnt(29)\n"
        "v_mfma_f32_16x16x32_bf16 v[124:127], v[184:187], v[204:207], v[124:127]\n"
        "s_bfe_u32 s27, s28, 0x40018\n"
        "s_lshl_b32 s69, s27, 12\n"
        "buffer_load_dwordx4 v[104:107], v237, s[60:63], s69 offen offset:0 sc1\n"
        "s_waitcnt vmcnt(29)\n"
        "v_mfma_f32_16x16x32_bf16 v[128:131], v[184:187], v[208:211], v[128:131]\n"
        "ds_read_b64_tr_b16 v[198:199], v233 offset:16896\n"
        "s_waitcnt vmcnt(28)\n"
        "v_mfma_f32_16x16x32_bf16 v[132:135], v[184:187], v[212:215], v[132:135]\n"
        "buffer_load_dwordx4 v[108:111], v237, s[60:63], s69 offen offset:1024 sc1\n"
        "s_waitcnt lgkmcnt(4)\n"
        "v_mfma_f32_16x16x32_bf16 v[136:139], v[188:191], v[200:203], v[136:139]\n"
        "v_mfma_f32_16x16x32_bf16 v[140:143], v[188:191], v[204:207], v[140:143]\n"
        "buffer_load_dwordx4 v[112:115], v237, s[60:63], s69 offen offset:2048 sc1\n"
        "v_mfma_f32_16x16x32_bf16 v[144:147], v[188:191], v[208:211], v[144:147]\n"
        "v_mfma_f32_16x16x32_bf16 v[148:151], v[188:191], v[212:215], v[148:151]\n"
        "buffer_load_dwordx4 v[116:119], v237, s[60:63], s69 offen offset:3072 sc1\n"
        "s_waitcnt lgkmcnt(2)\n"
        "v_mfma_f32_16x16x32_bf16 v[152:155], v[192:195], v[200:203], v[152:155]\n"
        "v_mfma_f32_16x16x32_bf16 v[156:159], v[192:195], v[204:207], v[156:159]\n"
        "s_add_u32 m0, s84, 32768\n"
        "s_add_u32 s66, s25, 0\n"
        "buffer_load_dwordx4 v228, s[52:55], s66 offen lds\n"
        "v_mfma_f32_16x16x32_bf16 v[160:163], v[192:195], v[208:211], v[160:163]\n"
        "v_mfma_f32_16x16x32_bf16 v[164:167], v[192:195], v[212:215], v[164:167]\n"
        "s_add_u32 m0, s84, 33792\n"
        "s_add_u32 s66, s25, 3584\n"
        "buffer_load_dwordx4 v229, s[52:55], s66 offen lds\n"
        "s_waitcnt lgkmcnt(0)\n"
        "v_mfma_f32_16x16x32_bf16 v[168:171], v[196:199], v[200:203], v[168:171]\n"
        "v_mfma_f32_16x16x32_bf16 v[172:175], v[196:199], v[204:207], v[172:175]\n"
        "s_add_u32 m0, s84, 34816\n"
        "s_add_u32 s66, s25, 7168\n"
        "buffer_load_dwordx4 v228, s[52:55], s66 offen lds\n"
        "v_mfma_f32_16x16x32_bf16 v[176:179], v[196:199], v[208:211], v[176:179]\n"
        "v_mfma_f32_16x16x32_bf16 v[180:183], v[196:199], v[212:215], v[180:183]\n"
        "s_add_u32 m0, s84, 35840\n"
        "s_add_u32 s66, s25, 10752\n"
        "buffer_load_dwordx4 v229, s[52:55], s66 offen lds\n"
        "s_waitcnt vmcnt(23)\n"
        "v_mfma_f32_16x16x32_bf16 v[120:123], v[8:11], v[72:75], v[120:123]\n"
        "s_waitcnt vmcnt(22)\n"
        "v_mfma_f32_16x16x32_bf16 v[124:127], v[8:11], v[76:79], v[124:127]\n"
        "s_add_u32 m0, s84, 147456\n"
        "s_add_u32 s66, s26, 0\n"
        "buffer_load_dwordx4 v228, s[52:55], s66 offen lds\n"
        "s_waitcnt vmcnt(22)\n"
        "v_mfma_f32_16x16x32_bf16 v[128:131], v[8:11], v[80:83], v[128:131]\n"
        "s_waitcnt vmcnt(21)\n"
        "v_mfma_f32_16x16x32_bf16 v[132:135], v[8:11], v[84:87], v[132:135]\n"
        "s_add_u32 m0, s84, 148480\n"
        "s_add_u32 s66, s26, 3584\n"
        "buffer_load_dwordx4 v229, s[52:55], s66 offen lds\n"
        "v_mfma_f32_16x16x32_bf16 v[136:139], v[12:15], v[72:75], v[136:139]\n"
        "v_mfma_f32_16x16x32_bf16 v[140:143], v[12:15], v[76:79], v[140:143]\n"
        "v_mfma_f32_16x16x32_bf16 v[144:147], v[12:15], v[80:83], v[144:147]\n"
        "v_mfma_f32_16x16x32_bf16 v[148:151], v[12:15], v[84:87], v[148:151]\n"
        "s_add_u32 m0, s84, 149504\n"
        "s_add_u32 s66, s26, 7168\n"
        "buffer_load_dwordx4 v228, s[52:55], s66 offen lds\n"
        "v_mfma_f32_16x16x32_bf16 v[152:155], v[16:19], v[72:75], v[152:155]\n"
        "v_mfma_f32_16x16x32_bf16 v[156:159], v[16:19], v[76:79], v[156:159]\n"
        "s_add_u32 m0, s84, 150528\n"
        "s_add_u32 s66, s26, 10752\n"
        "buffer_load_dwordx4 v229, s[52:55], s66 offen lds\n"
        "v_mfma_f32_16x16x32_bf16 v[160:163], v[16:19], v[80:83], v[160:163]\n"
        "v_mfma_f32_16x16x32_bf16 v[164:167], v[16:19], v[84:87], v[164:167]\n"
        "v_mfma_f32_16x16x32_bf16 v[168:171], v[20:23], v[72:75], v[168:171]\n"
        "v_add_u32 v250, 0x0, v250\n"
        "v_mfma_f32_16x16x32_bf16 v[172:175], v[20:23], v[76:79], v[172:175]\n"
        "v_xor_b32 v251, 64, v250\n"
        "s_barrier\n"
        "v_mfma_f32_16x16x32_bf16 v[176:179], v[20:23], v[80:83], v[176:179]\n"
        "v_xor_b32 v252, 128, v250\n"
        "v_mfma_f32_16x16x32_bf16 v[180:183], v[20:23], v[84:87], v[180:183]\n"
        "v_xor_b32 v253, 192, v250\n"
        "s_waitcnt vmcnt(19)\n"
        "v_mfma_f32_16x16x32_bf16 v[120:123], v[24:27], v[88:91], v[120:123]\n"
        "s_lshr_b32 s67, s64, 1\n"
        "s_lshl_b32 s67, s67, 3\n"
        "s_lshr_b32 s67, 0x3020100, s67\n"
        "s_and_b32 s67, s67, 0xff\n"
        "s_lshl_b32 s67, s67, 14\n"
        "s_and_b32 s68, s64, 1\n"
        "s_lshl_b32 s68, s68, 3\n"
        "v_lshrrev_b32 v216, 3, v1\n"
        "v_add_u32 v216, s68, v216\n"
        "v_lshlrev_b32 v217, 1, v5\n"
        "v_xor_b32 v216, v216, v217\n"
        "v_lshlrev_b32 v216, 4, v216\n"
        "v_lshl_add_u32 v216, v5, 9, v216\n"
        "v_add_u32 v230, s67, v216\n"
        "s_waitcnt vmcnt(18)\n"
        "v_mfma_f32_16x16x32_bf16 v[124:127], v[24:27], v[92:95], v[124:127]\n"
        "s_bfe_u32 s27, s28, 0x40000\n"
        "s_lshl_b32 s69, s27, 12\n"
        "s_add_u32 s69, s69, 28672\n"
        "buffer_load_dwordx4 v[200:203], v237, s[60:63], s69 offen offset:0 sc1\n"
        "s_waitcnt vmcnt(18)\n"
        "v_mfma_f32_16x16x32_bf16 v[128:131], v[24:27], v[96:99], v[128:131]\n"
        "s_waitcnt vmcnt(17)\n"
        "v_mfma_f32_16x16x32_bf16 v[132:135], v[24:27], v[100:103], v[132:135]\n"
        "buffer_load_dwordx4 v[204:207], v237, s[60:63], s69 offen offset:1024 sc1\n"
        "v_mfma_f32_16x16x32_bf16 v[136:139], v[28:31], v[88:91], v[136:139]\n"
        "v_mfma_f32_16x16x32_bf16 v[140:143], v[28:31], v[92:95], v[140:143]\n"
        "buffer_load_dwordx4 v[208:211], v237, s[60:63], s69 offen offset:2048 sc1\n"
        "v_mfma_f32_16x16x32_bf16 v[144:147], v[28:31], v[96:99], v[144:147]\n"
        "v_mfma_f32_16x16x32_bf16 v[148:151], v[28:31], v[100:103], v[148:151]\n"
        "buffer_load_dwordx4 v[212:215], v237, s[60:63], s69 offen offset:3072 sc1\n"
        "v_mfma_f32_16x16x32_bf16 v[152:155], v[32:35], v[88:91], v[152:155]\n"
        "v_mfma_f32_16x16x32_bf16 v[156:159], v[32:35], v[92:95], v[156:159]\n"
        "s_bfe_u32 s27, s28, 0x40004\n"
        "s_lshl_b32 s69, s27, 12\n"
        "s_add_u32 s69, s69, 28672\n"
        "buffer_load_dwordx4 v[72:75], v237, s[60:63], s69 offen offset:0 sc1\n"
        "v_mfma_f32_16x16x32_bf16 v[160:163], v[32:35], v[96:99], v[160:163]\n"
        "v_mfma_f32_16x16x32_bf16 v[164:167], v[32:35], v[100:103], v[164:167]\n"
        "buffer_load_dwordx4 v[76:79], v237, s[60:63], s69 offen offset:1024 sc1\n"
        "v_mfma_f32_16x16x32_bf16 v[168:171], v[36:39], v[88:91], v[168:171]\n"
        "v_mfma_f32_16x16x32_bf16 v[172:175], v[36:39], v[92:95], v[172:175]\n"
        "buffer_load_dwordx4 v[80:83], v237, s[60:63], s69 offen offset:2048 sc1\n"
        "v_mfma_f32_16x16x32_bf16 v[176:179], v[36:39], v[96:99], v[176:179]\n"
        "v_mfma_f32_16x16x32_bf16 v[180:183], v[36:39], v[100:103], v[180:183]\n"
        "buffer_load_dwordx4 v[84:87], v237, s[60:63], s69 offen offset:3072 sc1\n"
        "s_waitcnt vmcnt(19)\n"
        "v_mfma_f32_16x16x32_bf16 v[120:123], v[40:43], v[104:107], v[120:123]\n"
        "ds_read_b64_tr_b16 v[8:9], v226 offset:0\n"
        "s_waitcnt vmcnt(18)\n"
        "v_mfma_f32_16x16x32_bf16 v[124:127], v[40:43], v[108:111], v[124:127]\n"
        "ds_read_b64_tr_b16 v[10:11], v226 offset:512\n"
        "s_bfe_u32 s27, s28, 0x40008\n"
        "s_lshl_b32 s69, s27, 12\n"
        "s_add_u32 s69, s69, 28672\n"
        "buffer_load_dwordx4 v[88:91], v237, s[60:63], s69 offen offset:0 sc1\n"
        "s_waitcnt vmcnt(18)\n"
        "v_mfma_f32_16x16x32_bf16 v[128:131], v[40:43], v[112:115], v[128:131]\n"
        "ds_read_b64_tr_b16 v[12:13], v227 offset:0\n"
        "s_waitcnt vmcnt(17)\n"
        "v_mfma_f32_16x16x32_bf16 v[132:135], v[40:43], v[116:119], v[132:135]\n"
        "ds_read_b64_tr_b16 v[14:15], v227 offset:512\n"
        "v_mfma_f32_16x16x32_bf16 v[136:139], v[44:47], v[104:107], v[136:139]\n"
        "ds_read_b64_tr_b16 v[16:17], v232 offset:0\n"
        "v_mfma_f32_16x16x32_bf16 v[140:143], v[44:47], v[108:111], v[140:143]\n"
        "ds_read_b64_tr_b16 v[18:19], v232 offset:512\n"
        "buffer_load_dwordx4 v[92:95], v237, s[60:63], s69 offen offset:1024 sc1\n"
        "v_mfma_f32_16x16x32_bf16 v[144:147], v[44:47], v[112:115], v[144:147]\n"
        "ds_read_b64_tr_b16 v[20:21], v233 offset:0\n"
        "v_mfma_f32_16x16x32_bf16 v[148:151], v[44:47], v[116:119], v[148:151]\n"
        "ds_read_b64_tr_b16 v[22:23], v233 offset:512\n"
        "v_mfma_f32_16x16x32_bf16 v[152:155], v[48:51], v[104:107], v[152:155]\n"
        "v_mfma_f32_16x16x32_bf16 v[156:159], v[48:51], v[108:111], v[156:159]\n"
        "buffer_load_dwordx4 v[96:99], v237, s[60:63], s69 offen offset:2048 sc1\n"
        "v_mfma_f32_16x16x32_bf16 v[160:163], v[48:51], v[112:115], v[160:163]\n"
        "v_mfma_f32_16x16x32_bf16 v[164:167], v[48:51], v[116:119], v[164:167]\n"
        "v_mfma_f32_16x16x32_bf16 v[168:171], v[52:55], v[104:107], v[168:171]\n"
        "v_mfma_f32_16x16x32_bf16 v[172:175], v[52:55], v[108:111], v[172:175]\n"
        "buffer_load_dwordx4 v[100:103], v237, s[60:63], s69 offen offset:3072 sc1\n"
        "v_mfma_f32_16x16x32_bf16 v[176:179], v[52:55], v[112:115], v[176:179]\n"
        "v_mfma_f32_16x16x32_bf16 v[180:183], v[52:55], v[116:119], v[180:183]\n"
        "s_waitcnt vmcnt(11)\n"
        "v_mfma_f32_16x16x32_bf16 v[120:123], v[56:59], v[200:203], v[120:123]\n"
        "ds_read_b64_tr_b16 v[24:25], v218 offset:16384\n"
        "s_waitcnt vmcnt(10)\n"
        "v_mfma_f32_16x16x32_bf16 v[124:127], v[56:59], v[204:207], v[124:127]\n"
        "ds_read_b64_tr_b16 v[26:27], v218 offset:16896\n"
        "s_bfe_u32 s27, s28, 0x4000c\n"
        "s_lshl_b32 s69, s27, 12\n"
        "s_add_u32 s69, s69, 28672\n"
        "buffer_load_dwordx4 v[104:107], v237, s[60:63], s69 offen offset:0 sc1\n"
        "s_waitcnt vmcnt(10)\n"
        "v_mfma_f32_16x16x32_bf16 v[128:131], v[56:59], v[208:211], v[128:131]\n"
        "ds_read_b64_tr_b16 v[28:29], v219 offset:16384\n"
        "s_waitcnt vmcnt(9)\n"
        "v_mfma_f32_16x16x32_bf16 v[132:135], v[56:59], v[212:215], v[132:135]\n"
        "ds_read_b64_tr_b16 v[30:31], v219 offset:16896\n"
        "v_mfma_f32_16x16x32_bf16 v[136:139], v[60:63], v[200:203], v[136:139]\n"
        "ds_read_b64_tr_b16 v[184:185], v222 offset:32768\n"
        "v_mfma_f32_16x16x32_bf16 v[140:143], v[60:63], v[204:207], v[140:143]\n"
        "ds_read_b64_tr_b16 v[32:33], v220 offset:16384\n"
        "buffer_load_dwordx4 v[108:111], v237, s[60:63], s69 offen offset:1024 sc1\n"
        "v_mfma_f32_16x16x32_bf16 v[144:147], v[60:63], v[208:211], v[144:147]\n"
        "ds_read_b64_tr_b16 v[186:187], v222 offset:33280\n"
        "v_mfma_f32_16x16x32_bf16 v[148:151], v[60:63], v[212:215], v[148:151]\n"
        "s_waitcnt lgkmcnt(8)\n"
        "ds_read_b64_tr_b16 v[34:35], v220 offset:16896\n"
        "v_mfma_f32_16x16x32_bf16 v[152:155], v[64:67], v[200:203], v[152:155]\n"
        "ds_read_b64_tr_b16 v[188:189], v223 offset:32768\n"
        "v_mfma_f32_16x16x32_bf16 v[156:159], v[64:67], v[204:207], v[156:159]\n"
        "ds_read_b64_tr_b16 v[36:37], v221 offset:16384\n"
        "buffer_load_dwordx4 v[112:115], v237, s[60:63], s69 offen offset:2048 sc1\n"
        "v_mfma_f32_16x16x32_bf16 v[160:163], v[64:67], v[208:211], v[160:163]\n"
        "ds_read_b64_tr_b16 v[190:191], v223 offset:33280\n"
        "v_mfma_f32_16x16x32_bf16 v[164:167], v[64:67], v[212:215], v[164:167]\n"
        "ds_read_b64_tr_b16 v[38:39], v221 offset:16896\n"
        "v_mfma_f32_16x16x32_bf16 v[168:171], v[68:71], v[200:203], v[168:171]\n"
        "ds_read_b64_tr_b16 v[192:193], v224 offset:32768\n"
        "v_mfma_f32_16x16x32_bf16 v[172:175], v[68:71], v[204:207], v[172:175]\n"
        "buffer_load_dwordx4 v[116:119], v237, s[60:63], s69 offen offset:3072 sc1\n"
        "v_mfma_f32_16x16x32_bf16 v[176:179], v[68:71], v[208:211], v[176:179]\n"
        "ds_read_b64_tr_b16 v[194:195], v224 offset:33280\n"
        "v_mfma_f32_16x16x32_bf16 v[180:183], v[68:71], v[212:215], v[180:183]\n"
        "s_waitcnt vmcnt(11) lgkmcnt(7)\n"
        "v_mfma_f32_16x16x32_bf16 v[120:123], v[184:187], v[72:75], v[120:123]\n"
        "ds_read_b64_tr_b16 v[196:197], v225 offset:32768\n"
        "s_waitcnt vmcnt(10)\n"
        "v_mfma_f32_16x16x32_bf16 v[124:127], v[184:187], v[76:79], v[124:127]\n"
        "ds_read_b64_tr_b16 v[40:41], v218 offset:32768\n"
        "s_bfe_u32 s27, s28, 0x40010\n"
        "s_lshl_b32 s69, s27, 12\n"
        "s_add_u32 s69, s69, 28672\n"
        "buffer_load_dwordx4 v[200:203], v237, s[60:63], s69 offen offset:0 sc1\n"
        "s_waitcnt vmcnt(10)\n"
        "v_mfma_f32_16x16x32_bf16 v[128:131], v[184:187], v[80:83], v[128:131]\n"
        "ds_read_b64_tr_b16 v[198:199], v225 offset:33280\n"
        "s_waitcnt vmcnt(9)\n"
        "v_mfma_f32_16x16x32_bf16 v[132:135], v[184:187], v[84:87], v[132:135]\n"
        "ds_read_b64_tr_b16 v[42:43], v218 offset:33280\n"
        "s_waitcnt lgkmcnt(7)\n"
        "v_mfma_f32_16x16x32_bf16 v[136:139], v[188:191], v[72:75], v[136:139]\n"
        "ds_read_b64_tr_b16 v[184:185], v222 offset:49152\n"
        "v_mfma_f32_16x16x32_bf16 v[140:143], v[188:191], v[76:79], v[140:143]\n"
        "ds_read_b64_tr_b16 v[44:45], v219 offset:32768\n"
        "buffer_load_dwordx4 v[204:207], v237, s[60:63], s69 offen offset:1024 sc1\n"
        "v_mfma_f32_16x16x32_bf16 v[144:147], v[188:191], v[80:83], v[144:147]\n"
        "ds_read_b64_tr_b16 v[186:187], v222 offset:49664\n"
        "v_mfma_f32_16x16x32_bf16 v[148:151], v[188:191], v[84:87], v[148:151]\n"
        "ds_read_b64_tr_b16 v[46:47], v219 offset:33280\n"
        "s_waitcnt lgkmcnt(8)\n"
        "v_mfma_f32_16x16x32_bf16 v[152:155], v[192:195], v[72:75], v[152:155]\n"
        "ds_read_b64_tr_b16 v[188:189], v223 offset:49152\n"
        "v_mfma_f32_16x16x32_bf16 v[156:159], v[192:195], v[76:79], v[156:159]\n"
        "ds_read_b64_tr_b16 v[48:49], v220 offset:32768\n"
        "buffer_load_dwordx4 v[208:211], v237, s[60:63], s69 offen offset:2048 sc1\n"
        "v_mfma_f32_16x16x32_bf16 v[160:163], v[192:195], v[80:83], v[160:163]\n"
        "ds_read_b64_tr_b16 v[190:191], v223 offset:49664\n"
        "v_mfma_f32_16x16x32_bf16 v[164:167], v[192:195], v[84:87], v[164:167]\n"
        "ds_read_b64_tr_b16 v[50:51], v220 offset:33280\n"
        "s_waitcnt lgkmcnt(9)\n"
        "v_mfma_f32_16x16x32_bf16 v[168:171], v[196:199], v[72:75], v[168:171]\n"
        "ds_read_b64_tr_b16 v[192:193], v224 offset:49152\n"
        "v_mfma_f32_16x16x32_bf16 v[172:175], v[196:199], v[76:79], v[172:175]\n"
        "ds_read_b64_tr_b16 v[52:53], v221 offset:32768\n"
        "buffer_load_dwordx4 v[212:215], v237, s[60:63], s69 offen offset:3072 sc1\n"
        "v_mfma_f32_16x16x32_bf16 v[176:179], v[196:199], v[80:83], v[176:179]\n"
        "ds_read_b64_tr_b16 v[194:195], v224 offset:49664\n"
        "v_mfma_f32_16x16x32_bf16 v[180:183], v[196:199], v[84:87], v[180:183]\n"
        "ds_read_b64_tr_b16 v[54:55], v221 offset:33280\n"
        "s_waitcnt vmcnt(11) lgkmcnt(9)\n"
        "v_mfma_f32_16x16x32_bf16 v[120:123], v[184:187], v[88:91], v[120:123]\n"
        "ds_read_b64_tr_b16 v[196:197], v225 offset:49152\n"
        "s_waitcnt vmcnt(10)\n"
        "v_mfma_f32_16x16x32_bf16 v[124:127], v[184:187], v[92:95], v[124:127]\n"
        "ds_read_b64_tr_b16 v[56:57], v226 offset:16384\n"
        "s_bfe_u32 s27, s28, 0x40014\n"
        "s_lshl_b32 s69, s27, 12\n"
        "s_add_u32 s69, s69, 28672\n"
        "buffer_load_dwordx4 v[72:75], v237, s[60:63], s69 offen offset:0 sc1\n"
        "s_waitcnt vmcnt(10)\n"
        "v_mfma_f32_16x16x32_bf16 v[128:131], v[184:187], v[96:99], v[128:131]\n"
        "ds_read_b64_tr_b16 v[198:199], v225 offset:49664\n"
        "s_waitcnt vmcnt(9)\n"
        "v_mfma_f32_16x16x32_bf16 v[132:135], v[184:187], v[100:103], v[132:135]\n"
        "ds_read_b64_tr_b16 v[58:59], v226 offset:16896\n"
        "s_waitcnt lgkmcnt(9)\n"
        "v_mfma_f32_16x16x32_bf16 v[136:139], v[188:191], v[88:91], v[136:139]\n"
        "ds_read_b64_tr_b16 v[184:185], v218 offset:49152\n"
        "v_mfma_f32_16x16x32_bf16 v[140:143], v[188:191], v[92:95], v[140:143]\n"
        "ds_read_b64_tr_b16 v[60:61], v227 offset:16384\n"
        "buffer_load_dwordx4 v[76:79], v237, s[60:63], s69 offen offset:1024 sc1\n"
        "v_mfma_f32_16x16x32_bf16 v[144:147], v[188:191], v[96:99], v[144:147]\n"
        "ds_read_b64_tr_b16 v[186:187], v218 offset:49664\n"
        "v_mfma_f32_16x16x32_bf16 v[148:151], v[188:191], v[100:103], v[148:151]\n"
        "ds_read_b64_tr_b16 v[62:63], v227 offset:16896\n"
        "s_waitcnt lgkmcnt(9)\n"
        "v_mfma_f32_16x16x32_bf16 v[152:155], v[192:195], v[88:91], v[152:155]\n"
        "ds_read_b64_tr_b16 v[188:189], v219 offset:49152\n"
        "v_mfma_f32_16x16x32_bf16 v[156:159], v[192:195], v[92:95], v[156:159]\n"
        "ds_read_b64_tr_b16 v[64:65], v232 offset:16384\n"
        "buffer_load_dwordx4 v[80:83], v237, s[60:63], s69 offen offset:2048 sc1\n"
        "v_mfma_f32_16x16x32_bf16 v[160:163], v[192:195], v[96:99], v[160:163]\n"
        "ds_read_b64_tr_b16 v[190:191], v219 offset:49664\n"
        "v_mfma_f32_16x16x32_bf16 v[164:167], v[192:195], v[100:103], v[164:167]\n"
        "ds_read_b64_tr_b16 v[66:67], v232 offset:16896\n"
        "s_waitcnt lgkmcnt(9)\n"
        "v_mfma_f32_16x16x32_bf16 v[168:171], v[196:199], v[88:91], v[168:171]\n"
        "ds_read_b64_tr_b16 v[192:193], v220 offset:49152\n"
        "v_mfma_f32_16x16x32_bf16 v[172:175], v[196:199], v[92:95], v[172:175]\n"
        "ds_read_b64_tr_b16 v[68:69], v233 offset:16384\n"
        "buffer_load_dwordx4 v[84:87], v237, s[60:63], s69 offen offset:3072 sc1\n"
        "v_mfma_f32_16x16x32_bf16 v[176:179], v[196:199], v[96:99], v[176:179]\n"
        "ds_read_b64_tr_b16 v[194:195], v220 offset:49664\n"
        "v_mfma_f32_16x16x32_bf16 v[180:183], v[196:199], v[100:103], v[180:183]\n"
        "ds_read_b64_tr_b16 v[70:71], v233 offset:16896\n"
        "s_waitcnt vmcnt(11) lgkmcnt(9)\n"
        "v_mfma_f32_16x16x32_bf16 v[120:123], v[184:187], v[104:107], v[120:123]\n"
        "ds_read_b64_tr_b16 v[196:197], v221 offset:49152\n"
        "s_waitcnt vmcnt(10)\n"
        "v_mfma_f32_16x16x32_bf16 v[124:127], v[184:187], v[108:111], v[124:127]\n"
        "s_bfe_u32 s27, s28, 0x40018\n"
        "s_lshl_b32 s69, s27, 12\n"
        "s_add_u32 s69, s69, 28672\n"
        "buffer_load_dwordx4 v[88:91], v237, s[60:63], s69 offen offset:0 sc1\n"
        "s_waitcnt vmcnt(10)\n"
        "v_mfma_f32_16x16x32_bf16 v[128:131], v[184:187], v[112:115], v[128:131]\n"
        "ds_read_b64_tr_b16 v[198:199], v221 offset:49664\n"
        "s_waitcnt vmcnt(9)\n"
        "v_mfma_f32_16x16x32_bf16 v[132:135], v[184:187], v[116:119], v[132:135]\n"
        "s_waitcnt lgkmcnt(7)\n"
        "v_mfma_f32_16x16x32_bf16 v[136:139], v[188:191], v[104:107], v[136:139]\n"
        "ds_read_b64_tr_b16 v[184:185], v222 offset:0\n"
        "v_mfma_f32_16x16x32_bf16 v[140:143], v[188:191], v[108:111], v[140:143]\n"
        "buffer_load_dwordx4 v[92:95], v237, s[60:63], s69 offen offset:1024 sc1\n"
        "v_mfma_f32_16x16x32_bf16 v[144:147], v[188:191], v[112:115], v[144:147]\n"
        "s_nop 0\n"
        "s_cbranch_execz Lback\n"
        "ds_read_b64_tr_b16 v[186:187], v222 offset:512\n"
        "v_mfma_f32_16x16x32_bf16 v[148:151], v[188:191], v[116:119], v[148:151]\n"
        "s_waitcnt lgkmcnt(5)\n"
        "v_mfma_f32_16x16x32_bf16 v[152:155], v[192:195], v[104:107], v[152:155]\n"
        "ds_read_b64_tr_b16 v[188:189], v223 offset:0\n"
        "v_mfma_f32_16x16x32_bf16 v[156:159], v[192:195], v[108:111], v[156:159]\n"
        "buffer_load_dwordx4 v[96:99], v237, s[60:63], s69 offen offset:2048 sc1\n"
        "v_mfma_f32_16x16x32_bf16 v[160:163], v[192:195], v[112:115], v[160:163]\n"
        "ds_read_b64_tr_b16 v[190:191], v223 offset:512\n"
        "v_mfma_f32_16x16x32_bf16 v[164:167], v[192:195], v[116:119], v[164:167]\n"
        "s_waitcnt lgkmcnt(4)\n"
        "v_mfma_f32_16x16x32_bf16 v[168:171], v[196:199], v[104:107], v[168:171]\n"
        "ds_read_b64_tr_b16 v[192:193], v224 offset:0\n"
        "v_mfma_f32_16x16x32_bf16 v[172:175], v[196:199], v[108:111], v[172:175]\n"
        "buffer_load_dwordx4 v[100:103], v237, s[60:63], s69 offen offset:3072 sc1\n"
        "v_mfma_f32_16x16x32_bf16 v[176:179], v[196:199], v[112:115], v[176:179]\n"
        "ds_read_b64_tr_b16 v[194:195], v224 offset:512\n"
        "v_mfma_f32_16x16x32_bf16 v[180:183], v[196:199], v[116:119], v[180:183]\n"
        "s_waitcnt vmcnt(11) lgkmcnt(4)\n"
        "v_mfma_f32_16x16x32_bf16 v[120:123], v[184:187], v[200:203], v[120:123]\n"
        "ds_read_b64_tr_b16 v[196:197], v225 offset:0\n"
        "s_waitcnt vmcnt(10)\n"
        "v_mfma_f32_16x16x32_bf16 v[124:127], v[184:187], v[204:207], v[124:127]\n"
        "s_barrier\n"
        "s_waitcnt vmcnt(9)\n"
        "v_mfma_f32_16x16x32_bf16 v[128:131], v[184:187], v[208:211], v[128:131]\n"
        "ds_read_b64_tr_b16 v[198:199], v225 offset:512\n"
        "s_waitcnt vmcnt(8)\n"
        "v_mfma_f32_16x16x32_bf16 v[132:135], v[184:187], v[212:215], v[132:135]\n"
        "s_mul_i32 s66, s11, 4096\n"
        "s_add_u32 s66, s66, 57344\n"
        "buffer_load_dwordx4 v[104:107], v237, s[60:63], s66 offen offset:0 sc1\n"
        "s_waitcnt lgkmcnt(4)\n"
        "v_mfma_f32_16x16x32_bf16 v[136:139], v[188:191], v[200:203], v[136:139]\n"
        "ds_read_b64_tr_b16 v[184:185], v222 offset:16384\n"
        "v_mfma_f32_16x16x32_bf16 v[140:143], v[188:191], v[204:207], v[140:143]\n"
        "v_mfma_f32_16x16x32_bf16 v[144:147], v[188:191], v[208:211], v[144:147]\n"
        "ds_read_b64_tr_b16 v[186:187], v222 offset:16896\n"
        "v_mfma_f32_16x16x32_bf16 v[148:151], v[188:191], v[212:215], v[148:151]\n"
        "s_mul_i32 s66, s12, 4096\n"
        "s_add_u32 s66, s66, 57344\n"
        "buffer_load_dwordx4 v[108:111], v237, s[60:63], s66 offen offset:0 sc1\n"
        "s_waitcnt lgkmcnt(4)\n"
        "v_mfma_f32_16x16x32_bf16 v[152:155], v[192:195], v[200:203], v[152:155]\n"
        "ds_read_b64_tr_b16 v[188:189], v223 offset:16384\n"
        "v_mfma_f32_16x16x32_bf16 v[156:159], v[192:195], v[204:207], v[156:159]\n"
        "s_mul_i32 s66, s13, 4096\n"
        "s_add_u32 s66, s66, 57344\n"
        "buffer_load_dwordx4 v[112:115], v237, s[60:63], s66 offen offset:0 sc1\n"
        "v_mfma_f32_16x16x32_bf16 v[160:163], v[192:195], v[208:211], v[160:163]\n"
        "ds_read_b64_tr_b16 v[190:191], v223 offset:16896\n"
        "v_mfma_f32_16x16x32_bf16 v[164:167], v[192:195], v[212:215], v[164:167]\n"
        "s_waitcnt lgkmcnt(4)\n"
        "v_mfma_f32_16x16x32_bf16 v[168:171], v[196:199], v[200:203], v[168:171]\n"
        "ds_read_b64_tr_b16 v[192:193], v224 offset:16384\n"
        "v_mfma_f32_16x16x32_bf16 v[172:175], v[196:199], v[204:207], v[172:175]\n"
        "s_mul_i32 s66, s14, 4096\n"
        "s_add_u32 s66, s66, 57344\n"
        "buffer_load_dwordx4 v[116:119], v237, s[60:63], s66 offen offset:0 sc1\n"
        "v_mfma_f32_16x16x32_bf16 v[176:179], v[196:199], v[208:211], v[176:179]\n"
        "ds_read_b64_tr_b16 v[194:195], v224 offset:16896\n"
        "v_mfma_f32_16x16x32_bf16 v[180:183], v[196:199], v[212:215], v[180:183]\n"
        "s_waitcnt vmcnt(11) lgkmcnt(4)\n"
        "v_mfma_f32_16x16x32_bf16 v[120:123], v[184:187], v[72:75], v[120:123]\n"
        "ds_read_b64_tr_b16 v[196:197], v225 offset:16384\n"
        "s_waitcnt vmcnt(10)\n"
        "v_mfma_f32_16x16x32_bf16 v[124:127], v[184:187], v[76:79], v[124:127]\n"
        "s_mul_i32 s66, s11, 4096\n"
        "s_add_u32 s66, s66, 57344\n"
        "buffer_load_dwordx4 v[200:203], v237, s[60:63], s66 offen offset:1024 sc1\n"
        "s_waitcnt vmcnt(10)\n"
        "v_mfma_f32_16x16x32_bf16 v[128:131], v[184:187], v[80:83], v[128:131]\n"
        "ds_read_b64_tr_b16 v[198:199], v225 offset:16896\n"
        "s_waitcnt vmcnt(9)\n"
        "v_mfma_f32_16x16x32_bf16 v[132:135], v[184:187], v[84:87], v[132:135]\n"
        "s_waitcnt lgkmcnt(4)\n"
        "v_mfma_f32_16x16x32_bf16 v[136:139], v[188:191], v[72:75], v[136:139]\n"
        "ds_read_b64_tr_b16 v[184:185], v218 offset:0\n"
        "v_mfma_f32_16x16x32_bf16 v[140:143], v[188:191], v[76:79], v[140:143]\n"
        "s_mul_i32 s66, s12, 4096\n"
        "s_add_u32 s66, s66, 57344\n"
        "buffer_load_dwordx4 v[204:207], v237, s[60:63], s66 offen offset:1024 sc1\n"
        "v_mfma_f32_16x16x32_bf16 v[144:147], v[188:191], v[80:83], v[144:147]\n"
        "ds_read_b64_tr_b16 v[186:187], v218 offset:512\n"
        "v_mfma_f32_16x16x32_bf16 v[148:151], v[188:191], v[84:87], v[148:151]\n"
        "s_waitcnt lgkmcnt(4)\n"
        "v_mfma_f32_16x16x32_bf16 v[152:155], v[192:195], v[72:75], v[152:155]\n"
        "ds_read_b64_tr_b16 v[188:189], v219 offset:0\n"
        "v_mfma_f32_16x16x32_bf16 v[156:159], v[192:195], v[76:79], v[156:159]\n"
        "s_mul_i32 s66, s13, 4096\n"
        "s_add_u32 s66, s66, 57344\n"
        "buffer_load_dwordx4 v[208:211], v237, s[60:63], s66 offen offset:1024 sc1\n"
        "v_mfma_f32_16x16x32_bf16 v[160:163], v[192:195], v[80:83], v[160:163]\n"
        "ds_read_b64_tr_b16 v[190:191], v219 offset:512\n"
        "v_mfma_f32_16x16x32_bf16 v[164:167], v[192:195], v[84:87], v[164:167]\n"
        "s_waitcnt lgkmcnt(4)\n"
        "v_mfma_f32_16x16x32_bf16 v[168:171], v[196:199], v[72:75], v[168:171]\n"
        "ds_read_b64_tr_b16 v[192:193], v220 offset:0\n"
        "v_mfma_f32_16x16x32_bf16 v[172:175], v[196:199], v[76:79], v[172:175]\n"
        "s_mul_i32 s66, s14, 4096\n"
        "s_add_u32 s66, s66, 57344\n"
        "buffer_load_dwordx4 v[212:215], v237, s[60:63], s66 offen offset:1024 sc1\n"
        "v_mfma_f32_16x16x32_bf16 v[176:179], v[196:199], v[80:83], v[176:179]\n"
        "ds_read_b64_tr_b16 v[194:195], v220 offset:512\n"
        "v_mfma_f32_16x16x32_bf16 v[180:183], v[196:199], v[84:87], v[180:183]\n"
        "s_waitcnt vmcnt(11) lgkmcnt(4)\n"
        "v_mfma_f32_16x16x32_bf16 v[120:123], v[184:187], v[88:91], v[120:123]\n"
        "ds_read_b64_tr_b16 v[196:197], v221 offset:0\n"
        "s_waitcnt vmcnt(10)\n"
        "v_mfma_f32_16x16x32_bf16 v[124:127], v[184:187], v[92:95], v[124:127]\n"
        "s_mul_i32 s66, s11, 4096\n"
        "s_add_u32 s66, s66, 57344\n"
        "buffer_load_dwordx4 v[72:75], v237, s[60:63], s66 offen offset:2048 sc1\n"
        "s_waitcnt vmcnt(10)\n"
        "v_mfma_f32_16x16x32_bf16 v[128:131], v[184:187], v[96:99], v[128:131]\n"
        "ds_read_b64_tr_b16 v[198:199], v221 offset:512\n"
        "s_waitcnt vmcnt(9)\n"
        "v_mfma_f32_16x16x32_bf16 v[132:135], v[184:187], v[100:103], v[132:135]\n"
        "s_waitcnt lgkmcnt(4)\n"
        "v_mfma_f32_16x16x32_bf16 v[136:139], v[188:191], v[88:91], v[136:139]\n"
        "v_mfma_f32_16x16x32_bf16 v[140:143], v[188:191], v[92:95], v[140:143]\n"
        "s_mul_i32 s66, s12, 4096\n"
        "s_add_u32 s66, s66, 57344\n"
        "buffer_load_dwordx4 v[76:79], v237, s[60:63], s66 offen offset:2048 sc1\n"
        "v_mfma_f32_16x16x32_bf16 v[144:147], v[188:191], v[96:99], v[144:147]\n"
        "v_mfma_f32_16x16x32_bf16 v[148:151], v[188:191], v[100:103], v[148:151]\n"
        "s_waitcnt lgkmcnt(2)\n"
        "v_mfma_f32_16x16x32_bf16 v[152:155], v[192:195], v[88:91], v[152:155]\n"
        "v_mfma_f32_16x16x32_bf16 v[156:159], v[192:195], v[92:95], v[156:159]\n"
        "s_mul_i32 s66, s13, 4096\n"
        "s_add_u32 s66, s66, 57344\n"
        "buffer_load_dwordx4 v[80:83], v237, s[60:63], s66 offen offset:2048 sc1\n"
        "v_mfma_f32_16x16x32_bf16 v[160:163], v[192:195], v[96:99], v[160:163]\n"
        "v_mfma_f32_16x16x32_bf16 v[164:167], v[192:195], v[100:103], v[164:167]\n"
        "s_waitcnt lgkmcnt(0)\n"
        "v_mfma_f32_16x16x32_bf16 v[168:171], v[196:199], v[88:91], v[168:171]\n"
        "v_mfma_f32_16x16x32_bf16 v[172:175], v[196:199], v[92:95], v[172:175]\n"
        "s_mul_i32 s66, s14, 4096\n"
        "s_add_u32 s66, s66, 57344\n"
        "buffer_load_dwordx4 v[84:87], v237, s[60:63], s66 offen offset:2048 sc1\n"
        "v_mfma_f32_16x16x32_bf16 v[176:179], v[196:199], v[96:99], v[176:179]\n"
        "v_mfma_f32_16x16x32_bf16 v[180:183], v[196:199], v[100:103], v[180:183]\n"
        "s_waitcnt vmcnt(11)\n"
        "v_mfma_f32_16x16x32_bf16 v[120:123], v[8:11], v[104:107], v[120:123]\n"
        "v_lshlrev_b32 v216, 16, v246\n"
        "s_waitcnt vmcnt(10)\n"
        "v_mfma_f32_16x16x32_bf16 v[120:123], v[24:27], v[108:111], v[120:123]\n"
        "s_mul_i32 s66, s11, 4096\n"
        "s_add_u32 s66, s66, 57344\n"
        "buffer_load_dwordx4 v[88:91], v237, s[60:63], s66 offen offset:3072 sc1\n"
        "s_waitcnt vmcnt(10)\n"
        "v_mfma_f32_16x16x32_bf16 v[120:123], v[40:43], v[112:115], v[120:123]\n"
        "v_and_b32 v217, s78, v246\n"
        "s_waitcnt vmcnt(9)\n"
        "v_mfma_f32_16x16x32_bf16 v[120:123], v[56:59], v[116:119], v[120:123]\n"
        "v_mfma_f32_16x16x32_bf16 v[136:139], v[12:15], v[104:107], v[136:139]\n"
        "v_lshlrev_b32 v218, 16, v247\n"
        "v_mfma_f32_16x16x32_bf16 v[136:139], v[28:31], v[108:111], v[136:139]\n"
        "s_mul_i32 s66, s12, 4096\n"
        "s_add_u32 s66, s66, 57344\n"
        "buffer_load_dwordx4 v[92:95], v237, s[60:63], s66 offen offset:3072 sc1\n"
        "v_mfma_f32_16x16x32_bf16 v[136:139], v[44:47], v[112:115], v[136:139]\n"
        "v_and_b32 v219, s78, v247\n"
        "v_mfma_f32_16x16x32_bf16 v[136:139], v[60:63], v[116:119], v[136:139]\n"
        "ds_write_b128 v250, v[120:123] offset:0\n"
        "v_mfma_f32_16x16x32_bf16 v[152:155], v[16:19], v[104:107], v[152:155]\n"
        "v_lshlrev_b32 v220, 16, v248\n"
        "v_mfma_f32_16x16x32_bf16 v[152:155], v[32:35], v[108:111], v[152:155]\n"
        "s_mul_i32 s66, s13, 4096\n"
        "s_add_u32 s66, s66, 57344\n"
        "buffer_load_dwordx4 v[96:99], v237, s[60:63], s66 offen offset:3072 sc1\n"
        "v_mfma_f32_16x16x32_bf16 v[152:155], v[48:51], v[112:115], v[152:155]\n"
        "v_and_b32 v221, s78, v248\n"
        "v_mfma_f32_16x16x32_bf16 v[152:155], v[64:67], v[116:119], v[152:155]\n"
        "ds_write_b128 v251, v[136:139] offset:1024\n"
        "v_mfma_f32_16x16x32_bf16 v[168:171], v[20:23], v[104:107], v[168:171]\n"
        "v_lshlrev_b32 v222, 16, v249\n"
        "v_mfma_f32_16x16x32_bf16 v[168:171], v[36:39], v[108:111], v[168:171]\n"
        "s_mul_i32 s66, s14, 4096\n"
        "s_add_u32 s66, s66, 57344\n"
        "buffer_load_dwordx4 v[100:103], v237, s[60:63], s66 offen offset:3072 sc1\n"
        "v_mfma_f32_16x16x32_bf16 v[168:171], v[52:55], v[112:115], v[168:171]\n"
        "v_and_b32 v223, s78, v249\n"
        "v_mfma_f32_16x16x32_bf16 v[168:171], v[68:71], v[116:119], v[168:171]\n"
        "ds_write_b128 v252, v[152:155] offset:2048\n"
        "s_waitcnt vmcnt(11)\n"
        "v_mfma_f32_16x16x32_bf16 v[124:127], v[8:11], v[200:203], v[124:127]\n"
        "s_waitcnt vmcnt(10)\n"
        "v_mfma_f32_16x16x32_bf16 v[124:127], v[24:27], v[204:207], v[124:127]\n"
        "s_waitcnt vmcnt(9)\n"
        "v_mfma_f32_16x16x32_bf16 v[124:127], v[40:43], v[208:211], v[124:127]\n"
        "s_waitcnt vmcnt(8)\n"
        "v_mfma_f32_16x16x32_bf16 v[124:127], v[56:59], v[212:215], v[124:127]\n"
        "ds_write_b128 v253, v[168:171] offset:3072\n"
        "v_mfma_f32_16x16x32_bf16 v[140:143], v[12:15], v[200:203], v[140:143]\n"
        "v_mfma_f32_16x16x32_bf16 v[140:143], v[28:31], v[204:207], v[140:143]\n"
        "v_mfma_f32_16x16x32_bf16 v[140:143], v[44:47], v[208:211], v[140:143]\n"
        "v_mfma_f32_16x16x32_bf16 v[140:143], v[60:63], v[212:215], v[140:143]\n"
        "s_nop 2\n"
        "ds_write_b128 v250, v[124:127] offset:16384\n"
        "v_mfma_f32_16x16x32_bf16 v[156:159], v[16:19], v[200:203], v[156:159]\n"
        "v_mfma_f32_16x16x32_bf16 v[156:159], v[32:35], v[204:207], v[156:159]\n"
        "v_mfma_f32_16x16x32_bf16 v[156:159], v[48:51], v[208:211], v[156:159]\n"
        "v_mfma_f32_16x16x32_bf16 v[156:159], v[64:67], v[212:215], v[156:159]\n"
        "ds_write_b128 v251, v[140:143] offset:17408\n"
        "v_mfma_f32_16x16x32_bf16 v[172:175], v[20:23], v[200:203], v[172:175]\n"
        "v_mfma_f32_16x16x32_bf16 v[172:175], v[36:39], v[204:207], v[172:175]\n"
        "v_mfma_f32_16x16x32_bf16 v[172:175], v[52:55], v[208:211], v[172:175]\n"
        "v_mfma_f32_16x16x32_bf16 v[172:175], v[68:71], v[212:215], v[172:175]\n"
        "s_nop 2\n"
        "ds_write_b128 v252, v[156:159] offset:18432\n"
        "s_waitcnt vmcnt(7)\n"
        "v_mfma_f32_16x16x32_bf16 v[128:131], v[8:11], v[72:75], v[128:131]\n"
        "s_waitcnt vmcnt(6)\n"
        "v_mfma_f32_16x16x32_bf16 v[128:131], v[24:27], v[76:79], v[128:131]\n"
        "s_waitcnt vmcnt(5)\n"
        "v_mfma_f32_16x16x32_bf16 v[128:131], v[40:43], v[80:83], v[128:131]\n"
        "s_waitcnt vmcnt(4)\n"
        "v_mfma_f32_16x16x32_bf16 v[128:131], v[56:59], v[84:87], v[128:131]\n"
        "ds_write_b128 v253, v[172:175] offset:19456\n"
        "v_mfma_f32_16x16x32_bf16 v[144:147], v[12:15], v[72:75], v[144:147]\n"
        "v_mfma_f32_16x16x32_bf16 v[144:147], v[28:31], v[76:79], v[144:147]\n"
        "v_mfma_f32_16x16x32_bf16 v[144:147], v[44:47], v[80:83], v[144:147]\n"
        "v_mfma_f32_16x16x32_bf16 v[144:147], v[60:63], v[84:87], v[144:147]\n"
        "s_nop 2\n"
        "ds_write_b128 v250, v[128:131] offset:32768\n"
        "v_mfma_f32_16x16x32_bf16 v[160:163], v[16:19], v[72:75], v[160:163]\n"
        "v_mfma_f32_16x16x32_bf16 v[160:163], v[32:35], v[76:79], v[160:163]\n"
        "v_mfma_f32_16x16x32_bf16 v[160:163], v[48:51], v[80:83], v[160:163]\n"
        "v_mfma_f32_16x16x32_bf16 v[160:163], v[64:67], v[84:87], v[160:163]\n"
        "ds_write_b128 v251, v[144:147] offset:33792\n"
        "v_mfma_f32_16x16x32_bf16 v[176:179], v[20:23], v[72:75], v[176:179]\n"
        "v_mfma_f32_16x16x32_bf16 v[176:179], v[36:39], v[76:79], v[176:179]\n"
        "v_mfma_f32_16x16x32_bf16 v[176:179], v[52:55], v[80:83], v[176:179]\n"
        "v_mfma_f32_16x16x32_bf16 v[176:179], v[68:71], v[84:87], v[176:179]\n"
        "s_nop 2\n"
        "ds_write_b128 v252, v[160:163] offset:34816\n"
        "s_waitcnt vmcnt(3)\n"
        "v_mfma_f32_16x16x32_bf16 v[132:135], v[8:11], v[88:91], v[132:135]\n"
        "s_waitcnt vmcnt(2)\n"
        "v_mfma_f32_16x16x32_bf16 v[132:135], v[24:27], v[92:95], v[132:135]\n"
        "s_waitcnt vmcnt(1)\n"
        "v_mfma_f32_16x16x32_bf16 v[132:135], v[40:43], v[96:99], v[132:135]\n"
        "s_waitcnt vmcnt(0)\n"
        "v_mfma_f32_16x16x32_bf16 v[132:135], v[56:59], v[100:103], v[132:135]\n"
        "ds_write_b128 v253, v[176:179] offset:35840\n"
        "v_mfma_f32_16x16x32_bf16 v[148:151], v[12:15], v[88:91], v[148:151]\n"
        "v_mfma_f32_16x16x32_bf16 v[148:151], v[28:31], v[92:95], v[148:151]\n"
        "v_mfma_f32_16x16x32_bf16 v[148:151], v[44:47], v[96:99], v[148:151]\n"
        "v_mfma_f32_16x16x32_bf16 v[148:151], v[60:63], v[100:103], v[148:151]\n"
        "s_nop 2\n"
        "ds_write_b128 v250, v[132:135] offset:49152\n"
        "v_mfma_f32_16x16x32_bf16 v[164:167], v[16:19], v[88:91], v[164:167]\n"
        "v_mfma_f32_16x16x32_bf16 v[164:167], v[32:35], v[92:95], v[164:167]\n"
        "v_mfma_f32_16x16x32_bf16 v[164:167], v[48:51], v[96:99], v[164:167]\n"
        "v_mfma_f32_16x16x32_bf16 v[164:167], v[64:67], v[100:103], v[164:167]\n"
        "ds_write_b128 v251, v[148:151] offset:50176\n"
        "v_mfma_f32_16x16x32_bf16 v[180:183], v[20:23], v[88:91], v[180:183]\n"
        "v_mfma_f32_16x16x32_bf16 v[180:183], v[36:39], v[92:95], v[180:183]\n"
        "v_mfma_f32_16x16x32_bf16 v[180:183], v[52:55], v[96:99], v[180:183]\n"
        "v_mfma_f32_16x16x32_bf16 v[180:183], v[68:71], v[100:103], v[180:183]\n"
        "s_nop 2\n"
        "ds_write_b128 v252, v[164:167] offset:51200\n"
        "s_waitcnt lgkmcnt(8)\n"
        "s_nop 2\n"
        "ds_write_b128 v253, v[180:183] offset:52224\n"
        "s_waitcnt lgkmcnt(0)\n"
        "s_barrier\n"
        "ds_read_b128 v[8:11], v230 offset:0\n"
        "ds_read_b128 v[12:15], v230 offset:256\n"
        "ds_read_b128 v[16:19], v230 offset:4096\n"
        "ds_read_b128 v[20:23], v230 offset:4352\n"
        "ds_read_b128 v[24:27], v230 offset:8192\n"
        "ds_read_b128 v[28:31], v230 offset:8448\n"
        "ds_read_b128 v[32:35], v230 offset:12288\n"
        "ds_read_b128 v[36:39], v230 offset:12544\n"
        "s_waitcnt lgkmcnt(7)\n"
        "v_pk_add_f32 v[216:217], v[216:217], v[8:9]\n"
        "v_pk_add_f32 v[218:219], v[218:219], v[10:11]\n"
        "s_waitcnt lgkmcnt(6)\n"
        "v_pk_add_f32 v[220:221], v[220:221], v[12:13]\n"
        "v_pk_add_f32 v[222:223], v[222:223], v[14:15]\n"
        "s_waitcnt lgkmcnt(5)\n"
        "v_pk_add_f32 v[216:217], v[216:217], v[16:17]\n"
        "v_pk_add_f32 v[218:219], v[218:219], v[18:19]\n"
        "s_waitcnt lgkmcnt(4)\n"
        "v_pk_add_f32 v[220:221], v[220:221], v[20:21]\n"
        "v_pk_add_f32 v[222:223], v[222:223], v[22:23]\n"
        "s_waitcnt lgkmcnt(3)\n"
        "v_pk_add_f32 v[216:217], v[216:217], v[24:25]\n"
        "v_pk_add_f32 v[218:219], v[218:219], v[26:27]\n"
        "s_waitcnt lgkmcnt(2)\n"
        "v_pk_add_f32 v[220:221], v[220:221], v[28:29]\n"
        "v_pk_add_f32 v[222:223], v[222:223], v[30:31]\n"
        "s_waitcnt lgkmcnt(1)\n"
        "v_pk_add_f32 v[216:217], v[216:217], v[32:33]\n"
        "v_pk_add_f32 v[218:219], v[218:219], v[34:35]\n"
        "s_waitcnt lgkmcnt(0)\n"
        "v_pk_add_f32 v[220:221], v[220:221], v[36:37]\n"
        "v_pk_add_f32 v[222:223], v[222:223], v[38:39]\n"
        "v_cvt_pk_bf16_f32 v246, v216, v217\n"
        "v_cvt_pk_bf16_f32 v247, v218, v219\n"
        "v_cvt_pk_bf16_f32 v248, v220, v221\n"
        "v_cvt_pk_bf16_f32 v249, v222, v223\n"
        "buffer_store_dwordx4 v[246:249], v239, s[56:59], 0 offen\n"
        "s_branch Lend\n"
        "Lrole_Y:\n"
        "Lrole_Z:\n"
        "s_cmp_eq_u32 s96, 0\n"
        "s_cbranch_scc1 Lrole_A\n"
        "s_setprio 0\n"
        "s_lshl_b32 s75, s76, 2\n"
        "s_add_u32 s75, s75, s97\n"
        "s_lshl_b32 s74, s72, 1\n"
        "s_add_u32 s74, s74, s96\n"
        "s_lshl_b32 s80, s74, 1\n"
        "v_bfe_u32 v233, v1, 2, 2\n"
        "v_and_b32 v234, 1, v4\n"
        "v_lshl_add_u32 v234, v234, 2, v233\n"
        "v_lshlrev_b32 v234, 5, v234\n"
        "v_lshl_add_u32 v233, v4, 3, v233\n"
        "v_lshlrev_b32 v233, 9, v233\n"
        "v_and_b32 v235, 3, v1\n"
        "s_mul_i32 s67, s97, 112\n"
        "s_lshl_b32 s68, s76, 6\n"
        "s_add_u32 s67, s67, s68\n"
        "v_lshl_add_u32 v235, v235, 3, s67\n"
        "v_add_u32 v220, 0, v235\n"
        "v_xor_b32 v220, v220, v234\n"
        "v_add_u32 v220, v220, v233\n"
        "v_add_u32 v221, 32, v235\n"
        "v_xor_b32 v221, v221, v234\n"
        "v_add_u32 v221, v221, v233\n"
        "v_add_u32 v222, 56, v235\n"
        "v_xor_b32 v222, v222, v234\n"
        "v_add_u32 v222, v222, v233\n"
        "v_add_u32 v223, 88, v235\n"
        "v_min_u32 v223, 504, v223\n"
        "v_xor_b32 v223, v223, v234\n"
        "v_add_u32 v223, v223, v233\n"
        "v_add_u32 v216, 0, v220\n"
        "v_add_u32 v217, 0, v221\n"
        "v_add_u32 v218, 0, v222\n"
        "v_add_u32 v219, 0, v223\n"
        "s_waitcnt vmcnt(5) lgkmcnt(0)\n"
        "s_barrier\n"
        "ds_read_b128 v[72:75], v250 offset:0\n"
        "ds_read_b64_tr_b16 v[104:105], v216 offset:0\n"
        "ds_read_b64_tr_b16 v[106:107], v216 offset:2048\n"
        "ds_read_b128 v[76:79], v252 offset:0\n"
        "ds_read_b64_tr_b16 v[108:109], v217 offset:0\n"
        "ds_read_b64_tr_b16 v[110:111], v217 offset:2048\n"
        "ds_read_b128 v[80:83], v250 offset:2048\n"
        "ds_read_b64_tr_b16 v[112:113], v218 offset:0\n"
        "ds_read_b64_tr_b16 v[114:115], v218 offset:2048\n"
        "ds_read_b128 v[84:87], v252 offset:2048\n"
        "ds_read_b64_tr_b16 v[116:117], v219 offset:0\n"
        "ds_read_b64_tr_b16 v[118:119], v219 offset:2048\n"
        "v_add_u32 v216, 16384, v220\n"
        "v_add_u32 v217, 16384, v221\n"
        "v_add_u32 v218, 16384, v222\n"
        "v_add_u32 v219, 16384, v223\n"
        "s_waitcnt vmcnt(3) lgkmcnt(0)\n"
        "s_barrier\n"
        "v_readfirstlane_b32 s67, v244\n"
        "v_readfirstlane_b32 s68, v245\n"
        "s_mul_hi_u32 s71, s67, 0x4c957f2d\n"
        "s_mul_i32 s69, s67, 0x5851f42d\n"
        "s_add_u32 s71, s71, s69\n"
        "s_mul_i32 s69, s68, 0x4c957f2d\n"
        "s_add_u32 s71, s71, s69\n"
        "s_mul_i32 s70, s67, 0x4c957f2d\n"
        "s_add_u32 s70, s70, 0xf767814f\n"
        "s_addc_u32 s71, s71, 0x14057b7e\n"
        "v_mov_b32 v242, s70\n"
        "v_mov_b32 v243, s71\n"
        "v_mfma_f32_16x16x32_bf16 v[8:11], v[104:107], v[72:75], 0\n"
        "ds_read_b128 v[88:91], v251 offset:0\n"
        "v_mfma_f32_16x16x32_bf16 v[24:27], v[108:111], v[72:75], 0\n"
        "ds_read_b64_tr_b16 v[120:121], v216 offset:0\n"
        "v_mfma_f32_16x16x32_bf16 v[40:43], v[112:115], v[72:75], 0\n"
        "ds_read_b64_tr_b16 v[122:123], v216 offset:2048\n"
        "v_mfma_f32_16x16x32_bf16 v[56:59], v[116:119], v[72:75], 0\n"
        "ds_read_b128 v[92:95], v253 offset:0\n"
        "v_mfma_f32_16x16x32_bf16 v[12:15], v[104:107], v[76:79], 0\n"
        "ds_read_b64_tr_b16 v[124:125], v217 offset:0\n"
        "v_mfma_f32_16x16x32_bf16 v[28:31], v[108:111], v[76:79], 0\n"
        "ds_read_b64_tr_b16 v[126:127], v217 offset:2048\n"
        "v_mfma_f32_16x16x32_bf16 v[44:47], v[112:115], v[76:79], 0\n"
        "ds_read_b128 v[96:99], v251 offset:2048\n"
        "v_mfma_f32_16x16x32_bf16 v[60:63], v[116:119], v[76:79], 0\n"
        "ds_read_b64_tr_b16 v[128:129], v218 offset:0\n"
        "v_mfma_f32_16x16x32_bf16 v[16:19], v[104:107], v[80:83], 0\n"
        "ds_read_b64_tr_b16 v[130:131], v218 offset:2048\n"
        "v_mfma_f32_16x16x32_bf16 v[32:35], v[108:111], v[80:83], 0\n"
        "ds_read_b128 v[100:103], v253 offset:2048\n"
        "v_mfma_f32_16x16x32_bf16 v[48:51], v[112:115], v[80:83], 0\n"
        "ds_read_b64_tr_b16 v[132:133], v219 offset:0\n"
        "v_mfma_f32_16x16x32_bf16 v[64:67], v[116:119], v[80:83], 0\n"
        "ds_read_b64_tr_b16 v[134:135], v219 offset:2048\n"
        "v_mfma_f32_16x16x32_bf16 v[20:23], v[104:107], v[84:87], 0\n"
        "v_add_u32 v216, 32768, v220\n"
        "v_add_u32 v217, 32768, v221\n"
        "v_add_u32 v218, 32768, v222\n"
        "v_add_u32 v219, 32768, v223\n"
        "v_mfma_f32_16x16x32_bf16 v[36:39], v[108:111], v[84:87], 0\n"
        "v_mfma_f32_16x16x32_bf16 v[52:55], v[112:115], v[84:87], 0\n"
        "v_mfma_f32_16x16x32_bf16 v[68:71], v[116:119], v[84:87], 0\n"
        "s_waitcnt vmcnt(1) lgkmcnt(0)\n"
        "s_barrier\n"
        "s_mul_i32 s67, s80, 28672\n"
        "s_mul_i32 s68, s75, 3584\n"
        "s_add_u32 s68, s68, s67\n"
        "s_add_u32 s68, s68, 11505664\n"
        "v_lshl_add_u32 v233, v3, 4, s68\n"
        "v_lshl_add_u32 v228, v4, 8, v233\n"
        "v_mov_b32 v235, 0x40000000\n"
        "v_add_u32 v229, 1024, v228\n"
        "v_cmp_eq_u32 vcc, 3, v4\n"
        "s_nop 1\n"
        "v_cndmask_b32 v229, v229, v235, vcc\n"
        "s_mul_i32 s68, s75, 896\n"
        "s_add_u32 s68, s68, s67\n"
        "s_add_u32 s68, s68, 13602816\n"
        "v_mul_u32_u24 v233, 7168, v4\n"
        "v_lshl_add_u32 v233, v3, 4, v233\n"
        "v_add_u32 v230, s68, v233\n"
        "v_cmp_le_u32 vcc, 12, v3\n"
        "s_nop 1\n"
        "v_cndmask_b32 v226, v230, v235, vcc\n"
        "v_cmp_gt_u32 vcc, 4, v3\n"
        "s_nop 1\n"
        "v_cndmask_b32 v227, v230, v235, vcc\n"
        "s_cmp_eq_u32 s80, 0\n"
        "s_cselect_b32 s73, 28672, 0\n"
        "s_and_b32 s68, s75, 1\n"
        "s_add_u32 s67, s80, s68\n"
        "v_mfma_f32_16x16x32_bf16 v[8:11], v[120:123], v[88:91], v[8:11]\n"
        "ds_read_b128 v[72:75], v250 offset:8192\n"
        "v_mfma_f32_16x16x32_bf16 v[24:27], v[124:127], v[88:91], v[24:27]\n"
        "ds_read_b64_tr_b16 v[104:105], v216 offset:0\n"
        "v_mfma_f32_16x16x32_bf16 v[40:43], v[128:131], v[88:91], v[40:43]\n"
        "ds_read_b64_tr_b16 v[106:107], v216 offset:2048\n"
        "v_mfma_f32_16x16x32_bf16 v[56:59], v[132:135], v[88:91], v[56:59]\n"
        "ds_read_b128 v[76:79], v252 offset:8192\n"
        "v_mfma_f32_16x16x32_bf16 v[12:15], v[120:123], v[92:95], v[12:15]\n"
        "ds_read_b64_tr_b16 v[108:109], v217 offset:0\n"
        "v_mfma_f32_16x16x32_bf16 v[28:31], v[124:127], v[92:95], v[28:31]\n"
        "ds_read_b64_tr_b16 v[110:111], v217 offset:2048\n"
        "v_mfma_f32_16x16x32_bf16 v[44:47], v[128:131], v[92:95], v[44:47]\n"
        "ds_read_b128 v[80:83], v250 offset:10240\n"
        "v_mfma_f32_16x16x32_bf16 v[60:63], v[132:135], v[92:95], v[60:63]\n"
        "ds_read_b64_tr_b16 v[112:113], v218 offset:0\n"
        "v_mfma_f32_16x16x32_bf16 v[16:19], v[120:123], v[96:99], v[16:19]\n"
        "ds_read_b64_tr_b16 v[114:115], v218 offset:2048\n"
        "v_mfma_f32_16x16x32_bf16 v[32:35], v[124:127], v[96:99], v[32:35]\n"
        "ds_read_b128 v[84:87], v252 offset:10240\n"
        "v_mfma_f32_16x16x32_bf16 v[48:51], v[128:131], v[96:99], v[48:51]\n"
        "ds_read_b64_tr_b16 v[116:117], v219 offset:0\n"
        "v_mfma_f32_16x16x32_bf16 v[64:67], v[132:135], v[96:99], v[64:67]\n"
        "ds_read_b64_tr_b16 v[118:119], v219 offset:2048\n"
        "v_mfma_f32_16x16x32_bf16 v[20:23], v[120:123], v[100:103], v[20:23]\n"
        "v_add_u32 v216, 0, v220\n"
        "v_add_u32 v217, 0, v221\n"
        "v_add_u32 v218, 0, v222\n"
        "v_add_u32 v219, 0, v223\n"
        "v_mfma_f32_16x16x32_bf16 v[36:39], v[124:127], v[100:103], v[36:39]\n"
        "v_mfma_f32_16x16x32_bf16 v[52:55], v[128:131], v[100:103], v[52:55]\n"
        "v_mfma_f32_16x16x32_bf16 v[68:71], v[132:135], v[100:103], v[68:71]\n"
        "s_waitcnt lgkmcnt(0)\n"
        "s_barrier\n"
        "s_mul_i32 s67, s67, 36\n"
        "s_lshr_b32 s68, s75, 1\n"
        "s_add_u32 s67, s67, s68\n"
        "s_lshl_b32 s67, s67, 12\n"
        "s_add_u32 s67, s67, 1105920\n"
        "v_lshl_add_u32 v231, v1, 4, s67\n"
        "s_mul_i32 s67, s75, 56\n"
        "v_bfe_u32 v233, v1, 2, 1\n"
        "v_mul_u32_u24 v233, 55, v233\n"
        "v_add_u32 v233, s67, v233\n"
        "v_lshrrev_b32 v233, 6, v233\n"
        "v_bfe_u32 v234, v1, 3, 1\n"
        "v_mul_u32_u24 v234, 7, v234\n"
        "v_add_u32 v233, v233, v234\n"
        "v_min_u32 v234, 2, v4\n"
        "v_add_u32 v234, s80, v234\n"
        "v_max_u32 v234, 1, v234\n"
        "v_lshlrev_b32 v234, 11, v234\n"
        "v_lshl_add_u32 v233, v233, 7, v234\n"
        "v_add_u32 v232, 2048, v233\n"
        "s_lshl_b32 s95, s74, 4\n"
        "s_add_u32 s95, s95, s75\n"
        "s_lshl_b32 s95, s95, 7\n"
        "s_add_u32 s95, s95, 4096\n"
        "v_mfma_f32_16x16x32_bf16 v[8:11], v[104:107], v[72:75], v[8:11]\n"
        "ds_read_b128 v[88:91], v251 offset:8192\n"
        "v_mfma_f32_16x16x32_bf16 v[24:27], v[108:111], v[72:75], v[24:27]\n"
        "ds_read_b64_tr_b16 v[120:121], v216 offset:0\n"
        "v_mfma_f32_16x16x32_bf16 v[40:43], v[112:115], v[72:75], v[40:43]\n"
        "ds_read_b64_tr_b16 v[122:123], v216 offset:2048\n"
        "v_mfma_f32_16x16x32_bf16 v[56:59], v[116:119], v[72:75], v[56:59]\n"
        "ds_read_b128 v[92:95], v253 offset:8192\n"
        "v_mfma_f32_16x16x32_bf16 v[12:15], v[104:107], v[76:79], v[12:15]\n"
        "ds_read_b64_tr_b16 v[124:125], v217 offset:0\n"
        "v_mfma_f32_16x16x32_bf16 v[28:31], v[108:111], v[76:79], v[28:31]\n"
        "ds_read_b64_tr_b16 v[126:127], v217 offset:2048\n"
        "v_mfma_f32_16x16x32_bf16 v[44:47], v[112:115], v[76:79], v[44:47]\n"
        "ds_read_b128 v[96:99], v251 offset:10240\n"
        "v_mfma_f32_16x16x32_bf16 v[60:63], v[116:119], v[76:79], v[60:63]\n"
        "ds_read_b64_tr_b16 v[128:129], v218 offset:0\n"
        "v_mfma_f32_16x16x32_bf16 v[16:19], v[104:107], v[80:83], v[16:19]\n"
        "ds_read_b64_tr_b16 v[130:131], v218 offset:2048\n"
        "v_mfma_f32_16x16x32_bf16 v[32:35], v[108:111], v[80:83], v[32:35]\n"
        "ds_read_b128 v[100:103], v253 offset:10240\n"
        "v_mfma_f32_16x16x32_bf16 v[48:51], v[112:115], v[80:83], v[48:51]\n"
        "ds_read_b64_tr_b16 v[132:133], v219 offset:0\n"
        "v_mfma_f32_16x16x32_bf16 v[64:67], v[116:119], v[80:83], v[64:67]\n"
        "ds_read_b64_tr_b16 v[134:135], v219 offset:2048\n"
        "v_mfma_f32_16x16x32_bf16 v[20:23], v[104:107], v[84:87], v[20:23]\n"
        "v_add_u32 v216, 16384, v220\n"
        "v_add_u32 v217, 16384, v221\n"
        "v_add_u32 v218, 16384, v222\n"
        "v_add_u32 v219, 16384, v223\n"
        "v_mfma_f32_16x16x32_bf16 v[36:39], v[108:111], v[84:87], v[36:39]\n"
        "v_mfma_f32_16x16x32_bf16 v[52:55], v[112:115], v[84:87], v[52:55]\n"
        "v_mfma_f32_16x16x32_bf16 v[68:71], v[116:119], v[84:87], v[68:71]\n"
        "s_waitcnt lgkmcnt(0)\n"
        "s_barrier\n"
        "v_and_b32 v233, 1, v3\n"
        "v_cmp_eq_u32 s[82:83], 1, v233\n"
        "v_lshl_add_u32 v234, v4, 2, v233\n"
        "v_cmp_le_u32 s[84:85], v234, v3\n"
        "v_add_u32 v234, 2, v234\n"
        "v_cmp_le_u32 s[86:87], v234, v3\n"
        "v_mov_b32 v233, 0xc0c0100\n"
        "v_mov_b32 v234, 0x1000c0c\n"
        "v_mov_b32 v235, 0x0c0c0c0c\n"
        "v_cndmask_b32 v233, v233, v234, s[82:83]\n"
        "v_cndmask_b32 v236, v233, v235, s[84:85]\n"
        "v_cndmask_b32 v238, v235, v233, s[84:85]\n"
        "v_mov_b32 v233, 0xc0c0302\n"
        "v_mov_b32 v234, 0x3020c0c\n"
        "v_mov_b32 v235, 0x0c0c0c0c\n"
        "v_cndmask_b32 v233, v233, v234, s[82:83]\n"
        "v_cndmask_b32 v237, v233, v235, s[86:87]\n"
        "v_cndmask_b32 v239, v235, v233, s[86:87]\n"
        "v_mfma_f32_16x16x32_bf16 v[8:11], v[120:123], v[88:91], v[8:11]\n"
        "ds_read_b128 v[72:75], v250 offset:16384\n"
        "v_mfma_f32_16x16x32_bf16 v[24:27], v[124:127], v[88:91], v[24:27]\n"
        "ds_read_b64_tr_b16 v[104:105], v216 offset:0\n"
        "v_mfma_f32_16x16x32_bf16 v[40:43], v[128:131], v[88:91], v[40:43]\n"
        "ds_read_b64_tr_b16 v[106:107], v216 offset:2048\n"
        "v_mfma_f32_16x16x32_bf16 v[56:59], v[132:135], v[88:91], v[56:59]\n"
        "ds_read_b128 v[76:79], v252 offset:16384\n"
        "v_mfma_f32_16x16x32_bf16 v[12:15], v[120:123], v[92:95], v[12:15]\n"
        "ds_read_b64_tr_b16 v[108:109], v217 offset:0\n"
        "v_mfma_f32_16x16x32_bf16 v[28:31], v[124:127], v[92:95], v[28:31]\n"
        "ds_read_b64_tr_b16 v[110:111], v217 offset:2048\n"
        "v_mfma_f32_16x16x32_bf16 v[44:47], v[128:131], v[92:95], v[44:47]\n"
        "ds_read_b128 v[80:83], v250 offset:18432\n"
        "v_mfma_f32_16x16x32_bf16 v[60:63], v[132:135], v[92:95], v[60:63]\n"
        "ds_read_b64_tr_b16 v[112:113], v218 offset:0\n"
        "v_mfma_f32_16x16x32_bf16 v[16:19], v[120:123], v[96:99], v[16:19]\n"
        "ds_read_b64_tr_b16 v[114:115], v218 offset:2048\n"
        "v_mfma_f32_16x16x32_bf16 v[32:35], v[124:127], v[96:99], v[32:35]\n"
        "ds_read_b128 v[84:87], v252 offset:18432\n"
        "v_mfma_f32_16x16x32_bf16 v[48:51], v[128:131], v[96:99], v[48:51]\n"
        "ds_read_b64_tr_b16 v[116:117], v219 offset:0\n"
        "v_mfma_f32_16x16x32_bf16 v[64:67], v[132:135], v[96:99], v[64:67]\n"
        "ds_read_b64_tr_b16 v[118:119], v219 offset:2048\n"
        "v_mfma_f32_16x16x32_bf16 v[20:23], v[120:123], v[100:103], v[20:23]\n"
        "v_add_u32 v216, 32768, v220\n"
        "v_add_u32 v217, 32768, v221\n"
        "v_add_u32 v218, 32768, v222\n"
        "v_add_u32 v219, 32768, v223\n"
        "v_mfma_f32_16x16x32_bf16 v[36:39], v[124:127], v[100:103], v[36:39]\n"
        "v_mfma_f32_16x16x32_bf16 v[52:55], v[128:131], v[100:103], v[52:55]\n"
        "v_mfma_f32_16x16x32_bf16 v[68:71], v[132:135], v[100:103], v[68:71]\n"
        "s_waitcnt lgkmcnt(0)\n"
        "s_barrier\n"
        "v_mfma_f32_16x16x32_bf16 v[8:11], v[104:107], v[72:75], v[8:11]\n"
        "ds_read_b128 v[88:91], v251 offset:16384\n"
        "v_mfma_f32_16x16x32_bf16 v[24:27], v[108:111], v[72:75], v[24:27]\n"
        "ds_read_b64_tr_b16 v[120:121], v216 offset:0\n"
        "v_mfma_f32_16x16x32_bf16 v[40:43], v[112:115], v[72:75], v[40:43]\n"
        "ds_read_b64_tr_b16 v[122:123], v216 offset:2048\n"
        "v_mfma_f32_16x16x32_bf16 v[56:59], v[116:119], v[72:75], v[56:59]\n"
        "ds_read_b128 v[92:95], v253 offset:16384\n"
        "v_mfma_f32_16x16x32_bf16 v[12:15], v[104:107], v[76:79], v[12:15]\n"
        "s_cbranch_execz Lback\n"
        "ds_read_b64_tr_b16 v[124:125], v217 offset:0\n"
        "v_mfma_f32_16x16x32_bf16 v[28:31], v[108:111], v[76:79], v[28:31]\n"
        "ds_read_b64_tr_b16 v[126:127], v217 offset:2048\n"
        "v_mfma_f32_16x16x32_bf16 v[44:47], v[112:115], v[76:79], v[44:47]\n"
        "ds_read_b128 v[96:99], v251 offset:18432\n"
        "v_mfma_f32_16x16x32_bf16 v[60:63], v[116:119], v[76:79], v[60:63]\n"
        "ds_read_b64_tr_b16 v[128:129], v218 offset:0\n"
        "v_mfma_f32_16x16x32_bf16 v[16:19], v[104:107], v[80:83], v[16:19]\n"
        "ds_read_b64_tr_b16 v[130:131], v218 offset:2048\n"
        "v_mfma_f32_16x16x32_bf16 v[32:35], v[108:111], v[80:83], v[32:35]\n"
        "ds_read_b128 v[100:103], v253 offset:18432\n"
        "v_mfma_f32_16x16x32_bf16 v[48:51], v[112:115], v[80:83], v[48:51]\n"
        "ds_read_b64_tr_b16 v[132:133], v219 offset:0\n"
        "v_mfma_f32_16x16x32_bf16 v[64:67], v[116:119], v[80:83], v[64:67]\n"
        "ds_read_b64_tr_b16 v[134:135], v219 offset:2048\n"
        "v_mfma_f32_16x16x32_bf16 v[20:23], v[104:107], v[84:87], v[20:23]\n"
        "v_add_u32 v216, 0, v220\n"
        "v_add_u32 v217, 0, v221\n"
        "v_add_u32 v218, 0, v222\n"
        "v_add_u32 v219, 0, v223\n"
        "v_mfma_f32_16x16x32_bf16 v[36:39], v[108:111], v[84:87], v[36:39]\n"
        "v_mfma_f32_16x16x32_bf16 v[52:55], v[112:115], v[84:87], v[52:55]\n"
        "v_mfma_f32_16x16x32_bf16 v[68:71], v[116:119], v[84:87], v[68:71]\n"
        "s_waitcnt lgkmcnt(0)\n"
        "s_barrier\n"
        "v_mfma_f32_16x16x32_bf16 v[8:11], v[120:123], v[88:91], v[8:11]\n"
        "ds_read_b128 v[72:75], v250 offset:24576\n"
        "v_mfma_f32_16x16x32_bf16 v[24:27], v[124:127], v[88:91], v[24:27]\n"
        "ds_read_b64_tr_b16 v[104:105], v216 offset:0\n"
        "v_mfma_f32_16x16x32_bf16 v[40:43], v[128:131], v[88:91], v[40:43]\n"
        "ds_read_b64_tr_b16 v[106:107], v216 offset:2048\n"
        "v_mfma_f32_16x16x32_bf16 v[56:59], v[132:135], v[88:91], v[56:59]\n"
        "ds_read_b128 v[76:79], v252 offset:24576\n"
        "v_mfma_f32_16x16x32_bf16 v[12:15], v[120:123], v[92:95], v[12:15]\n"
        "ds_read_b64_tr_b16 v[108:109], v217 offset:0\n"
        "v_mfma_f32_16x16x32_bf16 v[28:31], v[124:127], v[92:95], v[28:31]\n"
        "ds_read_b64_tr_b16 v[110:111], v217 offset:2048\n"
        "v_mfma_f32_16x16x32_bf16 v[44:47], v[128:131], v[92:95], v[44:47]\n"
        "ds_read_b128 v[80:83], v250 offset:26624\n"
        "v_mfma_f32_16x16x32_bf16 v[60:63], v[132:135], v[92:95], v[60:63]\n"
        "ds_read_b64_tr_b16 v[112:113], v218 offset:0\n"
        "v_mfma_f32_16x16x32_bf16 v[16:19], v[120:123], v[96:99], v[16:19]\n"
        "ds_read_b64_tr_b16 v[114:115], v218 offset:2048\n"
        "v_mfma_f32_16x16x32_bf16 v[32:35], v[124:127], v[96:99], v[32:35]\n"
        "ds_read_b128 v[84:87], v252 offset:26624\n"
        "v_mfma_f32_16x16x32_bf16 v[48:51], v[128:131], v[96:99], v[48:51]\n"
        "ds_read_b64_tr_b16 v[116:117], v219 offset:0\n"
        "v_mfma_f32_16x16x32_bf16 v[64:67], v[132:135], v[96:99], v[64:67]\n"
        "ds_read_b64_tr_b16 v[118:119], v219 offset:2048\n"
        "v_mfma_f32_16x16x32_bf16 v[20:23], v[120:123], v[100:103], v[20:23]\n"
        "v_add_u32 v216, 49152, v220\n"
        "v_add_u32 v217, 49152, v221\n"
        "v_add_u32 v218, 49152, v222\n"
        "v_add_u32 v219, 49152, v223\n"
        "v_mfma_f32_16x16x32_bf16 v[36:39], v[124:127], v[100:103], v[36:39]\n"
        "v_mfma_f32_16x16x32_bf16 v[52:55], v[128:131], v[100:103], v[52:55]\n"
        "v_mfma_f32_16x16x32_bf16 v[68:71], v[132:135], v[100:103], v[68:71]\n"
        "s_waitcnt lgkmcnt(0)\n"
        "s_barrier\n"
        "v_mfma_f32_16x16x32_bf16 v[8:11], v[104:107], v[72:75], v[8:11]\n"
        "ds_read_b128 v[88:91], v251 offset:24576\n"
        "v_mfma_f32_16x16x32_bf16 v[24:27], v[108:111], v[72:75], v[24:27]\n"
        "ds_read_b64_tr_b16 v[120:121], v216 offset:0\n"
        "v_mfma_f32_16x16x32_bf16 v[40:43], v[112:115], v[72:75], v[40:43]\n"
        "ds_read_b64_tr_b16 v[122:123], v216 offset:2048\n"
        "v_mfma_f32_16x16x32_bf16 v[56:59], v[116:119], v[72:75], v[56:59]\n"
        "ds_read_b128 v[92:95], v253 offset:24576\n"
        "v_mfma_f32_16x16x32_bf16 v[12:15], v[104:107], v[76:79], v[12:15]\n"
        "ds_read_b64_tr_b16 v[124:125], v217 offset:0\n"
        "v_mfma_f32_16x16x32_bf16 v[28:31], v[108:111], v[76:79], v[28:31]\n"
        "ds_read_b64_tr_b16 v[126:127], v217 offset:2048\n"
        "v_mfma_f32_16x16x32_bf16 v[44:47], v[112:115], v[76:79], v[44:47]\n"
        "ds_read_b128 v[96:99], v251 offset:26624\n"
        "v_mfma_f32_16x16x32_bf16 v[60:63], v[116:119], v[76:79], v[60:63]\n"
        "ds_read_b64_tr_b16 v[128:129], v218 offset:0\n"
        "v_mfma_f32_16x16x32_bf16 v[16:19], v[104:107], v[80:83], v[16:19]\n"
        "ds_read_b64_tr_b16 v[130:131], v218 offset:2048\n"
        "v_mfma_f32_16x16x32_bf16 v[32:35], v[108:111], v[80:83], v[32:35]\n"
        "ds_read_b128 v[100:103], v253 offset:26624\n"
        "v_mfma_f32_16x16x32_bf16 v[48:51], v[112:115], v[80:83], v[48:51]\n"
        "ds_read_b64_tr_b16 v[132:133], v219 offset:0\n"
        "v_mfma_f32_16x16x32_bf16 v[64:67], v[116:119], v[80:83], v[64:67]\n"
        "ds_read_b64_tr_b16 v[134:135], v219 offset:2048\n"
        "v_mfma_f32_16x16x32_bf16 v[20:23], v[104:107], v[84:87], v[20:23]\n"
        "v_add_u32 v216, 16384, v220\n"
        "v_add_u32 v217, 16384, v221\n"
        "v_add_u32 v218, 16384, v222\n"
        "v_add_u32 v219, 16384, v223\n"
        "v_mfma_f32_16x16x32_bf16 v[36:39], v[108:111], v[84:87], v[36:39]\n"
        "v_mfma_f32_16x16x32_bf16 v[52:55], v[112:115], v[84:87], v[52:55]\n"
        "v_mfma_f32_16x16x32_bf16 v[68:71], v[116:119], v[84:87], v[68:71]\n"
        "s_waitcnt lgkmcnt(0)\n"
        "s_barrier\n"
        "s_mov_b32 s66, 0\n"
        "buffer_load_dwordx2 v[244:245], v232, s[60:63], s66 offen sc1\n"
        "v_mfma_f32_16x16x32_bf16 v[8:11], v[120:123], v[88:91], v[8:11]\n"
        "ds_read_b128 v[72:75], v250 offset:32768\n"
        "v_mfma_f32_16x16x32_bf16 v[24:27], v[124:127], v[88:91], v[24:27]\n"
        "ds_read_b64_tr_b16 v[104:105], v216 offset:0\n"
        "v_mfma_f32_16x16x32_bf16 v[40:43], v[128:131], v[88:91], v[40:43]\n"
        "ds_read_b64_tr_b16 v[106:107], v216 offset:2048\n"
        "v_mfma_f32_16x16x32_bf16 v[56:59], v[132:135], v[88:91], v[56:59]\n"
        "ds_read_b128 v[76:79], v252 offset:32768\n"
        "v_mfma_f32_16x16x32_bf16 v[12:15], v[120:123], v[92:95], v[12:15]\n"
        "ds_read_b64_tr_b16 v[108:109], v217 offset:0\n"
        "v_mfma_f32_16x16x32_bf16 v[28:31], v[124:127], v[92:95], v[28:31]\n"
        "ds_read_b64_tr_b16 v[110:111], v217 offset:2048\n"
        "v_mfma_f32_16x16x32_bf16 v[44:47], v[128:131], v[92:95], v[44:47]\n"
        "ds_read_b128 v[80:83], v250 offset:34816\n"
        "v_mfma_f32_16x16x32_bf16 v[60:63], v[132:135], v[92:95], v[60:63]\n"
        "ds_read_b64_tr_b16 v[112:113], v218 offset:0\n"
        "v_mfma_f32_16x16x32_bf16 v[16:19], v[120:123], v[96:99], v[16:19]\n"
        "ds_read_b64_tr_b16 v[114:115], v218 offset:2048\n"
        "v_mfma_f32_16x16x32_bf16 v[32:35], v[124:127], v[96:99], v[32:35]\n"
        "ds_read_b128 v[84:87], v252 offset:34816\n"
        "v_mfma_f32_16x16x32_bf16 v[48:51], v[128:131], v[96:99], v[48:51]\n"
        "ds_read_b64_tr_b16 v[116:117], v219 offset:0\n"
        "v_mfma_f32_16x16x32_bf16 v[64:67], v[132:135], v[96:99], v[64:67]\n"
        "ds_read_b64_tr_b16 v[118:119], v219 offset:2048\n"
        "v_mfma_f32_16x16x32_bf16 v[20:23], v[120:123], v[100:103], v[20:23]\n"
        "v_add_u32 v216, 32768, v220\n"
        "v_add_u32 v217, 32768, v221\n"
        "v_add_u32 v218, 32768, v222\n"
        "v_add_u32 v219, 32768, v223\n"
        "v_mfma_f32_16x16x32_bf16 v[36:39], v[124:127], v[100:103], v[36:39]\n"
        "v_mfma_f32_16x16x32_bf16 v[52:55], v[128:131], v[100:103], v[52:55]\n"
        "v_mfma_f32_16x16x32_bf16 v[68:71], v[132:135], v[100:103], v[68:71]\n"
        "s_waitcnt lgkmcnt(0)\n"
        "s_barrier\n"
        "v_mfma_f32_16x16x32_bf16 v[8:11], v[104:107], v[72:75], v[8:11]\n"
        "ds_read_b128 v[88:91], v251 offset:32768\n"
        "v_mfma_f32_16x16x32_bf16 v[24:27], v[108:111], v[72:75], v[24:27]\n"
        "ds_read_b64_tr_b16 v[120:121], v216 offset:0\n"
        "v_mfma_f32_16x16x32_bf16 v[40:43], v[112:115], v[72:75], v[40:43]\n"
        "ds_read_b64_tr_b16 v[122:123], v216 offset:2048\n"
        "v_mfma_f32_16x16x32_bf16 v[56:59], v[116:119], v[72:75], v[56:59]\n"
        "ds_read_b128 v[92:95], v253 offset:32768\n"
        "v_mfma_f32_16x16x32_bf16 v[12:15], v[104:107], v[76:79], v[12:15]\n"
        "ds_read_b64_tr_b16 v[124:125], v217 offset:0\n"
        "v_mfma_f32_16x16x32_bf16 v[28:31], v[108:111], v[76:79], v[28:31]\n"
        "ds_read_b64_tr_b16 v[126:127], v217 offset:2048\n"
        "v_mfma_f32_16x16x32_bf16 v[44:47], v[112:115], v[76:79], v[44:47]\n"
        "ds_read_b128 v[96:99], v251 offset:34816\n"
        "v_mfma_f32_16x16x32_bf16 v[60:63], v[116:119], v[76:79], v[60:63]\n"
        "ds_read_b64_tr_b16 v[128:129], v218 offset:0\n"
        "v_mfma_f32_16x16x32_bf16 v[16:19], v[104:107], v[80:83], v[16:19]\n"
        "ds_read_b64_tr_b16 v[130:131], v218 offset:2048\n"
        "v_mfma_f32_16x16x32_bf16 v[32:35], v[108:111], v[80:83], v[32:35]\n"
        "ds_read_b128 v[100:103], v253 offset:34816\n"
        "v_mfma_f32_16x16x32_bf16 v[48:51], v[112:115], v[80:83], v[48:51]\n"
        "ds_read_b64_tr_b16 v[132:133], v219 offset:0\n"
        "v_mfma_f32_16x16x32_bf16 v[64:67], v[116:119], v[80:83], v[64:67]\n"
        "ds_read_b64_tr_b16 v[134:135], v219 offset:2048\n"
        "v_mfma_f32_16x16x32_bf16 v[20:23], v[104:107], v[84:87], v[20:23]\n"
        "v_add_u32 v216, 114688, v220\n"
        "v_add_u32 v217, 114688, v221\n"
        "v_add_u32 v218, 114688, v222\n"
        "v_add_u32 v219, 114688, v223\n"
        "v_mfma_f32_16x16x32_bf16 v[36:39], v[108:111], v[84:87], v[36:39]\n"
        "v_mfma_f32_16x16x32_bf16 v[52:55], v[112:115], v[84:87], v[52:55]\n"
        "v_mfma_f32_16x16x32_bf16 v[68:71], v[116:119], v[84:87], v[68:71]\n"
        "s_waitcnt lgkmcnt(0)\n"
        "s_barrier\n"
        "s_waitcnt vmcnt(0)\n"
        "v_cmp_ne_u64 vcc, s[70:71], v[244:245]\n"
        "s_cbranch_vccz Lret17\n"
        "s_mov_b32 s79, 400000\n"
        "s_mov_b32 s66, 0\n"
        "Lspin17:\n"
        "s_sleep 2\n"
        "buffer_load_dwordx2 v[244:245], v232, s[60:63], s66 offen sc1\n"
        "s_waitcnt vmcnt(0)\n"
        "v_cmp_ne_u64 vcc, s[70:71], v[244:245]\n"
        "s_cbranch_vccz Lret17\n"
        "s_sub_u32 s79, s79, 1\n"
        "s_cmp_lg_u32 s79, 0\n"
        "s_cbranch_scc1 Lspin17\n"
        "Lret17:\n"
        "buffer_load_dwordx2 v[136:137], v228, s[60:63], s73 offen offset:8 sc1\n"
        "buffer_load_dwordx2 v[138:139], v229, s[60:63], s73 offen offset:8 sc1\n"
        "s_mov_b32 s66, 28672\n"
        "buffer_load_dwordx4 v[140:143], v228, s[60:63], s66 offen offset:0 sc1\n"
        "s_mov_b32 s66, 28672\n"
        "buffer_load_dwordx4 v[144:147], v229, s[60:63], s66 offen offset:0 sc1\n"
        "s_mov_b32 s66, 57344\n"
        "buffer_load_dwordx4 v[148:151], v228, s[60:63], s66 offen offset:0 sc1\n"
        "v_mfma_f32_16x16x32_bf16 v[8:11], v[120:123], v[88:91], v[8:11]\n"
        "ds_read_b128 v[72:75], v250 offset:40960\n"
        "v_mfma_f32_16x16x32_bf16 v[24:27], v[124:127], v[88:91], v[24:27]\n"
        "ds_read_b64_tr_b16 v[104:105], v216 offset:0\n"
        "v_mfma_f32_16x16x32_bf16 v[40:43], v[128:131], v[88:91], v[40:43]\n"
        "ds_read_b64_tr_b16 v[106:107], v216 offset:2048\n"
        "v_mfma_f32_16x16x32_bf16 v[56:59], v[132:135], v[88:91], v[56:59]\n"
        "ds_read_b128 v[76:79], v252 offset:40960\n"
        "v_mfma_f32_16x16x32_bf16 v[12:15], v[120:123], v[92:95], v[12:15]\n"
        "ds_read_b64_tr_b16 v[108:109], v217 offset:0\n"
        "v_mfma_f32_16x16x32_bf16 v[28:31], v[124:127], v[92:95], v[28:31]\n"
        "ds_read_b64_tr_b16 v[110:111], v217 offset:2048\n"
        "v_mfma_f32_16x16x32_bf16 v[44:47], v[128:131], v[92:95], v[44:47]\n"
        "ds_read_b128 v[80:83], v250 offset:43008\n"
        "v_mfma_f32_16x16x32_bf16 v[60:63], v[132:135], v[92:95], v[60:63]\n"
        "ds_read_b64_tr_b16 v[112:113], v218 offset:0\n"
        "v_mfma_f32_16x16x32_bf16 v[16:19], v[120:123], v[96:99], v[16:19]\n"
        "ds_read_b64_tr_b16 v[114:115], v218 offset:2048\n"
        "v_mfma_f32_16x16x32_bf16 v[32:35], v[124:127], v[96:99], v[32:35]\n"
        "ds_read_b128 v[84:87], v252 offset:43008\n"
        "v_mfma_f32_16x16x32_bf16 v[48:51], v[128:131], v[96:99], v[48:51]\n"
        "ds_read_b64_tr_b16 v[116:117], v219 offset:0\n"
        "v_mfma_f32_16x16x32_bf16 v[64:67], v[132:135], v[96:99], v[64:67]\n"
        "ds_read_b64_tr_b16 v[118:119], v219 offset:2048\n"
        "v_mfma_f32_16x16x32_bf16 v[20:23], v[120:123], v[100:103], v[20:23]\n"
        "v_add_u32 v216, 0, v220\n"
        "v_add_u32 v217, 0, v221\n"
        "v_add_u32 v218, 0, v222\n"
        "v_add_u32 v219, 0, v223\n"
        "v_mfma_f32_16x16x32_bf16 v[36:39], v[124:127], v[100:103], v[36:39]\n"
        "v_mfma_f32_16x16x32_bf16 v[52:55], v[128:131], v[100:103], v[52:55]\n"
        "v_mfma_f32_16x16x32_bf16 v[68:71], v[132:135], v[100:103], v[68:71]\n"
        "s_waitcnt lgkmcnt(0)\n"
        "s_barrier\n"
        "s_mov_b32 s66, 57344\n"
        "buffer_load_dwordx4 v[152:155], v229, s[60:63], s66 offen offset:0 sc1\n"
        "buffer_load_dwordx2 v[176:177], v230, s[60:63], s73 offen offset:8 sc1\n"
        "s_mov_b32 s66, 28672\n"
        "buffer_load_dwordx4 v[178:181], v230, s[60:63], s66 offen offset:0 sc1\n"
        "s_mov_b32 s66, 57344\n"
        "buffer_load_dwordx4 v[182:185], v230, s[60:63], s66 offen offset:0 sc1\n"
        "buffer_load_dwordx2 v[186:187], v226, s[60:63], s73 offen offset:264 sc1\n"
        "v_mfma_f32_16x16x32_bf16 v[8:11], v[104:107], v[72:75], v[8:11]\n"
        "ds_read_b128 v[88:91], v251 offset:40960\n"
        "v_mfma_f32_16x16x32_bf16 v[24:27], v[108:111], v[72:75], v[24:27]\n"
        "ds_read_b64_tr_b16 v[120:121], v216 offset:0\n"
        "v_mfma_f32_16x16x32_bf16 v[40:43], v[112:115], v[72:75], v[40:43]\n"
        "ds_read_b64_tr_b16 v[122:123], v216 offset:2048\n"
        "v_mfma_f32_16x16x32_bf16 v[56:59], v[116:119], v[72:75], v[56:59]\n"
        "ds_read_b128 v[92:95], v253 offset:40960\n"
        "v_mfma_f32_16x16x32_bf16 v[12:15], v[104:107], v[76:79], v[12:15]\n"
        "ds_read_b64_tr_b16 v[124:125], v217 offset:0\n"
        "v_mfma_f32_16x16x32_bf16 v[28:31], v[108:111], v[76:79], v[28:31]\n"
        "ds_read_b64_tr_b16 v[126:127], v217 offset:2048\n"
        "v_mfma_f32_16x16x32_bf16 v[44:47], v[112:115], v[76:79], v[44:47]\n"
        "ds_read_b128 v[96:99], v251 offset:43008\n"
        "v_mfma_f32_16x16x32_bf16 v[60:63], v[116:119], v[76:79], v[60:63]\n"
        "ds_read_b64_tr_b16 v[128:129], v218 offset:0\n"
        "v_mfma_f32_16x16x32_bf16 v[16:19], v[104:107], v[80:83], v[16:19]\n"
        "ds_read_b64_tr_b16 v[130:131], v218 offset:2048\n"
        "v_mfma_f32_16x16x32_bf16 v[32:35], v[108:111], v[80:83], v[32:35]\n"
        "ds_read_b128 v[100:103], v253 offset:43008\n"
        "v_mfma_f32_16x16x32_bf16 v[48:51], v[112:115], v[80:83], v[48:51]\n"
        "ds_read_b64_tr_b16 v[132:133], v219 offset:0\n"
        "v_mfma_f32_16x16x32_bf16 v[64:67], v[116:119], v[80:83], v[64:67]\n"
        "ds_read_b64_tr_b16 v[134:135], v219 offset:2048\n"
        "v_mfma_f32_16x16x32_bf16 v[20:23], v[104:107], v[84:87], v[20:23]\n"
        "v_add_u32 v216, 49152, v220\n"
        "v_add_u32 v217, 49152, v221\n"
        "v_add_u32 v218, 49152, v222\n"
        "v_add_u32 v219, 49152, v223\n"
        "v_mfma_f32_16x16x32_bf16 v[36:39], v[108:111], v[84:87], v[36:39]\n"
        "v_mfma_f32_16x16x32_bf16 v[52:55], v[112:115], v[84:87], v[52:55]\n"
        "v_mfma_f32_16x16x32_bf16 v[68:71], v[116:119], v[84:87], v[68:71]\n"
        "s_waitcnt lgkmcnt(0)\n"
        "s_barrier\n"
        "s_mov_b32 s66, 28672\n"
        "buffer_load_dwordx4 v[188:191], v226, s[60:63], s66 offen offset:256 sc1\n"
        "s_mov_b32 s66, 57344\n"
        "buffer_load_dwordx4 v[192:195], v226, s[60:63], s66 offen offset:256 sc1\n"
        "buffer_load_dwordx2 v[156:157], v228, s[60:63], s73 offen offset:1800 sc1\n"
        "buffer_load_dwordx2 v[158:159], v229, s[60:63], s73 offen offset:1800 sc1\n"
        "s_mov_b32 s66, 28672\n"
        "buffer_load_dwordx4 v[160:163], v228, s[60:63], s66 offen offset:1792 sc1\n"
        "v_mfma_f32_16x16x32_bf16 v[8:11], v[120:123], v[88:91], v[8:11]\n"
        "ds_read_b128 v[72:75], v250 offset:49152\n"
        "v_mfma_f32_16x16x32_bf16 v[24:27], v[124:127], v[88:91], v[24:27]\n"
        "ds_read_b64_tr_b16 v[104:105], v216 offset:0\n"
        "v_mfma_f32_16x16x32_bf16 v[40:43], v[128:131], v[88:91], v[40:43]\n"
        "ds_read_b64_tr_b16 v[106:107], v216 offset:2048\n"
        "v_mfma_f32_16x16x32_bf16 v[56:59], v[132:135], v[88:91], v[56:59]\n"
        "ds_read_b128 v[76:79], v252 offset:49152\n"
        "v_mfma_f32_16x16x32_bf16 v[12:15], v[120:123], v[92:95], v[12:15]\n"
        "ds_read_b64_tr_b16 v[108:109], v217 offset:0\n"
        "v_mfma_f32_16x16x32_bf16 v[28:31], v[124:127], v[92:95], v[28:31]\n"
        "ds_read_b64_tr_b16 v[110:111], v217 offset:2048\n"
        "v_mfma_f32_16x16x32_bf16 v[44:47], v[128:131], v[92:95], v[44:47]\n"
        "ds_read_b128 v[80:83], v250 offset:51200\n"
        "v_mfma_f32_16x16x32_bf16 v[60:63], v[132:135], v[92:95], v[60:63]\n"
        "ds_read_b64_tr_b16 v[112:113], v218 offset:0\n"
        "v_mfma_f32_16x16x32_bf16 v[16:19], v[120:123], v[96:99], v[16:19]\n"
        "ds_read_b64_tr_b16 v[114:115], v218 offset:2048\n"
        "v_mfma_f32_16x16x32_bf16 v[32:35], v[124:127], v[96:99], v[32:35]\n"
        "ds_read_b128 v[84:87], v252 offset:51200\n"
        "v_mfma_f32_16x16x32_bf16 v[48:51], v[128:131], v[96:99], v[48:51]\n"
        "ds_read_b64_tr_b16 v[116:117], v219 offset:0\n"
        "v_mfma_f32_16x16x32_bf16 v[64:67], v[132:135], v[96:99], v[64:67]\n"
        "ds_read_b64_tr_b16 v[118:119], v219 offset:2048\n"
        "v_mfma_f32_16x16x32_bf16 v[20:23], v[120:123], v[100:103], v[20:23]\n"
        "v_add_u32 v216, 65536, v220\n"
        "v_add_u32 v217, 65536, v221\n"
        "v_add_u32 v218, 65536, v222\n"
        "v_add_u32 v219, 65536, v223\n"
        "v_mfma_f32_16x16x32_bf16 v[36:39], v[124:127], v[100:103], v[36:39]\n"
        "v_mfma_f32_16x16x32_bf16 v[52:55], v[128:131], v[100:103], v[52:55]\n"
        "v_mfma_f32_16x16x32_bf16 v[68:71], v[132:135], v[100:103], v[68:71]\n"
        "s_waitcnt lgkmcnt(0)\n"
        "s_barrier\n"
        "s_mov_b32 s66, 28672\n"
        "buffer_load_dwordx4 v[164:167], v229, s[60:63], s66 offen offset:1792 sc1\n"
        "s_mov_b32 s66, 57344\n"
        "buffer_load_dwordx4 v[168:171], v228, s[60:63], s66 offen offset:1792 sc1\n"
        "s_mov_b32 s66, 57344\n"
        "buffer_load_dwordx4 v[172:175], v229, s[60:63], s66 offen offset:1792 sc1\n"
        "buffer_load_dwordx2 v[196:197], v227, s[60:63], s73 offen offset:392 sc1\n"
        "s_mov_b32 s66, 28672\n"
        "buffer_load_dwordx4 v[198:201], v227, s[60:63], s66 offen offset:384 sc1\n"
        "v_mfma_f32_16x16x32_bf16 v[8:11], v[104:107], v[72:75], v[8:11]\n"
        "ds_read_b128 v[88:91], v251 offset:49152\n"
        "v_mfma_f32_16x16x32_bf16 v[24:27], v[108:111], v[72:75], v[24:27]\n"
        "ds_read_b64_tr_b16 v[120:121], v216 offset:0\n"
        "v_mfma_f32_16x16x32_bf16 v[40:43], v[112:115], v[72:75], v[40:43]\n"
        "ds_read_b64_tr_b16 v[122:123], v216 offset:2048\n"
        "v_mfma_f32_16x16x32_bf16 v[56:59], v[116:119], v[72:75], v[56:59]\n"
        "ds_read_b128 v[92:95], v253 offset:49152\n"
        "v_mfma_f32_16x16x32_bf16 v[12:15], v[104:107], v[76:79], v[12:15]\n"
        "ds_read_b64_tr_b16 v[124:125], v217 offset:0\n"
        "v_mfma_f32_16x16x32_bf16 v[28:31], v[108:111], v[76:79], v[28:31]\n"
        "ds_read_b64_tr_b16 v[126:127], v217 offset:2048\n"
        "v_mfma_f32_16x16x32_bf16 v[44:47], v[112:115], v[76:79], v[44:47]\n"
        "ds_read_b128 v[96:99], v251 offset:51200\n"
        "v_mfma_f32_16x16x32_bf16 v[60:63], v[116:119], v[76:79], v[60:63]\n"
        "ds_read_b64_tr_b16 v[128:129], v218 offset:0\n"
        "v_mfma_f32_16x16x32_bf16 v[16:19], v[104:107], v[80:83], v[16:19]\n"
        "ds_read_b64_tr_b16 v[130:131], v218 offset:2048\n"
        "v_mfma_f32_16x16x32_bf16 v[32:35], v[108:111], v[80:83], v[32:35]\n"
        "ds_read_b128 v[100:103], v253 offset:51200\n"
        "v_mfma_f32_16x16x32_bf16 v[48:51], v[112:115], v[80:83], v[48:51]\n"
        "ds_read_b64_tr_b16 v[132:133], v219 offset:0\n"
        "v_mfma_f32_16x16x32_bf16 v[64:67], v[116:119], v[80:83], v[64:67]\n"
        "ds_read_b64_tr_b16 v[134:135], v219 offset:2048\n"
        "v_mfma_f32_16x16x32_bf16 v[20:23], v[104:107], v[84:87], v[20:23]\n"
        "v_mfma_f32_16x16x32_bf16 v[36:39], v[108:111], v[84:87], v[36:39]\n"
        "v_mfma_f32_16x16x32_bf16 v[52:55], v[112:115], v[84:87], v[52:55]\n"
        "v_mfma_f32_16x16x32_bf16 v[68:71], v[116:119], v[84:87], v[68:71]\n"
        "s_mov_b32 s66, 57344\n"
        "buffer_load_dwordx4 v[202:205], v227, s[60:63], s66 offen offset:384 sc1\n"
        "buffer_load_dwordx2 v[206:207], v230, s[60:63], s73 offen offset:648 sc1\n"
        "s_mov_b32 s66, 28672\n"
        "buffer_load_dwordx4 v[208:211], v230, s[60:63], s66 offen offset:640 sc1\n"
        "s_mov_b32 s66, 57344\n"
        "buffer_load_dwordx4 v[212:215], v230, s[60:63], s66 offen offset:640 sc1\n"
        "s_waitcnt lgkmcnt(9)\n"
        "v_mfma_f32_16x16x32_bf16 v[8:11], v[120:123], v[88:91], v[8:11]\n"
        "s_waitcnt lgkmcnt(6)\n"
        "v_mfma_f32_16x16x32_bf16 v[24:27], v[124:127], v[88:91], v[24:27]\n"
        "s_waitcnt lgkmcnt(3)\n"
        "v_mfma_f32_16x16x32_bf16 v[40:43], v[128:131], v[88:91], v[40:43]\n"
        "s_waitcnt lgkmcnt(0)\n"
        "v_mfma_f32_16x16x32_bf16 v[56:59], v[132:135], v[88:91], v[56:59]\n"
        "v_mfma_f32_16x16x32_bf16 v[12:15], v[120:123], v[92:95], v[12:15]\n"
        "v_mfma_f32_16x16x32_bf16 v[28:31], v[124:127], v[92:95], v[28:31]\n"
        "v_mfma_f32_16x16x32_bf16 v[44:47], v[128:131], v[92:95], v[44:47]\n"
        "v_mfma_f32_16x16x32_bf16 v[60:63], v[132:135], v[92:95], v[60:63]\n"
        "v_mfma_f32_16x16x32_bf16 v[16:19], v[120:123], v[96:99], v[16:19]\n"
        "v_mfma_f32_16x16x32_bf16 v[32:35], v[124:127], v[96:99], v[32:35]\n"
        "v_mfma_f32_16x16x32_bf16 v[48:51], v[128:131], v[96:99], v[48:51]\n"
        "v_mfma_f32_16x16x32_bf16 v[64:67], v[132:135], v[96:99], v[64:67]\n"
        "v_mfma_f32_16x16x32_bf16 v[20:23], v[120:123], v[100:103], v[20:23]\n"
        "v_mfma_f32_16x16x32_bf16 v[36:39], v[124:127], v[100:103], v[36:39]\n"
        "v_mfma_f32_16x16x32_bf16 v[52:55], v[128:131], v[100:103], v[52:55]\n"
        "v_mfma_f32_16x16x32_bf16 v[68:71], v[132:135], v[100:103], v[68:71]\n"
        "s_cmp_eq_u32 s80, 0\n"
        "s_cselect_b64 vcc, -1, 0\n"
        "v_mov_b32 v233, 0xcf9db2f6\n"
        "s_mov_b32 s69, 0x3e8b97f6\n"
        "s_mov_b32 s68, 0xbe8b97f6\n"
        "v_mov_b32 v216, s77\n"
        "v_mov_b32 v217, s77\n"
        "v_mov_b32 v218, s77\n"
        "v_mov_b32 v219, s77\n"
        "v_cvt_pk_bf16_f32 v72, v8, v9\n"
        "v_cvt_pk_bf16_f32 v73, v10, v11\n"
        "v_cvt_pk_bf16_f32 v74, v24, v25\n"
        "v_cvt_pk_bf16_f32 v75, v26, v27\n"
        "v_cvt_pk_bf16_f32 v76, v12, v13\n"
        "v_cvt_pk_bf16_f32 v77, v14, v15\n"
        "v_cvt_pk_bf16_f32 v78, v28, v29\n"
        "v_cvt_pk_bf16_f32 v79, v30, v31\n"
        "v_cvt_pk_bf16_f32 v80, v16, v17\n"
        "v_cvt_pk_bf16_f32 v81, v18, v19\n"
        "v_cvt_pk_bf16_f32 v82, v32, v33\n"
        "v_cvt_pk_bf16_f32 v83, v34, v35\n"
        "v_cvt_pk_bf16_f32 v84, v20, v21\n"
        "v_cvt_pk_bf16_f32 v85, v22, v23\n"
        "v_cvt_pk_bf16_f32 v86, v36, v37\n"
        "v_cvt_pk_bf16_f32 v87, v38, v39\n"
        "s_waitcnt vmcnt(20)\n"
        "v_swap_b32 v142, v144\n"
        "v_swap_b32 v143, v145\n"
        "s_waitcnt vmcnt(18)\n"
        "v_swap_b32 v150, v152\n"
        "v_swap_b32 v151, v153\n"
        "v_mfma_f32_16x16x32_bf16 v[8:11], v[136:139], v[72:75], 0\n"
        "v_mfma_f32_16x16x32_bf16 v[12:15], v[140:143], v[72:75], 0\n"
        "v_mfma_f32_16x16x32_bf16 v[16:19], v[140:143], v[76:79], 0\n"
        "v_mfma_f32_16x16x32_bf16 v[20:23], v[144:147], v[76:79], 0\n"
        "v_mfma_f32_16x16x32_bf16 v[24:27], v[144:147], v[80:83], 0\n"
        "v_mfma_f32_16x16x32_bf16 v[28:31], v[148:151], v[80:83], 0\n"
        "v_mfma_f32_16x16x32_bf16 v[32:35], v[148:151], v[84:87], 0\n"
        "v_mfma_f32_16x16x32_bf16 v[36:39], v[152:155], v[84:87], 0\n"
        "s_nop 0\n"
        "v_cndmask_b32 v8, v8, v9, s[82:83]\n"
        "s_nop 0\n"
        "v_cndmask_b32 v16, v16, v17, s[82:83]\n"
        "s_nop 0\n"
        "v_cndmask_b32 v24, v24, v25, s[82:83]\n"
        "s_nop 0\n"
        "v_cndmask_b32 v32, v32, v33, s[82:83]\n"
        "v_cndmask_b32 v10, v10, v11, s[82:83]\n"
        "v_cndmask_b32 v18, v18, v19, s[82:83]\n"
        "v_cndmask_b32 v26, v26, v27, s[82:83]\n"
        "v_cndmask_b32 v34, v34, v35, s[82:83]\n"
        "v_cndmask_b32 v12, v12, v13, s[82:83]\n"
        "v_cndmask_b32 v20, v20, v21, s[82:83]\n"
        "v_cndmask_b32 v28, v28, v29, s[82:83]\n"
        "v_cndmask_b32 v36, v36, v37, s[82:83]\n"
        "v_cndmask_b32 v14, v14, v15, s[82:83]\n"
        "v_cndmask_b32 v22, v22, v23, s[82:83]\n"
        "v_cndmask_b32 v30, v30, v31, s[82:83]\n"
        "v_cndmask_b32 v38, v38, v39, s[82:83]\n"
        "v_cndmask_b32 v8, v8, v233, vcc\n"
        "v_cndmask_b32 v10, v10, v233, vcc\n"
        "v_cndmask_b32 v8, v8, v12, s[84:85]\n"
        "v_cndmask_b32 v16, v16, v20, s[84:85]\n"
        "v_cndmask_b32 v24, v24, v28, s[84:85]\n"
        "v_cndmask_b32 v32, v32, v36, s[84:85]\n"
        "v_cndmask_b32 v9, v10, v14, s[86:87]\n"
        "v_cndmask_b32 v17, v18, v22, s[86:87]\n"
        "v_cndmask_b32 v25, v26, v30, s[86:87]\n"
        "v_cndmask_b32 v33, v34, v38, s[86:87]\n"
        "v_max_f32 v10, v8, v9\n"
        "v_max_f32 v18, v16, v17\n"
        "v_max_f32 v26, v24, v25\n"
        "v_max_f32 v34, v32, v33\n"
        "v_permlane32_swap_b32 v10, v18\n"
        "s_nop 0\n"
        "v_permlane32_swap_b32 v26, v34\n"
        "v_max_f32 v10, v10, v18\n"
        "v_max_f32 v26, v26, v34\n"
        "s_nop 1\n"
        "v_permlane16_swap_b32 v10, v26\n"
        "v_max_f32 v10, v10, v26\n"
        "v_mul_f32 v26, s68, v10\n"
        "v_mul_f32 v10, s68, v10\n"
        "s_nop 1\n"
        "v_permlane16_swap_b32 v10, v26\n"
        "v_mov_b32 v18, v10\n"
        "v_mov_b32 v34, v26\n"
        "s_nop 0\n"
        "v_permlane32_swap_b32 v10, v18\n"
        "v_permlane32_swap_b32 v26, v34\n"
        "v_fma_f32 v8, v8, s69, v10\n"
        "v_fma_f32 v16, v16, s69, v18\n"
        "v_fma_f32 v24, v24, s69, v26\n"
        "v_fma_f32 v32, v32, s69, v34\n"
        "v_fma_f32 v9, v9, s69, v10\n"
        "v_fma_f32 v17, v17, s69, v18\n"
        "v_fma_f32 v25, v25, s69, v26\n"
        "v_fma_f32 v33, v33, s69, v34\n"
        "v_exp_f32 v8, v8\n"
        "v_exp_f32 v16, v16\n"
        "v_exp_f32 v24, v24\n"
        "v_exp_f32 v32, v32\n"
        "v_exp_f32 v9, v9\n"
        "v_exp_f32 v17, v17\n"
        "v_exp_f32 v25, v25\n"
        "v_exp_f32 v33, v33\n"
        "v_cvt_pk_bf16_f32 v8, v8, v9\n"
        "v_cvt_pk_bf16_f32 v16, v16, v17\n"
        "v_cvt_pk_bf16_f32 v24, v24, v25\n"
        "v_cvt_pk_bf16_f32 v32, v32, v33\n"
        "v_perm_b32 v104, v8, v8, v236\n"
        "v_perm_b32 v108, v16, v16, v236\n"
        "v_perm_b32 v112, v24, v24, v236\n"
        "v_perm_b32 v116, v32, v32, v236\n"
        "v_perm_b32 v105, v8, v8, v237\n"
        "v_perm_b32 v109, v16, v16, v237\n"
        "v_perm_b32 v113, v24, v24, v237\n"
        "v_perm_b32 v117, v32, v32, v237\n"
        "v_perm_b32 v106, v8, v8, v238\n"
        "v_perm_b32 v110, v16, v16, v238\n"
        "v_perm_b32 v114, v24, v24, v238\n"
        "v_perm_b32 v118, v32, v32, v238\n"
        "v_perm_b32 v107, v8, v8, v239\n"
        "v_perm_b32 v111, v16, v16, v239\n"
        "v_perm_b32 v115, v24, v24, v239\n"
        "v_perm_b32 v119, v32, v32, v239\n"
        "v_mfma_f32_16x16x32_bf16 v[72:75], v[216:219], v[104:107], 0\n"
        "v_mfma_f32_16x16x32_bf16 v[76:79], v[216:219], v[108:111], 0\n"
        "v_mfma_f32_16x16x32_bf16 v[80:83], v[216:219], v[112:115], 0\n"
        "v_mfma_f32_16x16x32_bf16 v[84:87], v[216:219], v[116:119], 0\n"
        "s_waitcnt vmcnt(16)\n"
        "v_mfma_f32_16x16x32_bf16 v[8:11], v[176:179], v[104:107], 0\n"
        "s_waitcnt vmcnt(13)\n"
        "v_mfma_f32_16x16x32_bf16 v[12:15], v[186:189], v[104:107], 0\n"
        "v_mfma_f32_16x16x32_bf16 v[16:19], v[178:181], v[108:111], 0\n"
        "v_mfma_f32_16x16x32_bf16 v[20:23], v[188:191], v[108:111], 0\n"
        "v_mfma_f32_16x16x32_bf16 v[24:27], v[180:183], v[112:115], 0\n"
        "s_waitcnt vmcnt(12)\n"
        "v_mfma_f32_16x16x32_bf16 v[28:31], v[190:193], v[112:115], 0\n"
        "v_mfma_f32_16x16x32_bf16 v[32:35], v[182:185], v[116:119], 0\n"
        "v_mfma_f32_16x16x32_bf16 v[36:39], v[192:195], v[116:119], 0\n"
        "v_rcp_f32 v72, v72\n"
        "v_rcp_f32 v76, v76\n"
        "v_rcp_f32 v80, v80\n"
        "v_rcp_f32 v84, v84\n"
        "v_mov_b32 v73, v72\n"
        "v_mov_b32 v77, v76\n"
        "v_mov_b32 v81, v80\n"
        "v_mov_b32 v85, v84\n"
        "v_pk_mul_f32 v[8:9], v[8:9], v[72:73]\n"
        "v_pk_mul_f32 v[10:11], v[10:11], v[72:73]\n"
        "v_pk_mul_f32 v[12:13], v[12:13], v[72:73]\n"
        "v_pk_mul_f32 v[14:15], v[14:15], v[72:73]\n"
        "v_cvt_pk_bf16_f32 v72, v8, v9\n"
        "v_cvt_pk_bf16_f32 v73, v10, v11\n"
        "v_cvt_pk_bf16_f32 v74, v12, v13\n"
        "v_cvt_pk_bf16_f32 v75, v14, v15\n"
        "s_mov_b32 s66, 0\n"
        "buffer_store_dwordx4 v[72:75], v231, s[60:63], s66 offen offset:0 sc1\n"
        "v_pk_mul_f32 v[16:17], v[16:17], v[76:77]\n"
        "v_pk_mul_f32 v[18:19], v[18:19], v[76:77]\n"
        "v_pk_mul_f32 v[20:21], v[20:21], v[76:77]\n"
        "v_pk_mul_f32 v[22:23], v[22:23], v[76:77]\n"
        "v_cvt_pk_bf16_f32 v76, v16, v17\n"
        "v_cvt_pk_bf16_f32 v77, v18, v19\n"
        "v_cvt_pk_bf16_f32 v78, v20, v21\n"
        "v_cvt_pk_bf16_f32 v79, v22, v23\n"
        "buffer_store_dwordx4 v[76:79], v231, s[60:63], s66 offen offset:1024 sc1\n"
        "v_pk_mul_f32 v[24:25], v[24:25], v[80:81]\n"
        "v_pk_mul_f32 v[26:27], v[26:27], v[80:81]\n"
        "v_pk_mul_f32 v[28:29], v[28:29], v[80:81]\n"
        "v_pk_mul_f32 v[30:31], v[30:31], v[80:81]\n"
        "v_cvt_pk_bf16_f32 v80, v24, v25\n"
        "v_cvt_pk_bf16_f32 v81, v26, v27\n"
        "v_cvt_pk_bf16_f32 v82, v28, v29\n"
        "v_cvt_pk_bf16_f32 v83, v30, v31\n"
        "buffer_store_dwordx4 v[80:83], v231, s[60:63], s66 offen offset:2048 sc1\n"
        "v_pk_mul_f32 v[32:33], v[32:33], v[84:85]\n"
        "v_pk_mul_f32 v[34:35], v[34:35], v[84:85]\n"
        "v_pk_mul_f32 v[36:37], v[36:37], v[84:85]\n"
        "v_pk_mul_f32 v[38:39], v[38:39], v[84:85]\n"
        "v_cvt_pk_bf16_f32 v84, v32, v33\n"
        "v_cvt_pk_bf16_f32 v85, v34, v35\n"
        "v_cvt_pk_bf16_f32 v86, v36, v37\n"
        "v_cvt_pk_bf16_f32 v87, v38, v39\n"
        "buffer_store_dwordx4 v[84:87], v231, s[60:63], s66 offen offset:3072 sc1\n"
        "v_cvt_pk_bf16_f32 v88, v40, v41\n"
        "v_cvt_pk_bf16_f32 v89, v42, v43\n"
        "v_cvt_pk_bf16_f32 v90, v56, v57\n"
        "v_cvt_pk_bf16_f32 v91, v58, v59\n"
        "v_cvt_pk_bf16_f32 v92, v44, v45\n"
        "v_cvt_pk_bf16_f32 v93, v46, v47\n"
        "v_cvt_pk_bf16_f32 v94, v60, v61\n"
        "v_cvt_pk_bf16_f32 v95, v62, v63\n"
        "v_cvt_pk_bf16_f32 v96, v48, v49\n"
        "s_cbranch_execz Lback\n"
        "v_cvt_pk_bf16_f32 v97, v50, v51\n"
        "v_cvt_pk_bf16_f32 v98, v64, v65\n"
        "v_cvt_pk_bf16_f32 v99, v66, v67\n"
        "v_cvt_pk_bf16_f32 v100, v52, v53\n"
        "v_cvt_pk_bf16_f32 v101, v54, v55\n"
        "v_cvt_pk_bf16_f32 v102, v68, v69\n"
        "v_cvt_pk_bf16_f32 v103, v70, v71\n"
        "s_waitcnt vmcnt(12)\n"
        "v_swap_b32 v162, v164\n"
        "v_swap_b32 v163, v165\n"
        "s_waitcnt vmcnt(10)\n"
        "v_swap_b32 v170, v172\n"
        "v_swap_b32 v171, v173\n"
        "v_mfma_f32_16x16x32_bf16 v[40:43], v[156:159], v[88:91], 0\n"
        "v_mfma_f32_16x16x32_bf16 v[44:47], v[160:163], v[88:91], 0\n"
        "v_mfma_f32_16x16x32_bf16 v[48:51], v[160:163], v[92:95], 0\n"
        "v_mfma_f32_16x16x32_bf16 v[52:55], v[164:167], v[92:95], 0\n"
        "v_mfma_f32_16x16x32_bf16 v[56:59], v[164:167], v[96:99], 0\n"
        "v_mfma_f32_16x16x32_bf16 v[60:63], v[168:171], v[96:99], 0\n"
        "v_mfma_f32_16x16x32_bf16 v[64:67], v[168:171], v[100:103], 0\n"
        "v_mfma_f32_16x16x32_bf16 v[68:71], v[172:175], v[100:103], 0\n"
        "s_nop 0\n"
        "v_cndmask_b32 v40, v40, v41, s[82:83]\n"
        "s_nop 0\n"
        "v_cndmask_b32 v48, v48, v49, s[82:83]\n"
        "s_nop 0\n"
        "v_cndmask_b32 v56, v56, v57, s[82:83]\n"
        "s_nop 0\n"
        "v_cndmask_b32 v64, v64, v65, s[82:83]\n"
        "v_cndmask_b32 v42, v42, v43, s[82:83]\n"
        "v_cndmask_b32 v50, v50, v51, s[82:83]\n"
        "v_cndmask_b32 v58, v58, v59, s[82:83]\n"
        "v_cndmask_b32 v66, v66, v67, s[82:83]\n"
        "v_cndmask_b32 v44, v44, v45, s[82:83]\n"
        "v_cndmask_b32 v52, v52, v53, s[82:83]\n"
        "v_cndmask_b32 v60, v60, v61, s[82:83]\n"
        "v_cndmask_b32 v68, v68, v69, s[82:83]\n"
        "v_cndmask_b32 v46, v46, v47, s[82:83]\n"
        "v_cndmask_b32 v54, v54, v55, s[82:83]\n"
        "v_cndmask_b32 v62, v62, v63, s[82:83]\n"
        "v_cndmask_b32 v70, v70, v71, s[82:83]\n"
        "v_cndmask_b32 v40, v40, v233, vcc\n"
        "v_cndmask_b32 v42, v42, v233, vcc\n"
        "v_cndmask_b32 v40, v40, v44, s[84:85]\n"
        "v_cndmask_b32 v48, v48, v52, s[84:85]\n"
        "v_cndmask_b32 v56, v56, v60, s[84:85]\n"
        "v_cndmask_b32 v64, v64, v68, s[84:85]\n"
        "v_cndmask_b32 v41, v42, v46, s[86:87]\n"
        "v_cndmask_b32 v49, v50, v54, s[86:87]\n"
        "v_cndmask_b32 v57, v58, v62, s[86:87]\n"
        "v_cndmask_b32 v65, v66, v70, s[86:87]\n"
        "v_max_f32 v42, v40, v41\n"
        "v_max_f32 v50, v48, v49\n"
        "v_max_f32 v58, v56, v57\n"
        "v_max_f32 v66, v64, v65\n"
        "v_permlane32_swap_b32 v42, v50\n"
        "s_nop 0\n"
        "v_permlane32_swap_b32 v58, v66\n"
        "v_max_f32 v42, v42, v50\n"
        "v_max_f32 v58, v58, v66\n"
        "s_nop 1\n"
        "v_permlane16_swap_b32 v42, v58\n"
        "v_max_f32 v42, v42, v58\n"
        "v_mul_f32 v58, s68, v42\n"
        "v_mul_f32 v42, s68, v42\n"
        "s_nop 1\n"
        "v_permlane16_swap_b32 v42, v58\n"
        "v_mov_b32 v50, v42\n"
        "v_mov_b32 v66, v58\n"
        "s_nop 0\n"
        "v_permlane32_swap_b32 v42, v50\n"
        "v_permlane32_swap_b32 v58, v66\n"
        "v_fma_f32 v40, v40, s69, v42\n"
        "v_fma_f32 v48, v48, s69, v50\n"
        "v_fma_f32 v56, v56, s69, v58\n"
        "v_fma_f32 v64, v64, s69, v66\n"
        "v_fma_f32 v41, v41, s69, v42\n"
        "v_fma_f32 v49, v49, s69, v50\n"
        "v_fma_f32 v57, v57, s69, v58\n"
        "v_fma_f32 v65, v65, s69, v66\n"
        "v_exp_f32 v40, v40\n"
        "v_exp_f32 v48, v48\n"
        "v_exp_f32 v56, v56\n"
        "v_exp_f32 v64, v64\n"
        "v_exp_f32 v41, v41\n"
        "v_exp_f32 v49, v49\n"
        "v_exp_f32 v57, v57\n"
        "v_exp_f32 v65, v65\n"
        "v_cvt_pk_bf16_f32 v40, v40, v41\n"
        "v_cvt_pk_bf16_f32 v48, v48, v49\n"
        "v_cvt_pk_bf16_f32 v56, v56, v57\n"
        "v_cvt_pk_bf16_f32 v64, v64, v65\n"
        "v_perm_b32 v120, v40, v40, v236\n"
        "v_perm_b32 v124, v48, v48, v236\n"
        "v_perm_b32 v128, v56, v56, v236\n"
        "v_perm_b32 v132, v64, v64, v236\n"
        "v_perm_b32 v121, v40, v40, v237\n"
        "v_perm_b32 v125, v48, v48, v237\n"
        "v_perm_b32 v129, v56, v56, v237\n"
        "v_perm_b32 v133, v64, v64, v237\n"
        "v_perm_b32 v122, v40, v40, v238\n"
        "v_perm_b32 v126, v48, v48, v238\n"
        "v_perm_b32 v130, v56, v56, v238\n"
        "v_perm_b32 v134, v64, v64, v238\n"
        "v_perm_b32 v123, v40, v40, v239\n"
        "v_perm_b32 v127, v48, v48, v239\n"
        "v_perm_b32 v131, v56, v56, v239\n"
        "v_perm_b32 v135, v64, v64, v239\n"
        "v_mfma_f32_16x16x32_bf16 v[88:91], v[216:219], v[120:123], 0\n"
        "v_mfma_f32_16x16x32_bf16 v[92:95], v[216:219], v[124:127], 0\n"
        "v_mfma_f32_16x16x32_bf16 v[96:99], v[216:219], v[128:131], 0\n"
        "v_mfma_f32_16x16x32_bf16 v[100:103], v[216:219], v[132:135], 0\n"
        "s_waitcnt vmcnt(8)\n"
        "v_mfma_f32_16x16x32_bf16 v[40:43], v[196:199], v[120:123], 0\n"
        "s_waitcnt vmcnt(5)\n"
        "v_mfma_f32_16x16x32_bf16 v[44:47], v[206:209], v[120:123], 0\n"
        "v_mfma_f32_16x16x32_bf16 v[48:51], v[198:201], v[124:127], 0\n"
        "v_mfma_f32_16x16x32_bf16 v[52:55], v[208:211], v[124:127], 0\n"
        "v_mfma_f32_16x16x32_bf16 v[56:59], v[200:203], v[128:131], 0\n"
        "s_waitcnt vmcnt(4)\n"
        "v_mfma_f32_16x16x32_bf16 v[60:63], v[210:213], v[128:131], 0\n"
        "v_mfma_f32_16x16x32_bf16 v[64:67], v[202:205], v[132:135], 0\n"
        "v_mfma_f32_16x16x32_bf16 v[68:71], v[212:215], v[132:135], 0\n"
        "v_rcp_f32 v88, v88\n"
        "v_rcp_f32 v92, v92\n"
        "v_rcp_f32 v96, v96\n"
        "v_rcp_f32 v100, v100\n"
        "v_mov_b32 v89, v88\n"
        "v_mov_b32 v93, v92\n"
        "v_mov_b32 v97, v96\n"
        "v_mov_b32 v101, v100\n"
        "v_pk_mul_f32 v[40:41], v[40:41], v[88:89]\n"
        "v_pk_mul_f32 v[42:43], v[42:43], v[88:89]\n"
        "v_pk_mul_f32 v[44:45], v[44:45], v[88:89]\n"
        "v_pk_mul_f32 v[46:47], v[46:47], v[88:89]\n"
        "v_cvt_pk_bf16_f32 v88, v40, v41\n"
        "v_cvt_pk_bf16_f32 v89, v42, v43\n"
        "v_cvt_pk_bf16_f32 v90, v44, v45\n"
        "v_cvt_pk_bf16_f32 v91, v46, v47\n"
        "s_mov_b32 s66, 73728\n"
        "buffer_store_dwordx4 v[88:91], v231, s[60:63], s66 offen offset:0 sc1\n"
        "v_pk_mul_f32 v[48:49], v[48:49], v[92:93]\n"
        "v_pk_mul_f32 v[50:51], v[50:51], v[92:93]\n"
        "v_pk_mul_f32 v[52:53], v[52:53], v[92:93]\n"
        "v_pk_mul_f32 v[54:55], v[54:55], v[92:93]\n"
        "v_cvt_pk_bf16_f32 v92, v48, v49\n"
        "v_cvt_pk_bf16_f32 v93, v50, v51\n"
        "v_cvt_pk_bf16_f32 v94, v52, v53\n"
        "v_cvt_pk_bf16_f32 v95, v54, v55\n"
        "buffer_store_dwordx4 v[92:95], v231, s[60:63], s66 offen offset:1024 sc1\n"
        "v_pk_mul_f32 v[56:57], v[56:57], v[96:97]\n"
        "v_pk_mul_f32 v[58:59], v[58:59], v[96:97]\n"
        "v_pk_mul_f32 v[60:61], v[60:61], v[96:97]\n"
        "v_pk_mul_f32 v[62:63], v[62:63], v[96:97]\n"
        "v_cvt_pk_bf16_f32 v96, v56, v57\n"
        "v_cvt_pk_bf16_f32 v97, v58, v59\n"
        "v_cvt_pk_bf16_f32 v98, v60, v61\n"
        "v_cvt_pk_bf16_f32 v99, v62, v63\n"
        "buffer_store_dwordx4 v[96:99], v231, s[60:63], s66 offen offset:2048 sc1\n"
        "v_pk_mul_f32 v[64:65], v[64:65], v[100:101]\n"
        "v_pk_mul_f32 v[66:67], v[66:67], v[100:101]\n"
        "v_pk_mul_f32 v[68:69], v[68:69], v[100:101]\n"
        "v_pk_mul_f32 v[70:71], v[70:71], v[100:101]\n"
        "v_cvt_pk_bf16_f32 v100, v64, v65\n"
        "v_cvt_pk_bf16_f32 v101, v66, v67\n"
        "v_cvt_pk_bf16_f32 v102, v68, v69\n"
        "v_cvt_pk_bf16_f32 v103, v70, v71\n"
        "buffer_store_dwordx4 v[100:103], v231, s[60:63], s66 offen offset:3072 sc1\n"
        "s_waitcnt vmcnt(0)\n"
        "s_add_u32 s66, s95, 393216\n"
        "s_mov_b64 exec, 0xffff\n"
        "buffer_store_dwordx2 v[242:243], v2, s[60:63], s66 offen sc1\n"
        "s_mov_b64 exec, -1\n"
        "s_branch Lend\n"
        "Lrole_A:\n"
        "s_setprio 0\n"
        "s_lshl_b32 s75, s76, 2\n"
        "s_add_u32 s75, s75, s97\n"
        "s_lshl_b32 s74, s72, 1\n"
        "s_add_u32 s74, s74, s96\n"
        "s_lshl_b32 s80, s74, 1\n"
        "v_bfe_u32 v233, v1, 2, 2\n"
        "v_and_b32 v234, 1, v4\n"
        "v_lshl_add_u32 v234, v234, 2, v233\n"
        "v_lshlrev_b32 v234, 5, v234\n"
        "v_lshl_add_u32 v233, v4, 3, v233\n"
        "v_lshlrev_b32 v233, 9, v233\n"
        "v_and_b32 v235, 3, v1\n"
        "s_mul_i32 s67, s97, 112\n"
        "s_lshl_b32 s68, s76, 6\n"
        "s_add_u32 s67, s67, s68\n"
        "v_lshl_add_u32 v235, v235, 3, s67\n"
        "v_add_u32 v220, 0, v235\n"
        "v_xor_b32 v220, v220, v234\n"
        "v_add_u32 v220, v220, v233\n"
        "v_add_u32 v221, 32, v235\n"
        "v_xor_b32 v221, v221, v234\n"
        "v_add_u32 v221, v221, v233\n"
        "v_add_u32 v222, 56, v235\n"
        "v_xor_b32 v222, v222, v234\n"
        "v_add_u32 v222, v222, v233\n"
        "v_add_u32 v223, 88, v235\n"
        "v_min_u32 v223, 504, v223\n"
        "v_xor_b32 v223, v223, v234\n"
        "v_add_u32 v223, v223, v233\n"
        "s_mul_i32 s67, s80, 28672\n"
        "s_mul_i32 s68, s75, 3584\n"
        "s_add_u32 s68, s68, s67\n"
        "s_add_u32 s68, s68, 11505664\n"
        "v_lshl_add_u32 v233, v3, 4, s68\n"
        "v_lshl_add_u32 v228, v4, 8, v233\n"
        "v_mov_b32 v235, 0x40000000\n"
        "v_add_u32 v229, 1024, v228\n"
        "v_cmp_eq_u32 vcc, 3, v4\n"
        "s_nop 1\n"
        "v_cndmask_b32 v229, v229, v235, vcc\n"
        "s_mul_i32 s68, s75, 896\n"
        "s_add_u32 s68, s68, s67\n"
        "s_add_u32 s68, s68, 13602816\n"
        "v_mul_u32_u24 v233, 7168, v4\n"
        "v_lshl_add_u32 v233, v3, 4, v233\n"
        "v_add_u32 v230, s68, v233\n"
        "v_cmp_le_u32 vcc, 12, v3\n"
        "s_nop 1\n"
        "v_cndmask_b32 v226, v230, v235, vcc\n"
        "v_cmp_gt_u32 vcc, 4, v3\n"
        "s_nop 1\n"
        "v_cndmask_b32 v227, v230, v235, vcc\n"
        "s_cmp_eq_u32 s80, 0\n"
        "s_cselect_b32 s73, 28672, 0\n"
        "s_and_b32 s68, s75, 1\n"
        "s_add_u32 s67, s80, s68\n"
        "s_mul_i32 s67, s67, 36\n"
        "s_lshr_b32 s68, s75, 1\n"
        "s_add_u32 s67, s67, s68\n"
        "s_lshl_b32 s67, s67, 12\n"
        "s_add_u32 s67, s67, 1105920\n"
        "v_lshl_add_u32 v231, v1, 4, s67\n"
        "s_mul_i32 s67, s75, 56\n"
        "v_bfe_u32 v233, v1, 2, 1\n"
        "v_mul_u32_u24 v233, 55, v233\n"
        "v_add_u32 v233, s67, v233\n"
        "v_lshrrev_b32 v233, 6, v233\n"
        "v_bfe_u32 v234, v1, 3, 1\n"
        "v_mul_u32_u24 v234, 7, v234\n"
        "v_add_u32 v233, v233, v234\n"
        "v_min_u32 v234, 2, v4\n"
        "v_add_u32 v234, s80, v234\n"
        "v_max_u32 v234, 1, v234\n"
        "v_lshlrev_b32 v234, 11, v234\n"
        "v_lshl_add_u32 v233, v233, 7, v234\n"
        "v_add_u32 v232, 2048, v233\n"
        "s_lshl_b32 s95, s74, 4\n"
        "s_add_u32 s95, s95, s75\n"
        "s_lshl_b32 s95, s95, 7\n"
        "s_add_u32 s95, s95, 4096\n"
        "v_and_b32 v233, 1, v3\n"
        "v_cmp_eq_u32 s[82:83], 1, v233\n"
        "v_lshl_add_u32 v234, v4, 2, v233\n"
        "v_cmp_le_u32 s[84:85], v234, v3\n"
        "v_add_u32 v234, 2, v234\n"
        "v_cmp_le_u32 s[86:87], v234, v3\n"
        "v_mov_b32 v233, 0xc0c0100\n"
        "v_mov_b32 v234, 0x1000c0c\n"
        "v_mov_b32 v235, 0x0c0c0c0c\n"
        "v_cndmask_b32 v233, v233, v234, s[82:83]\n"
        "v_cndmask_b32 v236, v233, v235, s[84:85]\n"
        "v_cndmask_b32 v238, v235, v233, s[84:85]\n"
        "v_mov_b32 v233, 0xc0c0302\n"
        "v_mov_b32 v234, 0x3020c0c\n"
        "v_mov_b32 v235, 0x0c0c0c0c\n"
        "v_cndmask_b32 v233, v233, v234, s[82:83]\n"
        "v_cndmask_b32 v237, v233, v235, s[86:87]\n"
        "v_cndmask_b32 v239, v235, v233, s[86:87]\n"
        "s_waitcnt vmcnt(7)\n"
        "v_readfirstlane_b32 s67, v244\n"
        "v_readfirstlane_b32 s68, v245\n"
        "s_mul_hi_u32 s71, s67, 0x4c957f2d\n"
        "s_mul_i32 s69, s67, 0x5851f42d\n"
        "s_add_u32 s71, s71, s69\n"
        "s_mul_i32 s69, s68, 0x4c957f2d\n"
        "s_add_u32 s71, s71, s69\n"
        "s_mul_i32 s70, s67, 0x4c957f2d\n"
        "s_add_u32 s70, s70, 0xf767814f\n"
        "s_addc_u32 s71, s71, 0x14057b7e\n"
        "v_mov_b32 v242, s70\n"
        "v_mov_b32 v243, s71\n"
        "s_mov_b32 s66, 0xd0b0c0a\n"
        "s_lshl_b32 s79, s97, 3\n"
        "s_add_u32 s79, s79, 0x40000\n"
        "s_bfe_u32 s66, s66, s79\n"
        "s_cbranch_scc0 Lvb14\n"
        "s_lshl_b32 s66, s66, 12\n"
        "s_mov_b64 exec, 0\n"
        "s_getpc_b64 s[90:91]\n"
        "Lvp14:\n"
        "s_sub_u32 s66, s66, Lvp14-Lp0\n"
        "s_ashr_i32 s79, s66, 31\n"
        "s_add_u32 s90, s90, s66\n"
        "s_addc_u32 s91, s91, s79\n"
        "s_swappc_b64 vcc, s[90:91]\n"
        "s_mov_b64 exec, -1\n"
        "Lvb14:\n"
        "v_add_u32 v216, 0, v220\n"
        "v_add_u32 v217, 0, v221\n"
        "v_add_u32 v218, 0, v222\n"
        "v_add_u32 v219, 0, v223\n"
        "s_waitcnt vmcnt(5) lgkmcnt(0)\n"
        "s_barrier\n"
        "ds_read_b128 v[72:75], v250 offset:0\n"
        "ds_read_b64_tr_b16 v[104:105], v216 offset:0\n"
        "ds_read_b64_tr_b16 v[106:107], v216 offset:2048\n"
        "ds_read_b128 v[76:79], v252 offset:0\n"
        "ds_read_b64_tr_b16 v[108:109], v217 offset:0\n"
        "ds_read_b64_tr_b16 v[110:111], v217 offset:2048\n"
        "ds_read_b128 v[80:83], v250 offset:2048\n"
        "ds_read_b64_tr_b16 v[112:113], v218 offset:0\n"
        "ds_read_b64_tr_b16 v[114:115], v218 offset:2048\n"
        "ds_read_b128 v[84:87], v252 offset:2048\n"
        "ds_read_b64_tr_b16 v[116:117], v219 offset:0\n"
        "ds_read_b64_tr_b16 v[118:119], v219 offset:2048\n"
        "v_add_u32 v216, 16384, v220\n"
        "v_add_u32 v217, 16384, v221\n"
        "v_add_u32 v218, 16384, v222\n"
        "v_add_u32 v219, 16384, v223\n"
        "s_waitcnt vmcnt(3) lgkmcnt(0)\n"
        "s_barrier\n"
        "v_mfma_f32_16x16x32_bf16 v[8:11], v[104:107], v[72:75], 0\n"
        "ds_read_b128 v[88:91], v251 offset:0\n"
        "v_mfma_f32_16x16x32_bf16 v[24:27], v[108:111], v[72:75], 0\n"
        "ds_read_b64_tr_b16 v[120:121], v216 offset:0\n"
        "v_mfma_f32_16x16x32_bf16 v[40:43], v[112:115], v[72:75], 0\n"
        "ds_read_b64_tr_b16 v[122:123], v216 offset:2048\n"
        "v_mfma_f32_16x16x32_bf16 v[56:59], v[116:119], v[72:75], 0\n"
        "ds_read_b128 v[92:95], v253 offset:0\n"
        "v_mfma_f32_16x16x32_bf16 v[12:15], v[104:107], v[76:79], 0\n"
        "ds_read_b64_tr_b16 v[124:125], v217 offset:0\n"
        "v_mfma_f32_16x16x32_bf16 v[28:31], v[108:111], v[76:79], 0\n"
        "ds_read_b64_tr_b16 v[126:127], v217 offset:2048\n"
        "v_mfma_f32_16x16x32_bf16 v[44:47], v[112:115], v[76:79], 0\n"
        "ds_read_b128 v[96:99], v251 offset:2048\n"
        "v_mfma_f32_16x16x32_bf16 v[60:63], v[116:119], v[76:79], 0\n"
        "ds_read_b64_tr_b16 v[128:129], v218 offset:0\n"
        "v_mfma_f32_16x16x32_bf16 v[16:19], v[104:107], v[80:83], 0\n"
        "ds_read_b64_tr_b16 v[130:131], v218 offset:2048\n"
        "v_mfma_f32_16x16x32_bf16 v[32:35], v[108:111], v[80:83], 0\n"
        "ds_read_b128 v[100:103], v253 offset:2048\n"
        "v_mfma_f32_16x16x32_bf16 v[48:51], v[112:115], v[80:83], 0\n"
        "ds_read_b64_tr_b16 v[132:133], v219 offset:0\n"
        "v_mfma_f32_16x16x32_bf16 v[64:67], v[116:119], v[80:83], 0\n"
        "ds_read_b64_tr_b16 v[134:135], v219 offset:2048\n"
        "v_mfma_f32_16x16x32_bf16 v[20:23], v[104:107], v[84:87], 0\n"
        "v_add_u32 v216, 32768, v220\n"
        "v_add_u32 v217, 32768, v221\n"
        "v_add_u32 v218, 32768, v222\n"
        "v_add_u32 v219, 32768, v223\n"
        "v_mfma_f32_16x16x32_bf16 v[36:39], v[108:111], v[84:87], 0\n"
        "v_mfma_f32_16x16x32_bf16 v[52:55], v[112:115], v[84:87], 0\n"
        "v_mfma_f32_16x16x32_bf16 v[68:71], v[116:119], v[84:87], 0\n"
        "s_add_u32 m0, s65, 0\n"
        "s_mul_i32 s66, s5, 172032\n"
        "s_add_u32 s66, s66, 86016\n"
        "buffer_load_dwordx4 v224, s[36:39], s66 offen lds\n"
        "s_add_u32 m0, s65, 4096\n"
        "s_mul_i32 s66, s5, 172032\n"
        "s_add_u32 s66, s66, 86016\n"
        "buffer_load_dwordx4 v225, s[36:39], s66 offen lds\n"
        "s_add_u32 m0, s65, 8192\n"
        "s_mul_i32 s66, s5, 172032\n"
        "s_add_u32 s66, s66, 129024\n"
        "buffer_load_dwordx4 v224, s[36:39], s66 offen lds\n"
        "s_add_u32 m0, s65, 12288\n"
        "s_mul_i32 s66, s5, 172032\n"
        "s_add_u32 s66, s66, 129024\n"
        "buffer_load_dwordx4 v225, s[36:39], s66 offen lds\n"
        "s_mov_b32 s66, 0xd0b0c0a\n"
        "s_lshl_b32 s79, s97, 3\n"
        "s_add_u32 s79, s79, 0x40004\n"
        "s_bfe_u32 s66, s66, s79\n"
        "s_cbranch_scc0 Lvb15\n"
        "s_lshl_b32 s66, s66, 12\n"
        "s_mov_b64 exec, 0\n"
        "s_getpc_b64 s[90:91]\n"
        "Lvp15:\n"
        "s_sub_u32 s66, s66, Lvp15-Lp0\n"
        "s_ashr_i32 s79, s66, 31\n"
        "s_add_u32 s90, s90, s66\n"
        "s_addc_u32 s91, s91, s79\n"
        "s_swappc_b64 vcc, s[90:91]\n"
        "s_mov_b64 exec, -1\n"
        "Lvb15:\n"
        "s_waitcnt vmcnt(5) lgkmcnt(0)\n"
        "s_barrier\n"
        "v_mfma_f32_16x16x32_bf16 v[8:11], v[120:123], v[88:91], v[8:11]\n"
        "ds_read_b128 v[72:75], v250 offset:8192\n"
        "v_mfma_f32_16x16x32_bf16 v[24:27], v[124:127], v[88:91], v[24:27]\n"
        "ds_read_b64_tr_b16 v[104:105], v216 offset:0\n"
        "v_mfma_f32_16x16x32_bf16 v[40:43], v[128:131], v[88:91], v[40:43]\n"
        "ds_read_b64_tr_b16 v[106:107], v216 offset:2048\n"
        "v_mfma_f32_16x16x32_bf16 v[56:59], v[132:135], v[88:91], v[56:59]\n"
        "ds_read_b128 v[76:79], v252 offset:8192\n"
        "v_mfma_f32_16x16x32_bf16 v[12:15], v[120:123], v[92:95], v[12:15]\n"
        "ds_read_b64_tr_b16 v[108:109], v217 offset:0\n"
        "v_mfma_f32_16x16x32_bf16 v[28:31], v[124:127], v[92:95], v[28:31]\n"
        "ds_read_b64_tr_b16 v[110:111], v217 offset:2048\n"
        "v_mfma_f32_16x16x32_bf16 v[44:47], v[128:131], v[92:95], v[44:47]\n"
        "ds_read_b128 v[80:83], v250 offset:10240\n"
        "v_mfma_f32_16x16x32_bf16 v[60:63], v[132:135], v[92:95], v[60:63]\n"
        "ds_read_b64_tr_b16 v[112:113], v218 offset:0\n"
        "v_mfma_f32_16x16x32_bf16 v[16:19], v[120:123], v[96:99], v[16:19]\n"
        "ds_read_b64_tr_b16 v[114:115], v218 offset:2048\n"
        "v_mfma_f32_16x16x32_bf16 v[32:35], v[124:127], v[96:99], v[32:35]\n"
        "ds_read_b128 v[84:87], v252 offset:10240\n"
        "v_mfma_f32_16x16x32_bf16 v[48:51], v[128:131], v[96:99], v[48:51]\n"
        "ds_read_b64_tr_b16 v[116:117], v219 offset:0\n"
        "v_mfma_f32_16x16x32_bf16 v[64:67], v[132:135], v[96:99], v[64:67]\n"
        "ds_read_b64_tr_b16 v[118:119], v219 offset:2048\n"
        "v_mfma_f32_16x16x32_bf16 v[20:23], v[120:123], v[100:103], v[20:23]\n"
        "v_add_u32 v216, 0, v220\n"
        "v_add_u32 v217, 0, v221\n"
        "v_add_u32 v218, 0, v222\n"
        "v_add_u32 v219, 0, v223\n"
        "v_mfma_f32_16x16x32_bf16 v[36:39], v[124:127], v[100:103], v[36:39]\n"
        "v_mfma_f32_16x16x32_bf16 v[52:55], v[128:131], v[100:103], v[52:55]\n"
        "v_mfma_f32_16x16x32_bf16 v[68:71], v[132:135], v[100:103], v[68:71]\n"
        "s_add_u32 m0, s65, 16384\n"
        "s_mul_i32 s66, s6, 172032\n"
        "buffer_load_dwordx4 v224, s[36:39], s66 offen lds\n"
        "s_add_u32 m0, s65, 20480\n"
        "s_mul_i32 s66, s6, 172032\n"
        "buffer_load_dwordx4 v225, s[36:39], s66 offen lds\n"
        "s_add_u32 m0, s65, 24576\n"
        "s_mul_i32 s66, s6, 172032\n"
        "s_add_u32 s66, s66, 43008\n"
        "buffer_load_dwordx4 v224, s[36:39], s66 offen lds\n"
        "s_add_u32 m0, s65, 28672\n"
        "s_mul_i32 s66, s6, 172032\n"
        "s_add_u32 s66, s66, 43008\n"
        "buffer_load_dwordx4 v225, s[36:39], s66 offen lds\n"
        "s_waitcnt vmcnt(4) lgkmcnt(0)\n"
        "s_barrier\n"
        "v_mfma_f32_16x16x32_bf16 v[8:11], v[104:107], v[72:75], v[8:11]\n"
        "ds_read_b128 v[88:91], v251 offset:8192\n"
        "v_mfma_f32_16x16x32_bf16 v[24:27], v[108:111], v[72:75], v[24:27]\n"
        "ds_read_b64_tr_b16 v[120:121], v216 offset:0\n"
        "v_mfma_f32_16x16x32_bf16 v[40:43], v[112:115], v[72:75], v[40:43]\n"
        "ds_read_b64_tr_b16 v[122:123], v216 offset:2048\n"
        "v_mfma_f32_16x16x32_bf16 v[56:59], v[116:119], v[72:75], v[56:59]\n"
        "ds_read_b128 v[92:95], v253 offset:8192\n"
        "v_mfma_f32_16x16x32_bf16 v[12:15], v[104:107], v[76:79], v[12:15]\n"
        "ds_read_b64_tr_b16 v[124:125], v217 offset:0\n"
        "v_mfma_f32_16x16x32_bf16 v[28:31], v[108:111], v[76:79], v[28:31]\n"
        "ds_read_b64_tr_b16 v[126:127], v217 offset:2048\n"
        "v_mfma_f32_16x16x32_bf16 v[44:47], v[112:115], v[76:79], v[44:47]\n"
        "ds_read_b128 v[96:99], v251 offset:10240\n"
        "v_mfma_f32_16x16x32_bf16 v[60:63], v[116:119], v[76:79], v[60:63]\n"
        "ds_read_b64_tr_b16 v[128:129], v218 offset:0\n"
        "v_mfma_f32_16x16x32_bf16 v[16:19], v[104:107], v[80:83], v[16:19]\n"
        "ds_read_b64_tr_b16 v[130:131], v218 offset:2048\n"
        "v_mfma_f32_16x16x32_bf16 v[32:35], v[108:111], v[80:83], v[32:35]\n"
        "ds_read_b128 v[100:103], v253 offset:10240\n"
        "v_mfma_f32_16x16x32_bf16 v[48:51], v[112:115], v[80:83], v[48:51]\n"
        "ds_read_b64_tr_b16 v[132:133], v219 offset:0\n"
        "v_mfma_f32_16x16x32_bf16 v[64:67], v[116:119], v[80:83], v[64:67]\n"
        "ds_read_b64_tr_b16 v[134:135], v219 offset:2048\n"
        "v_mfma_f32_16x16x32_bf16 v[20:23], v[104:107], v[84:87], v[20:23]\n"
        "v_add_u32 v216, 16384, v220\n"
        "v_add_u32 v217, 16384, v221\n"
        "v_add_u32 v218, 16384, v222\n"
        "v_add_u32 v219, 16384, v223\n"
        "v_mfma_f32_16x16x32_bf16 v[36:39], v[108:111], v[84:87], v[36:39]\n"
        "v_mfma_f32_16x16x32_bf16 v[52:55], v[112:115], v[84:87], v[52:55]\n"
        "v_mfma_f32_16x16x32_bf16 v[68:71], v[116:119], v[84:87], v[68:71]\n"
        "s_add_u32 m0, s65, 32768\n"
        "s_mul_i32 s66, s6, 172032\n"
        "s_add_u32 s66, s66, 86016\n"
        "buffer_load_dwordx4 v224, s[36:39], s66 offen lds\n"
        "s_add_u32 m0, s65, 36864\n"
        "s_mul_i32 s66, s6, 172032\n"
        "s_add_u32 s66, s66, 86016\n"
        "buffer_load_dwordx4 v225, s[36:39], s66 offen lds\n"
        "s_add_u32 m0, s65, 40960\n"
        "s_mul_i32 s66, s6, 172032\n"
        "s_add_u32 s66, s66, 129024\n"
        "buffer_load_dwordx4 v224, s[36:39], s66 offen lds\n"
        "s_add_u32 m0, s65, 45056\n"
        "s_mul_i32 s66, s6, 172032\n"
        "s_add_u32 s66, s66, 129024\n"
        "buffer_load_dwordx4 v225, s[36:39], s66 offen lds\n"
        "s_waitcnt vmcnt(4) lgkmcnt(0)\n"
        "s_barrier\n"
        "v_mfma_f32_16x16x32_bf16 v[8:11], v[120:123], v[88:91], v[8:11]\n"
        "ds_read_b128 v[72:75], v250 offset:16384\n"
        "v_mfma_f32_16x16x32_bf16 v[24:27], v[124:127], v[88:91], v[24:27]\n"
        "ds_read_b64_tr_b16 v[104:105], v216 offset:0\n"
        "v_mfma_f32_16x16x32_bf16 v[40:43], v[128:131], v[88:91], v[40:43]\n"
        "ds_read_b64_tr_b16 v[106:107], v216 offset:2048\n"
        "v_mfma_f32_16x16x32_bf16 v[56:59], v[132:135], v[88:91], v[56:59]\n"
        "ds_read_b128 v[76:79], v252 offset:16384\n"
        "v_mfma_f32_16x16x32_bf16 v[12:15], v[120:123], v[92:95], v[12:15]\n"
        "ds_read_b64_tr_b16 v[108:109], v217 offset:0\n"
        "v_mfma_f32_16x16x32_bf16 v[28:31], v[124:127], v[92:95], v[28:31]\n"
        "ds_read_b64_tr_b16 v[110:111], v217 offset:2048\n"
        "v_mfma_f32_16x16x32_bf16 v[44:47], v[128:131], v[92:95], v[44:47]\n"
        "ds_read_b128 v[80:83], v250 offset:18432\n"
        "v_mfma_f32_16x16x32_bf16 v[60:63], v[132:135], v[92:95], v[60:63]\n"
        "ds_read_b64_tr_b16 v[112:113], v218 offset:0\n"
        "v_mfma_f32_16x16x32_bf16 v[16:19], v[120:123], v[96:99], v[16:19]\n"
        "ds_read_b64_tr_b16 v[114:115], v218 offset:2048\n"
        "v_mfma_f32_16x16x32_bf16 v[32:35], v[124:127], v[96:99], v[32:35]\n"
        "ds_read_b128 v[84:87], v252 offset:18432\n"
        "v_mfma_f32_16x16x32_bf16 v[48:51], v[128:131], v[96:99], v[48:51]\n"
        "ds_read_b64_tr_b16 v[116:117], v219 offset:0\n"
        "v_mfma_f32_16x16x32_bf16 v[64:67], v[132:135], v[96:99], v[64:67]\n"
        "ds_read_b64_tr_b16 v[118:119], v219 offset:2048\n"
        "v_mfma_f32_16x16x32_bf16 v[20:23], v[120:123], v[100:103], v[20:23]\n"
        "v_add_u32 v216, 32768, v220\n"
        "v_add_u32 v217, 32768, v221\n"
        "v_add_u32 v218, 32768, v222\n"
        "v_add_u32 v219, 32768, v223\n"
        "v_mfma_f32_16x16x32_bf16 v[36:39], v[124:127], v[100:103], v[36:39]\n"
        "v_mfma_f32_16x16x32_bf16 v[52:55], v[128:131], v[100:103], v[52:55]\n"
        "v_mfma_f32_16x16x32_bf16 v[68:71], v[132:135], v[100:103], v[68:71]\n"
        "s_add_u32 m0, s65, 0\n"
        "s_mul_i32 s66, s7, 172032\n"
        "buffer_load_dwordx4 v224, s[36:39], s66 offen lds\n"
        "s_add_u32 m0, s65, 4096\n"
        "s_mul_i32 s66, s7, 172032\n"
        "buffer_load_dwordx4 v225, s[36:39], s66 offen lds\n"
        "s_add_u32 m0, s65, 8192\n"
        "s_mul_i32 s66, s7, 172032\n"
        "s_add_u32 s66, s66, 43008\n"
        "buffer_load_dwordx4 v224, s[36:39], s66 offen lds\n"
        "s_add_u32 m0, s65, 12288\n"
        "s_mul_i32 s66, s7, 172032\n"
        "s_add_u32 s66, s66, 43008\n"
        "buffer_load_dwordx4 v225, s[36:39], s66 offen lds\n"
        "s_add_u32 m0, s65, 49152\n"
        "s_mul_i32 s66, s7, 172032\n"
        "s_add_u32 s66, s66, 86016\n"
        "buffer_load_dwordx4 v224, s[36:39], s66 offen lds\n"
        "s_add_u32 m0, s65, 53248\n"
        "s_mul_i32 s66, s7, 172032\n"
        "s_add_u32 s66, s66, 86016\n"
        "buffer_load_dwordx4 v225, s[36:39], s66 offen lds\n"
        "s_mov_b32 s48, s26\n"
        "s_and_b32 s49, s27, 0xffff\n"
        "s_mov_b32 s50, 1605632\n"
        "s_mov_b32 s51, 0x20000\n"
        "s_lshl2_add_u32 s66, s94, s97\n"
        "s_mul_i32 s66, s66, 6272\n"
        "v_min_u32 v246, 48, v1\n"
        "v_lshlrev_b32 v246, 7, v246\n"
        "buffer_load_dword v247, v246, s[48:51], s66 offen\n"
        "s_mov_b32 s48, s26\n"
        "s_and_b32 s49, s27, 0xffff\n"
        "s_mov_b32 s50, 1605632\n"
        "s_mov_b32 s51, 0x20000\n"
        "s_lshl2_add_u32 s66, s94, s97\n"
        "s_mul_i32 s66, s66, 6272\n"
        "s_add_u32 s66, s66, 802816\n"
        "v_min_u32 v246, 48, v1\n"
        "v_lshlrev_b32 v246, 7, v246\n"
        "buffer_load_dword v248, v246, s[48:51], s66 offen\n"
        "s_mov_b32 s48, s20\n"
        "s_and_b32 s49, s21, 0xffff\n"
        "s_mov_b32 s50, 401408\n"
        "s_mov_b32 s51, 0x20000\n"
        "s_lshl2_add_u32 s66, s94, s97\n"
        "s_mul_i32 s66, s66, 3200\n"
        "v_min_u32 v246, 24, v1\n"
        "v_lshlrev_b32 v246, 7, v246\n"
        "buffer_load_dword v249, v246, s[48:51], s66 offen\n"
        "s_waitcnt vmcnt(9) lgkmcnt(0)\n"
        "s_barrier\n"
        "v_mfma_f32_16x16x32_bf16 v[8:11], v[104:107], v[72:75], v[8:11]\n"
        "ds_read_b128 v[88:91], v251 offset:16384\n"
        "v_mfma_f32_16x16x32_bf16 v[24:27], v[108:111], v[72:75], v[24:27]\n"
        "ds_read_b64_tr_b16 v[120:121], v216 offset:0\n"
        "v_mfma_f32_16x16x32_bf16 v[40:43], v[112:115], v[72:75], v[40:43]\n"
        "ds_read_b64_tr_b16 v[122:123], v216 offset:2048\n"
        "v_mfma_f32_16x16x32_bf16 v[56:59], v[116:119], v[72:75], v[56:59]\n"
        "ds_read_b128 v[92:95], v253 offset:16384\n"
        "v_mfma_f32_16x16x32_bf16 v[12:15], v[104:107], v[76:79], v[12:15]\n"
        "ds_read_b64_tr_b16 v[124:125], v217 offset:0\n"
        "v_mfma_f32_16x16x32_bf16 v[28:31], v[108:111], v[76:79], v[28:31]\n"
        "ds_read_b64_tr_b16 v[126:127], v217 offset:2048\n"
        "v_mfma_f32_16x16x32_bf16 v[44:47], v[112:115], v[76:79], v[44:47]\n"
        "ds_read_b128 v[96:99], v251 offset:18432\n"
        "v_mfma_f32_16x16x32_bf16 v[60:63], v[116:119], v[76:79], v[60:63]\n"
        "ds_read_b64_tr_b16 v[128:129], v218 offset:0\n"
        "v_mfma_f32_16x16x32_bf16 v[16:19], v[104:107], v[80:83], v[16:19]\n"
        "ds_read_b64_tr_b16 v[130:131], v218 offset:2048\n"
        "v_mfma_f32_16x16x32_bf16 v[32:35], v[108:111], v[80:83], v[32:35]\n"
        "ds_read_b128 v[100:103], v253 offset:18432\n"
        "v_mfma_f32_16x16x32_bf16 v[48:51], v[112:115], v[80:83], v[48:51]\n"
        "ds_read_b64_tr_b16 v[132:133], v219 offset:0\n"
        "v_mfma_f32_16x16x32_bf16 v[64:67], v[116:119], v[80:83], v[64:67]\n"
        "ds_read_b64_tr_b16 v[134:135], v219 offset:2048\n"
        "v_mfma_f32_16x16x32_bf16 v[20:23], v[104:107], v[84:87], v[20:23]\n"
        "v_add_u32 v216, 0, v220\n"
        "v_add_u32 v217, 0, v221\n"
        "v_add_u32 v218, 0, v222\n"
        "v_add_u32 v219, 0, v223\n"
        "v_mfma_f32_16x16x32_bf16 v[36:39], v[108:111], v[84:87], v[36:39]\n"
        "v_mfma_f32_16x16x32_bf16 v[52:55], v[112:115], v[84:87], v[52:55]\n"
        "s_nop 0\n"
        "s_cbranch_execz Lback\n"
        "v_mfma_f32_16x16x32_bf16 v[68:71], v[116:119], v[84:87], v[68:71]\n"
        "s_add_u32 m0, s65, 57344\n"
        "s_mul_i32 s66, s7, 172032\n"
        "s_add_u32 s66, s66, 129024\n"
        "buffer_load_dwordx4 v224, s[36:39], s66 offen lds\n"
        "s_add_u32 m0, s65, 61440\n"
        "s_mul_i32 s66, s7, 172032\n"
        "s_add_u32 s66, s66, 129024\n"
        "buffer_load_dwordx4 v225, s[36:39], s66 offen lds\n"
        "s_add_u32 m0, s65, 16384\n"
        "s_mul_i32 s66, s8, 172032\n"
        "buffer_load_dwordx4 v224, s[36:39], s66 offen lds\n"
        "s_add_u32 m0, s65, 20480\n"
        "s_mul_i32 s66, s8, 172032\n"
        "buffer_load_dwordx4 v225, s[36:39], s66 offen lds\n"
        "s_add_u32 m0, s65, 24576\n"
        "s_mul_i32 s66, s8, 172032\n"
        "s_add_u32 s66, s66, 43008\n"
        "buffer_load_dwordx4 v224, s[36:39], s66 offen lds\n"
        "s_add_u32 m0, s65, 28672\n"
        "s_mul_i32 s66, s8, 172032\n"
        "s_add_u32 s66, s66, 43008\n"
        "buffer_load_dwordx4 v225, s[36:39], s66 offen lds\n"
        "s_waitcnt vmcnt(11) lgkmcnt(0)\n"
        "s_barrier\n"
        "v_mfma_f32_16x16x32_bf16 v[8:11], v[120:123], v[88:91], v[8:11]\n"
        "ds_read_b128 v[72:75], v250 offset:24576\n"
        "v_mfma_f32_16x16x32_bf16 v[24:27], v[124:127], v[88:91], v[24:27]\n"
        "ds_read_b64_tr_b16 v[104:105], v216 offset:0\n"
        "v_mfma_f32_16x16x32_bf16 v[40:43], v[128:131], v[88:91], v[40:43]\n"
        "ds_read_b64_tr_b16 v[106:107], v216 offset:2048\n"
        "v_mfma_f32_16x16x32_bf16 v[56:59], v[132:135], v[88:91], v[56:59]\n"
        "ds_read_b128 v[76:79], v252 offset:24576\n"
        "v_mfma_f32_16x16x32_bf16 v[12:15], v[120:123], v[92:95], v[12:15]\n"
        "ds_read_b64_tr_b16 v[108:109], v217 offset:0\n"
        "v_mfma_f32_16x16x32_bf16 v[28:31], v[124:127], v[92:95], v[28:31]\n"
        "ds_read_b64_tr_b16 v[110:111], v217 offset:2048\n"
        "v_mfma_f32_16x16x32_bf16 v[44:47], v[128:131], v[92:95], v[44:47]\n"
        "ds_read_b128 v[80:83], v250 offset:26624\n"
        "v_mfma_f32_16x16x32_bf16 v[60:63], v[132:135], v[92:95], v[60:63]\n"
        "ds_read_b64_tr_b16 v[112:113], v218 offset:0\n"
        "v_mfma_f32_16x16x32_bf16 v[16:19], v[120:123], v[96:99], v[16:19]\n"
        "ds_read_b64_tr_b16 v[114:115], v218 offset:2048\n"
        "v_mfma_f32_16x16x32_bf16 v[32:35], v[124:127], v[96:99], v[32:35]\n"
        "ds_read_b128 v[84:87], v252 offset:26624\n"
        "v_mfma_f32_16x16x32_bf16 v[48:51], v[128:131], v[96:99], v[48:51]\n"
        "ds_read_b64_tr_b16 v[116:117], v219 offset:0\n"
        "v_mfma_f32_16x16x32_bf16 v[64:67], v[132:135], v[96:99], v[64:67]\n"
        "ds_read_b64_tr_b16 v[118:119], v219 offset:2048\n"
        "v_mfma_f32_16x16x32_bf16 v[20:23], v[120:123], v[100:103], v[20:23]\n"
        "v_add_u32 v216, 49152, v220\n"
        "v_add_u32 v217, 49152, v221\n"
        "v_add_u32 v218, 49152, v222\n"
        "v_add_u32 v219, 49152, v223\n"
        "v_mfma_f32_16x16x32_bf16 v[36:39], v[124:127], v[100:103], v[36:39]\n"
        "v_mfma_f32_16x16x32_bf16 v[52:55], v[128:131], v[100:103], v[52:55]\n"
        "v_mfma_f32_16x16x32_bf16 v[68:71], v[132:135], v[100:103], v[68:71]\n"
        "s_add_u32 m0, s65, 32768\n"
        "s_mul_i32 s66, s8, 172032\n"
        "s_add_u32 s66, s66, 86016\n"
        "buffer_load_dwordx4 v224, s[36:39], s66 offen lds\n"
        "s_add_u32 m0, s65, 36864\n"
        "s_mul_i32 s66, s8, 172032\n"
        "s_add_u32 s66, s66, 86016\n"
        "buffer_load_dwordx4 v225, s[36:39], s66 offen lds\n"
        "s_add_u32 m0, s65, 40960\n"
        "s_mul_i32 s66, s8, 172032\n"
        "s_add_u32 s66, s66, 129024\n"
        "buffer_load_dwordx4 v224, s[36:39], s66 offen lds\n"
        "s_add_u32 m0, s65, 45056\n"
        "s_mul_i32 s66, s8, 172032\n"
        "s_add_u32 s66, s66, 129024\n"
        "buffer_load_dwordx4 v225, s[36:39], s66 offen lds\n"
        "s_add_u32 m0, s65, 114688\n"
        "s_mul_i32 s66, s9, 172032\n"
        "buffer_load_dwordx4 v224, s[36:39], s66 offen lds\n"
        "s_add_u32 m0, s65, 118784\n"
        "s_mul_i32 s66, s9, 172032\n"
        "buffer_load_dwordx4 v225, s[36:39], s66 offen lds\n"
        "s_waitcnt vmcnt(10) lgkmcnt(0)\n"
        "s_barrier\n"
        "v_mfma_f32_16x16x32_bf16 v[8:11], v[104:107], v[72:75], v[8:11]\n"
        "ds_read_b128 v[88:91], v251 offset:24576\n"
        "v_mfma_f32_16x16x32_bf16 v[24:27], v[108:111], v[72:75], v[24:27]\n"
        "ds_read_b64_tr_b16 v[120:121], v216 offset:0\n"
        "v_mfma_f32_16x16x32_bf16 v[40:43], v[112:115], v[72:75], v[40:43]\n"
        "ds_read_b64_tr_b16 v[122:123], v216 offset:2048\n"
        "v_mfma_f32_16x16x32_bf16 v[56:59], v[116:119], v[72:75], v[56:59]\n"
        "ds_read_b128 v[92:95], v253 offset:24576\n"
        "v_mfma_f32_16x16x32_bf16 v[12:15], v[104:107], v[76:79], v[12:15]\n"
        "ds_read_b64_tr_b16 v[124:125], v217 offset:0\n"
        "v_mfma_f32_16x16x32_bf16 v[28:31], v[108:111], v[76:79], v[28:31]\n"
        "ds_read_b64_tr_b16 v[126:127], v217 offset:2048\n"
        "v_mfma_f32_16x16x32_bf16 v[44:47], v[112:115], v[76:79], v[44:47]\n"
        "ds_read_b128 v[96:99], v251 offset:26624\n"
        "v_mfma_f32_16x16x32_bf16 v[60:63], v[116:119], v[76:79], v[60:63]\n"
        "ds_read_b64_tr_b16 v[128:129], v218 offset:0\n"
        "v_mfma_f32_16x16x32_bf16 v[16:19], v[104:107], v[80:83], v[16:19]\n"
        "ds_read_b64_tr_b16 v[130:131], v218 offset:2048\n"
        "v_mfma_f32_16x16x32_bf16 v[32:35], v[108:111], v[80:83], v[32:35]\n"
        "ds_read_b128 v[100:103], v253 offset:26624\n"
        "v_mfma_f32_16x16x32_bf16 v[48:51], v[112:115], v[80:83], v[48:51]\n"
        "ds_read_b64_tr_b16 v[132:133], v219 offset:0\n"
        "v_mfma_f32_16x16x32_bf16 v[64:67], v[116:119], v[80:83], v[64:67]\n"
        "ds_read_b64_tr_b16 v[134:135], v219 offset:2048\n"
        "v_mfma_f32_16x16x32_bf16 v[20:23], v[104:107], v[84:87], v[20:23]\n"
        "v_add_u32 v216, 16384, v220\n"
        "v_add_u32 v217, 16384, v221\n"
        "v_add_u32 v218, 16384, v222\n"
        "v_add_u32 v219, 16384, v223\n"
        "v_mfma_f32_16x16x32_bf16 v[36:39], v[108:111], v[84:87], v[36:39]\n"
        "v_mfma_f32_16x16x32_bf16 v[52:55], v[112:115], v[84:87], v[52:55]\n"
        "v_mfma_f32_16x16x32_bf16 v[68:71], v[116:119], v[84:87], v[68:71]\n"
        "s_add_u32 m0, s65, 122880\n"
        "s_mul_i32 s66, s9, 172032\n"
        "s_add_u32 s66, s66, 43008\n"
        "buffer_load_dwordx4 v224, s[36:39], s66 offen lds\n"
        "s_add_u32 m0, s65, 126976\n"
        "s_mul_i32 s66, s9, 172032\n"
        "s_add_u32 s66, s66, 43008\n"
        "buffer_load_dwordx4 v225, s[36:39], s66 offen lds\n"
        "s_add_u32 m0, s65, 0\n"
        "s_mul_i32 s66, s9, 172032\n"
        "s_add_u32 s66, s66, 86016\n"
        "buffer_load_dwordx4 v224, s[36:39], s66 offen lds\n"
        "s_add_u32 m0, s65, 4096\n"
        "s_mul_i32 s66, s9, 172032\n"
        "s_add_u32 s66, s66, 86016\n"
        "buffer_load_dwordx4 v225, s[36:39], s66 offen lds\n"
        "s_add_u32 m0, s65, 8192\n"
        "s_mul_i32 s66, s9, 172032\n"
        "s_add_u32 s66, s66, 129024\n"
        "buffer_load_dwordx4 v224, s[36:39], s66 offen lds\n"
        "s_add_u32 m0, s65, 12288\n"
        "s_mul_i32 s66, s9, 172032\n"
        "s_add_u32 s66, s66, 129024\n"
        "buffer_load_dwordx4 v225, s[36:39], s66 offen lds\n"
        "s_mov_b32 s66, 0\n"
        "buffer_load_dwordx2 v[244:245], v232, s[60:63], s66 offen sc1\n"
        "s_waitcnt vmcnt(13) lgkmcnt(0)\n"
        "s_barrier\n"
        "v_mfma_f32_16x16x32_bf16 v[8:11], v[120:123], v[88:91], v[8:11]\n"
        "ds_read_b128 v[72:75], v250 offset:32768\n"
        "v_mfma_f32_16x16x32_bf16 v[24:27], v[124:127], v[88:91], v[24:27]\n"
        "ds_read_b64_tr_b16 v[104:105], v216 offset:0\n"
        "v_mfma_f32_16x16x32_bf16 v[40:43], v[128:131], v[88:91], v[40:43]\n"
        "ds_read_b64_tr_b16 v[106:107], v216 offset:2048\n"
        "v_mfma_f32_16x16x32_bf16 v[56:59], v[132:135], v[88:91], v[56:59]\n"
        "ds_read_b128 v[76:79], v252 offset:32768\n"
        "v_mfma_f32_16x16x32_bf16 v[12:15], v[120:123], v[92:95], v[12:15]\n"
        "ds_read_b64_tr_b16 v[108:109], v217 offset:0\n"
        "v_mfma_f32_16x16x32_bf16 v[28:31], v[124:127], v[92:95], v[28:31]\n"
        "ds_read_b64_tr_b16 v[110:111], v217 offset:2048\n"
        "v_mfma_f32_16x16x32_bf16 v[44:47], v[128:131], v[92:95], v[44:47]\n"
        "ds_read_b128 v[80:83], v250 offset:34816\n"
        "v_mfma_f32_16x16x32_bf16 v[60:63], v[132:135], v[92:95], v[60:63]\n"
        "ds_read_b64_tr_b16 v[112:113], v218 offset:0\n"
        "v_mfma_f32_16x16x32_bf16 v[16:19], v[120:123], v[96:99], v[16:19]\n"
        "ds_read_b64_tr_b16 v[114:115], v218 offset:2048\n"
        "v_mfma_f32_16x16x32_bf16 v[32:35], v[124:127], v[96:99], v[32:35]\n"
        "ds_read_b128 v[84:87], v252 offset:34816\n"
        "v_mfma_f32_16x16x32_bf16 v[48:51], v[128:131], v[96:99], v[48:51]\n"
        "ds_read_b64_tr_b16 v[116:117], v219 offset:0\n"
        "v_mfma_f32_16x16x32_bf16 v[64:67], v[132:135], v[96:99], v[64:67]\n"
        "ds_read_b64_tr_b16 v[118:119], v219 offset:2048\n"
        "v_mfma_f32_16x16x32_bf16 v[20:23], v[120:123], v[100:103], v[20:23]\n"
        "v_add_u32 v216, 32768, v220\n"
        "v_add_u32 v217, 32768, v221\n"
        "v_add_u32 v218, 32768, v222\n"
        "v_add_u32 v219, 32768, v223\n"
        "v_mfma_f32_16x16x32_bf16 v[36:39], v[124:127], v[100:103], v[36:39]\n"
        "v_mfma_f32_16x16x32_bf16 v[52:55], v[128:131], v[100:103], v[52:55]\n"
        "v_mfma_f32_16x16x32_bf16 v[68:71], v[132:135], v[100:103], v[68:71]\n"
        "s_add_u32 m0, s65, 49152\n"
        "s_mul_i32 s66, s10, 172032\n"
        "buffer_load_dwordx4 v224, s[36:39], s66 offen lds\n"
        "s_add_u32 m0, s65, 53248\n"
        "s_mul_i32 s66, s10, 172032\n"
        "buffer_load_dwordx4 v225, s[36:39], s66 offen lds\n"
        "s_add_u32 m0, s65, 57344\n"
        "s_mul_i32 s66, s10, 172032\n"
        "s_add_u32 s66, s66, 43008\n"
        "buffer_load_dwordx4 v224, s[36:39], s66 offen lds\n"
        "s_add_u32 m0, s65, 61440\n"
        "s_mul_i32 s66, s10, 172032\n"
        "s_add_u32 s66, s66, 43008\n"
        "buffer_load_dwordx4 v225, s[36:39], s66 offen lds\n"
        "s_add_u32 m0, s65, 65536\n"
        "s_mul_i32 s66, s10, 172032\n"
        "s_add_u32 s66, s66, 86016\n"
        "buffer_load_dwordx4 v224, s[36:39], s66 offen lds\n"
        "s_add_u32 m0, s65, 69632\n"
        "s_mul_i32 s66, s10, 172032\n"
        "s_add_u32 s66, s66, 86016\n"
        "buffer_load_dwordx4 v225, s[36:39], s66 offen lds\n"
        "s_waitcnt vmcnt(15) lgkmcnt(0)\n"
        "s_barrier\n"
        "v_mfma_f32_16x16x32_bf16 v[8:11], v[104:107], v[72:75], v[8:11]\n"
        "ds_read_b128 v[88:91], v251 offset:32768\n"
        "v_mfma_f32_16x16x32_bf16 v[24:27], v[108:111], v[72:75], v[24:27]\n"
        "ds_read_b64_tr_b16 v[120:121], v216 offset:0\n"
        "v_mfma_f32_16x16x32_bf16 v[40:43], v[112:115], v[72:75], v[40:43]\n"
        "ds_read_b64_tr_b16 v[122:123], v216 offset:2048\n"
        "v_mfma_f32_16x16x32_bf16 v[56:59], v[116:119], v[72:75], v[56:59]\n"
        "ds_read_b128 v[92:95], v253 offset:32768\n"
        "v_mfma_f32_16x16x32_bf16 v[12:15], v[104:107], v[76:79], v[12:15]\n"
        "ds_read_b64_tr_b16 v[124:125], v217 offset:0\n"
        "v_mfma_f32_16x16x32_bf16 v[28:31], v[108:111], v[76:79], v[28:31]\n"
        "ds_read_b64_tr_b16 v[126:127], v217 offset:2048\n"
        "v_mfma_f32_16x16x32_bf16 v[44:47], v[112:115], v[76:79], v[44:47]\n"
        "ds_read_b128 v[96:99], v251 offset:34816\n"
        "v_mfma_f32_16x16x32_bf16 v[60:63], v[116:119], v[76:79], v[60:63]\n"
        "ds_read_b64_tr_b16 v[128:129], v218 offset:0\n"
        "v_mfma_f32_16x16x32_bf16 v[16:19], v[104:107], v[80:83], v[16:19]\n"
        "ds_read_b64_tr_b16 v[130:131], v218 offset:2048\n"
        "v_mfma_f32_16x16x32_bf16 v[32:35], v[108:111], v[80:83], v[32:35]\n"
        "ds_read_b128 v[100:103], v253 offset:34816\n"
        "v_mfma_f32_16x16x32_bf16 v[48:51], v[112:115], v[80:83], v[48:51]\n"
        "ds_read_b64_tr_b16 v[132:133], v219 offset:0\n"
        "v_mfma_f32_16x16x32_bf16 v[64:67], v[116:119], v[80:83], v[64:67]\n"
        "ds_read_b64_tr_b16 v[134:135], v219 offset:2048\n"
        "v_mfma_f32_16x16x32_bf16 v[20:23], v[104:107], v[84:87], v[20:23]\n"
        "v_add_u32 v216, 114688, v220\n"
        "v_add_u32 v217, 114688, v221\n"
        "v_add_u32 v218, 114688, v222\n"
        "v_add_u32 v219, 114688, v223\n"
        "v_mfma_f32_16x16x32_bf16 v[36:39], v[108:111], v[84:87], v[36:39]\n"
        "v_mfma_f32_16x16x32_bf16 v[52:55], v[112:115], v[84:87], v[52:55]\n"
        "v_mfma_f32_16x16x32_bf16 v[68:71], v[116:119], v[84:87], v[68:71]\n"
        "s_waitcnt vmcnt(6)\n"
        "v_cmp_ne_u64 vcc, s[70:71], v[244:245]\n"
        "s_cbranch_vccz Lret16\n"
        "s_mov_b32 s79, 400000\n"
        "s_mov_b32 s66, 0\n"
        "Lspin16:\n"
        "s_sleep 2\n"
        "buffer_load_dwordx2 v[244:245], v232, s[60:63], s66 offen sc1\n"
        "s_waitcnt vmcnt(0)\n"
        "v_cmp_ne_u64 vcc, s[70:71], v[244:245]\n"
        "s_cbranch_vccz Lret16\n"
        "s_sub_u32 s79, s79, 1\n"
        "s_cmp_lg_u32 s79, 0\n"
        "s_cbranch_scc1 Lspin16\n"
        "Lret16:\n"
        "s_add_u32 m0, s65, 73728\n"
        "s_mul_i32 s66, s10, 172032\n"
        "s_add_u32 s66, s66, 129024\n"
        "buffer_load_dwordx4 v224, s[36:39], s66 offen lds\n"
        "s_add_u32 m0, s65, 77824\n"
        "s_mul_i32 s66, s10, 172032\n"
        "s_add_u32 s66, s66, 129024\n"
        "buffer_load_dwordx4 v225, s[36:39], s66 offen lds\n"
        "buffer_load_dwordx2 v[136:137], v228, s[60:63], s73 offen offset:8 sc1\n"
        "buffer_load_dwordx2 v[138:139], v229, s[60:63], s73 offen offset:8 sc1\n"
        "s_mov_b32 s66, 28672\n"
        "buffer_load_dwordx4 v[140:143], v228, s[60:63], s66 offen offset:0 sc1\n"
        "s_mov_b32 s66, 28672\n"
        "buffer_load_dwordx4 v[144:147], v229, s[60:63], s66 offen offset:0 sc1\n"
        "s_mov_b32 s66, 57344\n"
        "buffer_load_dwordx4 v[148:151], v228, s[60:63], s66 offen offset:0 sc1\n"
        "s_waitcnt lgkmcnt(0)\n"
        "s_barrier\n"
        "v_mfma_f32_16x16x32_bf16 v[8:11], v[120:123], v[88:91], v[8:11]\n"
        "ds_read_b128 v[72:75], v250 offset:40960\n"
        "v_mfma_f32_16x16x32_bf16 v[24:27], v[124:127], v[88:91], v[24:27]\n"
        "ds_read_b64_tr_b16 v[104:105], v216 offset:0\n"
        "v_mfma_f32_16x16x32_bf16 v[40:43], v[128:131], v[88:91], v[40:43]\n"
        "ds_read_b64_tr_b16 v[106:107], v216 offset:2048\n"
        "v_mfma_f32_16x16x32_bf16 v[56:59], v[132:135], v[88:91], v[56:59]\n"
        "ds_read_b128 v[76:79], v252 offset:40960\n"
        "v_mfma_f32_16x16x32_bf16 v[12:15], v[120:123], v[92:95], v[12:15]\n"
        "ds_read_b64_tr_b16 v[108:109], v217 offset:0\n"
        "v_mfma_f32_16x16x32_bf16 v[28:31], v[124:127], v[92:95], v[28:31]\n"
        "ds_read_b64_tr_b16 v[110:111], v217 offset:2048\n"
        "v_mfma_f32_16x16x32_bf16 v[44:47], v[128:131], v[92:95], v[44:47]\n"
        "ds_read_b128 v[80:83], v250 offset:43008\n"
        "v_mfma_f32_16x16x32_bf16 v[60:63], v[132:135], v[92:95], v[60:63]\n"
        "ds_read_b64_tr_b16 v[112:113], v218 offset:0\n"
        "v_mfma_f32_16x16x32_bf16 v[16:19], v[120:123], v[96:99], v[16:19]\n"
        "ds_read_b64_tr_b16 v[114:115], v218 offset:2048\n"
        "v_mfma_f32_16x16x32_bf16 v[32:35], v[124:127], v[96:99], v[32:35]\n"
        "ds_read_b128 v[84:87], v252 offset:43008\n"
        "v_mfma_f32_16x16x32_bf16 v[48:51], v[128:131], v[96:99], v[48:51]\n"
        "ds_read_b64_tr_b16 v[116:117], v219 offset:0\n"
        "v_mfma_f32_16x16x32_bf16 v[64:67], v[132:135], v[96:99], v[64:67]\n"
        "ds_read_b64_tr_b16 v[118:119], v219 offset:2048\n"
        "v_mfma_f32_16x16x32_bf16 v[20:23], v[120:123], v[100:103], v[20:23]\n"
        "v_add_u32 v216, 0, v220\n"
        "v_add_u32 v217, 0, v221\n"
        "v_add_u32 v218, 0, v222\n"
        "v_add_u32 v219, 0, v223\n"
        "v_mfma_f32_16x16x32_bf16 v[36:39], v[124:127], v[100:103], v[36:39]\n"
        "v_mfma_f32_16x16x32_bf16 v[52:55], v[128:131], v[100:103], v[52:55]\n"
        "v_mfma_f32_16x16x32_bf16 v[68:71], v[132:135], v[100:103], v[68:71]\n"
        "s_mov_b32 s66, 57344\n"
        "buffer_load_dwordx4 v[152:155], v229, s[60:63], s66 offen offset:0 sc1\n"
        "buffer_load_dwordx2 v[176:177], v230, s[60:63], s73 offen offset:8 sc1\n"
        "s_mov_b32 s66, 28672\n"
        "buffer_load_dwordx4 v[178:181], v230, s[60:63], s66 offen offset:0 sc1\n"
        "s_mov_b32 s66, 57344\n"
        "buffer_load_dwordx4 v[182:185], v230, s[60:63], s66 offen offset:0 sc1\n"
        "buffer_load_dwordx2 v[186:187], v226, s[60:63], s73 offen offset:264 sc1\n"
        "s_waitcnt lgkmcnt(0)\n"
        "s_barrier\n"
        "v_mfma_f32_16x16x32_bf16 v[8:11], v[104:107], v[72:75], v[8:11]\n"
        "ds_read_b128 v[88:91], v251 offset:40960\n"
        "v_mfma_f32_16x16x32_bf16 v[24:27], v[108:111], v[72:75], v[24:27]\n"
        "ds_read_b64_tr_b16 v[120:121], v216 offset:0\n"
        "v_mfma_f32_16x16x32_bf16 v[40:43], v[112:115], v[72:75], v[40:43]\n"
        "ds_read_b64_tr_b16 v[122:123], v216 offset:2048\n"
        "v_mfma_f32_16x16x32_bf16 v[56:59], v[116:119], v[72:75], v[56:59]\n"
        "ds_read_b128 v[92:95], v253 offset:40960\n"
        "v_mfma_f32_16x16x32_bf16 v[12:15], v[104:107], v[76:79], v[12:15]\n"
        "ds_read_b64_tr_b16 v[124:125], v217 offset:0\n"
        "v_mfma_f32_16x16x32_bf16 v[28:31], v[108:111], v[76:79], v[28:31]\n"
        "ds_read_b64_tr_b16 v[126:127], v217 offset:2048\n"
        "v_mfma_f32_16x16x32_bf16 v[44:47], v[112:115], v[76:79], v[44:47]\n"
        "ds_read_b128 v[96:99], v251 offset:43008\n"
        "v_mfma_f32_16x16x32_bf16 v[60:63], v[116:119], v[76:79], v[60:63]\n"
        "ds_read_b64_tr_b16 v[128:129], v218 offset:0\n"
        "v_mfma_f32_16x16x32_bf16 v[16:19], v[104:107], v[80:83], v[16:19]\n"
        "ds_read_b64_tr_b16 v[130:131], v218 offset:2048\n"
        "v_mfma_f32_16x16x32_bf16 v[32:35], v[108:111], v[80:83], v[32:35]\n"
        "ds_read_b128 v[100:103], v253 offset:43008\n"
        "v_mfma_f32_16x16x32_bf16 v[48:51], v[112:115], v[80:83], v[48:51]\n"
        "ds_read_b64_tr_b16 v[132:133], v219 offset:0\n"
        "v_mfma_f32_16x16x32_bf16 v[64:67], v[116:119], v[80:83], v[64:67]\n"
        "ds_read_b64_tr_b16 v[134:135], v219 offset:2048\n"
        "v_mfma_f32_16x16x32_bf16 v[20:23], v[104:107], v[84:87], v[20:23]\n"
        "v_add_u32 v216, 49152, v220\n"
        "v_add_u32 v217, 49152, v221\n"
        "v_add_u32 v218, 49152, v222\n"
        "v_add_u32 v219, 49152, v223\n"
        "v_mfma_f32_16x16x32_bf16 v[36:39], v[108:111], v[84:87], v[36:39]\n"
        "v_mfma_f32_16x16x32_bf16 v[52:55], v[112:115], v[84:87], v[52:55]\n"
        "v_mfma_f32_16x16x32_bf16 v[68:71], v[116:119], v[84:87], v[68:71]\n"
        "s_mov_b32 s66, 28672\n"
        "buffer_load_dwordx4 v[188:191], v226, s[60:63], s66 offen offset:256 sc1\n"
        "s_mov_b32 s66, 57344\n"
        "buffer_load_dwordx4 v[192:195], v226, s[60:63], s66 offen offset:256 sc1\n"
        "buffer_load_dwordx2 v[156:157], v228, s[60:63], s73 offen offset:1800 sc1\n"
        "buffer_load_dwordx2 v[158:159], v229, s[60:63], s73 offen offset:1800 sc1\n"
        "s_mov_b32 s66, 28672\n"
        "buffer_load_dwordx4 v[160:163], v228, s[60:63], s66 offen offset:1792 sc1\n"
        "s_waitcnt vmcnt(19) lgkmcnt(0)\n"
        "s_barrier\n"
        "v_mfma_f32_16x16x32_bf16 v[8:11], v[120:123], v[88:91], v[8:11]\n"
        "ds_read_b128 v[72:75], v250 offset:49152\n"
        "v_mfma_f32_16x16x32_bf16 v[24:27], v[124:127], v[88:91], v[24:27]\n"
        "ds_read_b64_tr_b16 v[104:105], v216 offset:0\n"
        "v_mfma_f32_16x16x32_bf16 v[40:43], v[128:131], v[88:91], v[40:43]\n"
        "ds_read_b64_tr_b16 v[106:107], v216 offset:2048\n"
        "v_mfma_f32_16x16x32_bf16 v[56:59], v[132:135], v[88:91], v[56:59]\n"
        "ds_read_b128 v[76:79], v252 offset:49152\n"
        "v_mfma_f32_16x16x32_bf16 v[12:15], v[120:123], v[92:95], v[12:15]\n"
        "ds_read_b64_tr_b16 v[108:109], v217 offset:0\n"
        "v_mfma_f32_16x16x32_bf16 v[28:31], v[124:127], v[92:95], v[28:31]\n"
        "ds_read_b64_tr_b16 v[110:111], v217 offset:2048\n"
        "v_mfma_f32_16x16x32_bf16 v[44:47], v[128:131], v[92:95], v[44:47]\n"
        "ds_read_b128 v[80:83], v250 offset:51200\n"
        "v_mfma_f32_16x16x32_bf16 v[60:63], v[132:135], v[92:95], v[60:63]\n"
        "ds_read_b64_tr_b16 v[112:113], v218 offset:0\n"
        "v_mfma_f32_16x16x32_bf16 v[16:19], v[120:123], v[96:99], v[16:19]\n"
        "ds_read_b64_tr_b16 v[114:115], v218 offset:2048\n"
        "v_mfma_f32_16x16x32_bf16 v[32:35], v[124:127], v[96:99], v[32:35]\n"
        "ds_read_b128 v[84:87], v252 offset:51200\n"
        "v_mfma_f32_16x16x32_bf16 v[48:51], v[128:131], v[96:99], v[48:51]\n"
        "ds_read_b64_tr_b16 v[116:117], v219 offset:0\n"
        "v_mfma_f32_16x16x32_bf16 v[64:67], v[132:135], v[96:99], v[64:67]\n"
        "ds_read_b64_tr_b16 v[118:119], v219 offset:2048\n"
        "v_mfma_f32_16x16x32_bf16 v[20:23], v[120:123], v[100:103], v[20:23]\n"
        "v_add_u32 v216, 65536, v220\n"
        "v_add_u32 v217, 65536, v221\n"
        "v_add_u32 v218, 65536, v222\n"
        "v_add_u32 v219, 65536, v223\n"
        "v_mfma_f32_16x16x32_bf16 v[36:39], v[124:127], v[100:103], v[36:39]\n"
        "v_mfma_f32_16x16x32_bf16 v[52:55], v[128:131], v[100:103], v[52:55]\n"
        "v_mfma_f32_16x16x32_bf16 v[68:71], v[132:135], v[100:103], v[68:71]\n"
        "s_mov_b32 s66, 28672\n"
        "buffer_load_dwordx4 v[164:167], v229, s[60:63], s66 offen offset:1792 sc1\n"
        "s_mov_b32 s66, 57344\n"
        "buffer_load_dwordx4 v[168:171], v228, s[60:63], s66 offen offset:1792 sc1\n"
        "s_mov_b32 s66, 57344\n"
        "buffer_load_dwordx4 v[172:175], v229, s[60:63], s66 offen offset:1792 sc1\n"
        "buffer_load_dwordx2 v[196:197], v227, s[60:63], s73 offen offset:392 sc1\n"
        "s_mov_b32 s66, 28672\n"
        "buffer_load_dwordx4 v[198:201], v227, s[60:63], s66 offen offset:384 sc1\n"
        "s_waitcnt vmcnt(20) lgkmcnt(0)\n"
        "s_barrier\n"
        "v_mfma_f32_16x16x32_bf16 v[8:11], v[104:107], v[72:75], v[8:11]\n"
        "ds_read_b128 v[88:91], v251 offset:49152\n"
        "v_mfma_f32_16x16x32_bf16 v[24:27], v[108:111], v[72:75], v[24:27]\n"
        "ds_read_b64_tr_b16 v[120:121], v216 offset:0\n"
        "v_mfma_f32_16x16x32_bf16 v[40:43], v[112:115], v[72:75], v[40:43]\n"
        "ds_read_b64_tr_b16 v[122:123], v216 offset:2048\n"
        "v_mfma_f32_16x16x32_bf16 v[56:59], v[116:119], v[72:75], v[56:59]\n"
        "ds_read_b128 v[92:95], v253 offset:49152\n"
        "v_mfma_f32_16x16x32_bf16 v[12:15], v[104:107], v[76:79], v[12:15]\n"
        "ds_read_b64_tr_b16 v[124:125], v217 offset:0\n"
        "v_mfma_f32_16x16x32_bf16 v[28:31], v[108:111], v[76:79], v[28:31]\n"
        "ds_read_b64_tr_b16 v[126:127], v217 offset:2048\n"
        "v_mfma_f32_16x16x32_bf16 v[44:47], v[112:115], v[76:79], v[44:47]\n"
        "ds_read_b128 v[96:99], v251 offset:51200\n"
        "v_mfma_f32_16x16x32_bf16 v[60:63], v[116:119], v[76:79], v[60:63]\n"
        "ds_read_b64_tr_b16 v[128:129], v218 offset:0\n"
        "v_mfma_f32_16x16x32_bf16 v[16:19], v[104:107], v[80:83], v[16:19]\n"
        "ds_read_b64_tr_b16 v[130:131], v218 offset:2048\n"
        "v_mfma_f32_16x16x32_bf16 v[32:35], v[108:111], v[80:83], v[32:35]\n"
        "ds_read_b128 v[100:103], v253 offset:51200\n"
        "v_mfma_f32_16x16x32_bf16 v[48:51], v[112:115], v[80:83], v[48:51]\n"
        "ds_read_b64_tr_b16 v[132:133], v219 offset:0\n"
        "v_mfma_f32_16x16x32_bf16 v[64:67], v[116:119], v[80:83], v[64:67]\n"
        "ds_read_b64_tr_b16 v[134:135], v219 offset:2048\n"
        "v_mfma_f32_16x16x32_bf16 v[20:23], v[104:107], v[84:87], v[20:23]\n"
        "v_mfma_f32_16x16x32_bf16 v[36:39], v[108:111], v[84:87], v[36:39]\n"
        "v_mfma_f32_16x16x32_bf16 v[52:55], v[112:115], v[84:87], v[52:55]\n"
        "v_mfma_f32_16x16x32_bf16 v[68:71], v[116:119], v[84:87], v[68:71]\n"
        "s_mov_b32 s66, 57344\n"
        "buffer_load_dwordx4 v[202:205], v227, s[60:63], s66 offen offset:384 sc1\n"
        "buffer_load_dwordx2 v[206:207], v230, s[60:63], s73 offen offset:648 sc1\n"
        "s_mov_b32 s66, 28672\n"
        "buffer_load_dwordx4 v[208:211], v230, s[60:63], s66 offen offset:640 sc1\n"
        "s_mov_b32 s66, 57344\n"
        "buffer_load_dwordx4 v[212:215], v230, s[60:63], s66 offen offset:640 sc1\n"
        "s_waitcnt lgkmcnt(9)\n"
        "v_mfma_f32_16x16x32_bf16 v[8:11], v[120:123], v[88:91], v[8:11]\n"
        "s_waitcnt lgkmcnt(6)\n"
        "v_mfma_f32_16x16x32_bf16 v[24:27], v[124:127], v[88:91], v[24:27]\n"
        "s_waitcnt lgkmcnt(3)\n"
        "v_mfma_f32_16x16x32_bf16 v[40:43], v[128:131], v[88:91], v[40:43]\n"
        "s_waitcnt lgkmcnt(0)\n"
        "v_mfma_f32_16x16x32_bf16 v[56:59], v[132:135], v[88:91], v[56:59]\n"
        "v_mfma_f32_16x16x32_bf16 v[12:15], v[120:123], v[92:95], v[12:15]\n"
        "v_mfma_f32_16x16x32_bf16 v[28:31], v[124:127], v[92:95], v[28:31]\n"
        "v_mfma_f32_16x16x32_bf16 v[44:47], v[128:131], v[92:95], v[44:47]\n"
        "v_mfma_f32_16x16x32_bf16 v[60:63], v[132:135], v[92:95], v[60:63]\n"
        "v_mfma_f32_16x16x32_bf16 v[16:19], v[120:123], v[96:99], v[16:19]\n"
        "v_mfma_f32_16x16x32_bf16 v[32:35], v[124:127], v[96:99], v[32:35]\n"
        "v_mfma_f32_16x16x32_bf16 v[48:51], v[128:131], v[96:99], v[48:51]\n"
        "v_mfma_f32_16x16x32_bf16 v[64:67], v[132:135], v[96:99], v[64:67]\n"
        "v_mfma_f32_16x16x32_bf16 v[20:23], v[120:123], v[100:103], v[20:23]\n"
        "v_mfma_f32_16x16x32_bf16 v[36:39], v[124:127], v[100:103], v[36:39]\n"
        "v_mfma_f32_16x16x32_bf16 v[52:55], v[128:131], v[100:103], v[52:55]\n"
        "v_mfma_f32_16x16x32_bf16 v[68:71], v[132:135], v[100:103], v[68:71]\n"
        "s_cmp_eq_u32 s80, 0\n"
        "s_cselect_b64 vcc, -1, 0\n"
        "v_mov_b32 v233, 0xcf9db2f6\n"
        "s_mov_b32 s69, 0x3e8b97f6\n"
        "s_mov_b32 s68, 0xbe8b97f6\n"
        "v_mov_b32 v216, s77\n"
        "v_mov_b32 v217, s77\n"
        "v_mov_b32 v218, s77\n"
        "v_mov_b32 v219, s77\n"
        "v_cvt_pk_bf16_f32 v72, v8, v9\n"
        "v_cvt_pk_bf16_f32 v73, v10, v11\n"
        "v_cvt_pk_bf16_f32 v74, v24, v25\n"
        "v_cvt_pk_bf16_f32 v75, v26, v27\n"
        "v_cvt_pk_bf16_f32 v76, v12, v13\n"
        "v_cvt_pk_bf16_f32 v77, v14, v15\n"
        "v_cvt_pk_bf16_f32 v78, v28, v29\n"
        "v_cvt_pk_bf16_f32 v79, v30, v31\n"
        "v_cvt_pk_bf16_f32 v80, v16, v17\n"
        "v_cvt_pk_bf16_f32 v81, v18, v19\n"
        "v_cvt_pk_bf16_f32 v82, v32, v33\n"
        "v_cvt_pk_bf16_f32 v83, v34, v35\n"
        "v_cvt_pk_bf16_f32 v84, v20, v21\n"
        "v_cvt_pk_bf16_f32 v85, v22, v23\n"
        "v_cvt_pk_bf16_f32 v86, v36, v37\n"
        "v_cvt_pk_bf16_f32 v87, v38, v39\n"
        "s_waitcnt vmcnt(20)\n"
        "v_swap_b32 v142, v144\n"
        "v_swap_b32 v143, v145\n"
        "s_waitcnt vmcnt(18)\n"
        "v_swap_b32 v150, v152\n"
        "v_swap_b32 v151, v153\n"
        "v_mfma_f32_16x16x32_bf16 v[8:11], v[136:139], v[72:75], 0\n"
        "v_mfma_f32_16x16x32_bf16 v[12:15], v[140:143], v[72:75], 0\n"
        "v_mfma_f32_16x16x32_bf16 v[16:19], v[140:143], v[76:79], 0\n"
        "v_mfma_f32_16x16x32_bf16 v[20:23], v[144:147], v[76:79], 0\n"
        "v_mfma_f32_16x16x32_bf16 v[24:27], v[144:147], v[80:83], 0\n"
        "v_mfma_f32_16x16x32_bf16 v[28:31], v[148:151], v[80:83], 0\n"
        "v_mfma_f32_16x16x32_bf16 v[32:35], v[148:151], v[84:87], 0\n"
        "v_mfma_f32_16x16x32_bf16 v[36:39], v[152:155], v[84:87], 0\n"
        "s_nop 0\n"
        "v_cndmask_b32 v8, v8, v9, s[82:83]\n"
        "s_nop 0\n"
        "v_cndmask_b32 v16, v16, v17, s[82:83]\n"
        "s_nop 0\n"
        "v_cndmask_b32 v24, v24, v25, s[82:83]\n"
        "s_nop 0\n"
        "v_cndmask_b32 v32, v32, v33, s[82:83]\n"
        "v_cndmask_b32 v10, v10, v11, s[82:83]\n"
        "v_cndmask_b32 v18, v18, v19, s[82:83]\n"
        "v_cndmask_b32 v26, v26, v27, s[82:83]\n"
        "v_cndmask_b32 v34, v34, v35, s[82:83]\n"
        "v_cndmask_b32 v12, v12, v13, s[82:83]\n"
        "v_cndmask_b32 v20, v20, v21, s[82:83]\n"
        "v_cndmask_b32 v28, v28, v29, s[82:83]\n"
        "v_cndmask_b32 v36, v36, v37, s[82:83]\n"
        "v_cndmask_b32 v14, v14, v15, s[82:83]\n"
        "v_cndmask_b32 v22, v22, v23, s[82:83]\n"
        "v_cndmask_b32 v30, v30, v31, s[82:83]\n"
        "v_cndmask_b32 v38, v38, v39, s[82:83]\n"
        "v_cndmask_b32 v8, v8, v233, vcc\n"
        "v_cndmask_b32 v10, v10, v233, vcc\n"
        "v_cndmask_b32 v8, v8, v12, s[84:85]\n"
        "v_cndmask_b32 v16, v16, v20, s[84:85]\n"
        "v_cndmask_b32 v24, v24, v28, s[84:85]\n"
        "v_cndmask_b32 v32, v32, v36, s[84:85]\n"
        "v_cndmask_b32 v9, v10, v14, s[86:87]\n"
        "v_cndmask_b32 v17, v18, v22, s[86:87]\n"
        "v_cndmask_b32 v25, v26, v30, s[86:87]\n"
        "v_cndmask_b32 v33, v34, v38, s[86:87]\n"
        "v_max_f32 v10, v8, v9\n"
        "v_max_f32 v18, v16, v17\n"
        "v_max_f32 v26, v24, v25\n"
        "v_max_f32 v34, v32, v33\n"
        "v_permlane32_swap_b32 v10, v18\n"
        "s_nop 0\n"
        "v_permlane32_swap_b32 v26, v34\n"
        "v_max_f32 v10, v10, v18\n"
        "v_max_f32 v26, v26, v34\n"
        "s_nop 1\n"
        "v_permlane16_swap_b32 v10, v26\n"
        "v_max_f32 v10, v10, v26\n"
        "v_mul_f32 v26, s68, v10\n"
        "v_mul_f32 v10, s68, v10\n"
        "s_nop 1\n"
        "v_permlane16_swap_b32 v10, v26\n"
        "v_mov_b32 v18, v10\n"
        "v_mov_b32 v34, v26\n"
        "s_nop 0\n"
        "v_permlane32_swap_b32 v10, v18\n"
        "v_permlane32_swap_b32 v26, v34\n"
        "v_fma_f32 v8, v8, s69, v10\n"
        "v_fma_f32 v16, v16, s69, v18\n"
        "v_fma_f32 v24, v24, s69, v26\n"
        "v_fma_f32 v32, v32, s69, v34\n"
        "v_fma_f32 v9, v9, s69, v10\n"
        "v_fma_f32 v17, v17, s69, v18\n"
        "v_fma_f32 v25, v25, s69, v26\n"
        "v_fma_f32 v33, v33, s69, v34\n"
        "v_exp_f32 v8, v8\n"
        "v_exp_f32 v16, v16\n"
        "v_exp_f32 v24, v24\n"
        "v_exp_f32 v32, v32\n"
        "v_exp_f32 v9, v9\n"
        "v_exp_f32 v17, v17\n"
        "v_exp_f32 v25, v25\n"
        "v_exp_f32 v33, v33\n"
        "v_cvt_pk_bf16_f32 v8, v8, v9\n"
        "v_cvt_pk_bf16_f32 v16, v16, v17\n"
        "v_cvt_pk_bf16_f32 v24, v24, v25\n"
        "v_cvt_pk_bf16_f32 v32, v32, v33\n"
        "v_perm_b32 v104, v8, v8, v236\n"
        "s_nop 0\n"
        "s_cbranch_execz Lback\n"
        "v_perm_b32 v108, v16, v16, v236\n"
        "v_perm_b32 v112, v24, v24, v236\n"
        "v_perm_b32 v116, v32, v32, v236\n"
        "v_perm_b32 v105, v8, v8, v237\n"
        "v_perm_b32 v109, v16, v16, v237\n"
        "v_perm_b32 v113, v24, v24, v237\n"
        "v_perm_b32 v117, v32, v32, v237\n"
        "v_perm_b32 v106, v8, v8, v238\n"
        "v_perm_b32 v110, v16, v16, v238\n"
        "v_perm_b32 v114, v24, v24, v238\n"
        "v_perm_b32 v118, v32, v32, v238\n"
        "v_perm_b32 v107, v8, v8, v239\n"
        "v_perm_b32 v111, v16, v16, v239\n"
        "v_perm_b32 v115, v24, v24, v239\n"
        "v_perm_b32 v119, v32, v32, v239\n"
        "v_mfma_f32_16x16x32_bf16 v[72:75], v[216:219], v[104:107], 0\n"
        "v_mfma_f32_16x16x32_bf16 v[76:79], v[216:219], v[108:111], 0\n"
        "v_mfma_f32_16x16x32_bf16 v[80:83], v[216:219], v[112:115], 0\n"
        "v_mfma_f32_16x16x32_bf16 v[84:87], v[216:219], v[116:119], 0\n"
        "s_waitcnt vmcnt(16)\n"
        "v_mfma_f32_16x16x32_bf16 v[8:11], v[176:179], v[104:107], 0\n"
        "s_waitcnt vmcnt(13)\n"
        "v_mfma_f32_16x16x32_bf16 v[12:15], v[186:189], v[104:107], 0\n"
        "v_mfma_f32_16x16x32_bf16 v[16:19], v[178:181], v[108:111], 0\n"
        "v_mfma_f32_16x16x32_bf16 v[20:23], v[188:191], v[108:111], 0\n"
        "v_mfma_f32_16x16x32_bf16 v[24:27], v[180:183], v[112:115], 0\n"
        "s_waitcnt vmcnt(12)\n"
        "v_mfma_f32_16x16x32_bf16 v[28:31], v[190:193], v[112:115], 0\n"
        "v_mfma_f32_16x16x32_bf16 v[32:35], v[182:185], v[116:119], 0\n"
        "v_mfma_f32_16x16x32_bf16 v[36:39], v[192:195], v[116:119], 0\n"
        "v_rcp_f32 v72, v72\n"
        "v_rcp_f32 v76, v76\n"
        "v_rcp_f32 v80, v80\n"
        "v_rcp_f32 v84, v84\n"
        "v_mov_b32 v73, v72\n"
        "v_mov_b32 v77, v76\n"
        "v_mov_b32 v81, v80\n"
        "v_mov_b32 v85, v84\n"
        "v_pk_mul_f32 v[8:9], v[8:9], v[72:73]\n"
        "v_pk_mul_f32 v[10:11], v[10:11], v[72:73]\n"
        "v_pk_mul_f32 v[12:13], v[12:13], v[72:73]\n"
        "v_pk_mul_f32 v[14:15], v[14:15], v[72:73]\n"
        "v_cvt_pk_bf16_f32 v72, v8, v9\n"
        "v_cvt_pk_bf16_f32 v73, v10, v11\n"
        "v_cvt_pk_bf16_f32 v74, v12, v13\n"
        "v_cvt_pk_bf16_f32 v75, v14, v15\n"
        "s_mov_b32 s66, 0\n"
        "buffer_store_dwordx4 v[72:75], v231, s[60:63], s66 offen offset:0 sc1\n"
        "v_pk_mul_f32 v[16:17], v[16:17], v[76:77]\n"
        "v_pk_mul_f32 v[18:19], v[18:19], v[76:77]\n"
        "v_pk_mul_f32 v[20:21], v[20:21], v[76:77]\n"
        "v_pk_mul_f32 v[22:23], v[22:23], v[76:77]\n"
        "v_cvt_pk_bf16_f32 v76, v16, v17\n"
        "v_cvt_pk_bf16_f32 v77, v18, v19\n"
        "v_cvt_pk_bf16_f32 v78, v20, v21\n"
        "v_cvt_pk_bf16_f32 v79, v22, v23\n"
        "buffer_store_dwordx4 v[76:79], v231, s[60:63], s66 offen offset:1024 sc1\n"
        "v_pk_mul_f32 v[24:25], v[24:25], v[80:81]\n"
        "v_pk_mul_f32 v[26:27], v[26:27], v[80:81]\n"
        "v_pk_mul_f32 v[28:29], v[28:29], v[80:81]\n"
        "v_pk_mul_f32 v[30:31], v[30:31], v[80:81]\n"
        "v_cvt_pk_bf16_f32 v80, v24, v25\n"
        "v_cvt_pk_bf16_f32 v81, v26, v27\n"
        "v_cvt_pk_bf16_f32 v82, v28, v29\n"
        "v_cvt_pk_bf16_f32 v83, v30, v31\n"
        "buffer_store_dwordx4 v[80:83], v231, s[60:63], s66 offen offset:2048 sc1\n"
        "v_pk_mul_f32 v[32:33], v[32:33], v[84:85]\n"
        "v_pk_mul_f32 v[34:35], v[34:35], v[84:85]\n"
        "v_pk_mul_f32 v[36:37], v[36:37], v[84:85]\n"
        "v_pk_mul_f32 v[38:39], v[38:39], v[84:85]\n"
        "v_cvt_pk_bf16_f32 v84, v32, v33\n"
        "v_cvt_pk_bf16_f32 v85, v34, v35\n"
        "v_cvt_pk_bf16_f32 v86, v36, v37\n"
        "v_cvt_pk_bf16_f32 v87, v38, v39\n"
        "buffer_store_dwordx4 v[84:87], v231, s[60:63], s66 offen offset:3072 sc1\n"
        "v_cvt_pk_bf16_f32 v88, v40, v41\n"
        "v_cvt_pk_bf16_f32 v89, v42, v43\n"
        "v_cvt_pk_bf16_f32 v90, v56, v57\n"
        "v_cvt_pk_bf16_f32 v91, v58, v59\n"
        "v_cvt_pk_bf16_f32 v92, v44, v45\n"
        "v_cvt_pk_bf16_f32 v93, v46, v47\n"
        "v_cvt_pk_bf16_f32 v94, v60, v61\n"
        "v_cvt_pk_bf16_f32 v95, v62, v63\n"
        "v_cvt_pk_bf16_f32 v96, v48, v49\n"
        "v_cvt_pk_bf16_f32 v97, v50, v51\n"
        "v_cvt_pk_bf16_f32 v98, v64, v65\n"
        "v_cvt_pk_bf16_f32 v99, v66, v67\n"
        "v_cvt_pk_bf16_f32 v100, v52, v53\n"
        "v_cvt_pk_bf16_f32 v101, v54, v55\n"
        "v_cvt_pk_bf16_f32 v102, v68, v69\n"
        "v_cvt_pk_bf16_f32 v103, v70, v71\n"
        "s_waitcnt vmcnt(12)\n"
        "v_swap_b32 v162, v164\n"
        "v_swap_b32 v163, v165\n"
        "s_waitcnt vmcnt(10)\n"
        "v_swap_b32 v170, v172\n"
        "v_swap_b32 v171, v173\n"
        "v_mfma_f32_16x16x32_bf16 v[40:43], v[156:159], v[88:91], 0\n"
        "v_mfma_f32_16x16x32_bf16 v[44:47], v[160:163], v[88:91], 0\n"
        "v_mfma_f32_16x16x32_bf16 v[48:51], v[160:163], v[92:95], 0\n"
        "v_mfma_f32_16x16x32_bf16 v[52:55], v[164:167], v[92:95], 0\n"
        "v_mfma_f32_16x16x32_bf16 v[56:59], v[164:167], v[96:99], 0\n"
        "v_mfma_f32_16x16x32_bf16 v[60:63], v[168:171], v[96:99], 0\n"
        "v_mfma_f32_16x16x32_bf16 v[64:67], v[168:171], v[100:103], 0\n"
        "v_mfma_f32_16x16x32_bf16 v[68:71], v[172:175], v[100:103], 0\n"
        "s_nop 0\n"
        "v_cndmask_b32 v40, v40, v41, s[82:83]\n"
        "s_nop 0\n"
        "v_cndmask_b32 v48, v48, v49, s[82:83]\n"
        "s_nop 0\n"
        "v_cndmask_b32 v56, v56, v57, s[82:83]\n"
        "s_nop 0\n"
        "v_cndmask_b32 v64, v64, v65, s[82:83]\n"
        "v_cndmask_b32 v42, v42, v43, s[82:83]\n"
        "v_cndmask_b32 v50, v50, v51, s[82:83]\n"
        "v_cndmask_b32 v58, v58, v59, s[82:83]\n"
        "v_cndmask_b32 v66, v66, v67, s[82:83]\n"
        "v_cndmask_b32 v44, v44, v45, s[82:83]\n"
        "v_cndmask_b32 v52, v52, v53, s[82:83]\n"
        "v_cndmask_b32 v60, v60, v61, s[82:83]\n"
        "v_cndmask_b32 v68, v68, v69, s[82:83]\n"
        "v_cndmask_b32 v46, v46, v47, s[82:83]\n"
        "v_cndmask_b32 v54, v54, v55, s[82:83]\n"
        "v_cndmask_b32 v62, v62, v63, s[82:83]\n"
        "v_cndmask_b32 v70, v70, v71, s[82:83]\n"
        "v_cndmask_b32 v40, v40, v233, vcc\n"
        "v_cndmask_b32 v42, v42, v233, vcc\n"
        "v_cndmask_b32 v40, v40, v44, s[84:85]\n"
        "v_cndmask_b32 v48, v48, v52, s[84:85]\n"
        "v_cndmask_b32 v56, v56, v60, s[84:85]\n"
        "v_cndmask_b32 v64, v64, v68, s[84:85]\n"
        "v_cndmask_b32 v41, v42, v46, s[86:87]\n"
        "v_cndmask_b32 v49, v50, v54, s[86:87]\n"
        "v_cndmask_b32 v57, v58, v62, s[86:87]\n"
        "v_cndmask_b32 v65, v66, v70, s[86:87]\n"
        "v_max_f32 v42, v40, v41\n"
        "v_max_f32 v50, v48, v49\n"
        "v_max_f32 v58, v56, v57\n"
        "v_max_f32 v66, v64, v65\n"
        "v_permlane32_swap_b32 v42, v50\n"
        "s_nop 0\n"
        "v_permlane32_swap_b32 v58, v66\n"
        "v_max_f32 v42, v42, v50\n"
        "v_max_f32 v58, v58, v66\n"
        "s_nop 1\n"
        "v_permlane16_swap_b32 v42, v58\n"
        "v_max_f32 v42, v42, v58\n"
        "v_mul_f32 v58, s68, v42\n"
        "v_mul_f32 v42, s68, v42\n"
        "s_nop 1\n"
        "v_permlane16_swap_b32 v42, v58\n"
        "v_mov_b32 v50, v42\n"
        "v_mov_b32 v66, v58\n"
        "s_nop 0\n"
        "v_permlane32_swap_b32 v42, v50\n"
        "v_permlane32_swap_b32 v58, v66\n"
        "v_fma_f32 v40, v40, s69, v42\n"
        "v_fma_f32 v48, v48, s69, v50\n"
        "v_fma_f32 v56, v56, s69, v58\n"
        "v_fma_f32 v64, v64, s69, v66\n"
        "v_fma_f32 v41, v41, s69, v42\n"
        "v_fma_f32 v49, v49, s69, v50\n"
        "v_fma_f32 v57, v57, s69, v58\n"
        "v_fma_f32 v65, v65, s69, v66\n"
        "v_exp_f32 v40, v40\n"
        "v_exp_f32 v48, v48\n"
        "v_exp_f32 v56, v56\n"
        "v_exp_f32 v64, v64\n"
        "v_exp_f32 v41, v41\n"
        "v_exp_f32 v49, v49\n"
        "v_exp_f32 v57, v57\n"
        "v_exp_f32 v65, v65\n"
        "v_cvt_pk_bf16_f32 v40, v40, v41\n"
        "v_cvt_pk_bf16_f32 v48, v48, v49\n"
        "v_cvt_pk_bf16_f32 v56, v56, v57\n"
        "v_cvt_pk_bf16_f32 v64, v64, v65\n"
        "v_perm_b32 v120, v40, v40, v236\n"
        "v_perm_b32 v124, v48, v48, v236\n"
        "v_perm_b32 v128, v56, v56, v236\n"
        "v_perm_b32 v132, v64, v64, v236\n"
        "v_perm_b32 v121, v40, v40, v237\n"
        "v_perm_b32 v125, v48, v48, v237\n"
        "v_perm_b32 v129, v56, v56, v237\n"
        "v_perm_b32 v133, v64, v64, v237\n"
        "v_perm_b32 v122, v40, v40, v238\n"
        "v_perm_b32 v126, v48, v48, v238\n"
        "v_perm_b32 v130, v56, v56, v238\n"
        "v_perm_b32 v134, v64, v64, v238\n"
        "v_perm_b32 v123, v40, v40, v239\n"
        "v_perm_b32 v127, v48, v48, v239\n"
        "v_perm_b32 v131, v56, v56, v239\n"
        "v_perm_b32 v135, v64, v64, v239\n"
        "v_mfma_f32_16x16x32_bf16 v[88:91], v[216:219], v[120:123], 0\n"
        "v_mfma_f32_16x16x32_bf16 v[92:95], v[216:219], v[124:127], 0\n"
        "v_mfma_f32_16x16x32_bf16 v[96:99], v[216:219], v[128:131], 0\n"
        "v_mfma_f32_16x16x32_bf16 v[100:103], v[216:219], v[132:135], 0\n"
        "s_waitcnt vmcnt(8)\n"
        "v_mfma_f32_16x16x32_bf16 v[40:43], v[196:199], v[120:123], 0\n"
        "s_waitcnt vmcnt(5)\n"
        "v_mfma_f32_16x16x32_bf16 v[44:47], v[206:209], v[120:123], 0\n"
        "v_mfma_f32_16x16x32_bf16 v[48:51], v[198:201], v[124:127], 0\n"
        "v_mfma_f32_16x16x32_bf16 v[52:55], v[208:211], v[124:127], 0\n"
        "v_mfma_f32_16x16x32_bf16 v[56:59], v[200:203], v[128:131], 0\n"
        "s_waitcnt vmcnt(4)\n"
        "v_mfma_f32_16x16x32_bf16 v[60:63], v[210:213], v[128:131], 0\n"
        "v_mfma_f32_16x16x32_bf16 v[64:67], v[202:205], v[132:135], 0\n"
        "v_mfma_f32_16x16x32_bf16 v[68:71], v[212:215], v[132:135], 0\n"
        "v_rcp_f32 v88, v88\n"
        "v_rcp_f32 v92, v92\n"
        "v_rcp_f32 v96, v96\n"
        "v_rcp_f32 v100, v100\n"
        "v_mov_b32 v89, v88\n"
        "v_mov_b32 v93, v92\n"
        "v_mov_b32 v97, v96\n"
        "v_mov_b32 v101, v100\n"
        "v_pk_mul_f32 v[40:41], v[40:41], v[88:89]\n"
        "v_pk_mul_f32 v[42:43], v[42:43], v[88:89]\n"
        "v_pk_mul_f32 v[44:45], v[44:45], v[88:89]\n"
        "v_pk_mul_f32 v[46:47], v[46:47], v[88:89]\n"
        "v_cvt_pk_bf16_f32 v88, v40, v41\n"
        "v_cvt_pk_bf16_f32 v89, v42, v43\n"
        "v_cvt_pk_bf16_f32 v90, v44, v45\n"
        "v_cvt_pk_bf16_f32 v91, v46, v47\n"
        "s_mov_b32 s66, 73728\n"
        "buffer_store_dwordx4 v[88:91], v231, s[60:63], s66 offen offset:0 sc1\n"
        "v_pk_mul_f32 v[48:49], v[48:49], v[92:93]\n"
        "v_pk_mul_f32 v[50:51], v[50:51], v[92:93]\n"
        "v_pk_mul_f32 v[52:53], v[52:53], v[92:93]\n"
        "v_pk_mul_f32 v[54:55], v[54:55], v[92:93]\n"
        "v_cvt_pk_bf16_f32 v92, v48, v49\n"
        "v_cvt_pk_bf16_f32 v93, v50, v51\n"
        "v_cvt_pk_bf16_f32 v94, v52, v53\n"
        "v_cvt_pk_bf16_f32 v95, v54, v55\n"
        "buffer_store_dwordx4 v[92:95], v231, s[60:63], s66 offen offset:1024 sc1\n"
        "v_pk_mul_f32 v[56:57], v[56:57], v[96:97]\n"
        "v_pk_mul_f32 v[58:59], v[58:59], v[96:97]\n"
        "v_pk_mul_f32 v[60:61], v[60:61], v[96:97]\n"
        "v_pk_mul_f32 v[62:63], v[62:63], v[96:97]\n"
        "v_cvt_pk_bf16_f32 v96, v56, v57\n"
        "v_cvt_pk_bf16_f32 v97, v58, v59\n"
        "v_cvt_pk_bf16_f32 v98, v60, v61\n"
        "v_cvt_pk_bf16_f32 v99, v62, v63\n"
        "buffer_store_dwordx4 v[96:99], v231, s[60:63], s66 offen offset:2048 sc1\n"
        "v_pk_mul_f32 v[64:65], v[64:65], v[100:101]\n"
        "v_pk_mul_f32 v[66:67], v[66:67], v[100:101]\n"
        "v_pk_mul_f32 v[68:69], v[68:69], v[100:101]\n"
        "v_pk_mul_f32 v[70:71], v[70:71], v[100:101]\n"
        "v_cvt_pk_bf16_f32 v100, v64, v65\n"
        "v_cvt_pk_bf16_f32 v101, v66, v67\n"
        "v_cvt_pk_bf16_f32 v102, v68, v69\n"
        "v_cvt_pk_bf16_f32 v103, v70, v71\n"
        "buffer_store_dwordx4 v[100:103], v231, s[60:63], s66 offen offset:3072 sc1\n"
        "s_waitcnt vmcnt(0)\n"
        "s_add_u32 s66, s95, 393216\n"
        "s_mov_b64 exec, 0xffff\n"
        "buffer_store_dwordx2 v[242:243], v2, s[60:63], s66 offen sc1\n"
        "s_mov_b64 exec, -1\n"
        "s_or_b32 s67, s94, s64\n"
        "s_cmp_lg_u32 s67, 0\n"
        "s_cbranch_scc1 Lend\n"
        "v_lshlrev_b32 v8, 7, v3\n"
        "v_lshl_add_u32 v8, v4, 13, v8\n"
        "v_lshlrev_b32 v9, 9, v3\n"
        "v_lshl_add_u32 v9, v4, 12, v9\n"
        "v_add_u32 v9, 386048, v9\n"
        "v_cmp_gt_u32 vcc, 14, v3\n"
        "s_nop 1\n"
        "v_cndmask_b32 v16, v9, v8, vcc\n"
        "v_mov_b32 v8, 0x4000\n"
        "v_mov_b32 v9, 0x8000\n"
        "v_cndmask_b32 v8, v8, v9, vcc\n"
        "v_add_u32 v17, v16, v8\n"
        "v_add_u32 v18, v17, v8\n"
        "v_add_u32 v19, v18, v8\n"
        "s_mov_b32 s66, 4096\n"
        "s_mov_b32 s79, 400000\n"
        "Lep:\n"
        "buffer_load_dwordx2 v[8:9], v16, s[60:63], s66 offen sc1\n"
        "buffer_load_dwordx2 v[10:11], v17, s[60:63], s66 offen sc1\n"
        "buffer_load_dwordx2 v[12:13], v18, s[60:63], s66 offen sc1\n"
        "buffer_load_dwordx2 v[14:15], v19, s[60:63], s66 offen sc1\n"
        "s_waitcnt vmcnt(0)\n"
        "v_cmp_ne_u64 s[82:83], s[70:71], v[8:9]\n"
        "v_cmp_ne_u64 s[84:85], s[70:71], v[10:11]\n"
        "v_cmp_ne_u64 s[86:87], s[70:71], v[12:13]\n"
        "v_cmp_ne_u64 s[88:89], s[70:71], v[14:15]\n"
        "s_nop 1\n"
        "s_or_b64 s[82:83], s[82:83], s[84:85]\n"
        "s_or_b64 s[86:87], s[86:87], s[88:89]\n"
        "s_or_b64 s[82:83], s[82:83], s[86:87]\n"
        "s_cbranch_scc0 Lepd\n"
        "s_sleep 8\n"
        "s_sub_u32 s79, s79, 1\n"
        "s_cmp_lg_u32 s79, 0\n"
        "s_cbranch_scc1 Lep\n"
        "Lepd:\n"
        "s_mov_b64 exec, 1\n"
        "buffer_store_dwordx2 v[242:243], off, s[60:63], 0 sc1\n"
        "s_mov_b64 exec, -1\n"
        "s_branch Lend\n"
        "Lend:\n"
        :
        : "s"(__builtin_amdgcn_kernarg_segment_ptr()), "s"(blockIdx.x), "v"(threadIdx.x), "s"((unsigned)(size_t)lds)
        : "memory", "vcc", "scc",
          "s4", "s5", "s6", "s7", "s8", "s9", "s10", "s11", "s12", "s13", "s14", "s15", "s16", "s17", "s18", "s19",
          "s20", "s21", "s22", "s23", "s24", "s25", "s26", "s27", "s28", "s29", "s30", "s31", "s33", "s34", "s35", "s36",
          "s37", "s38", "s39", "s40", "s41", "s42", "s43", "s44", "s45", "s46", "s47", "s48", "s49", "s50", "s51", "s52",
          "s53", "s54", "s55", "s56", "s57", "s58", "s59", "s60", "s61", "s62", "s63", "s64", "s65", "s66", "s67", "s68",
          "s69", "s70", "s71", "s72", "s73", "s74", "s75", "s76", "s77", "s78", "s79", "s80", "s81", "s82", "s83", "s84",
          "s85", "s86", "s87", "s88", "s89", "s90", "s91", "s92", "s93", "s94", "s95", "s96", "s97", "s98", "s99", "v1",
          "v2", "v3", "v4", "v5", "v6", "v7", "v8", "v9", "v10", "v11", "v12", "v13", "v14", "v15", "v16", "v17",
          "v18", "v19", "v20", "v21", "v22", "v23", "v24", "v25", "v26", "v27", "v28", "v29", "v30", "v31", "v32", "v33",
          "v34", "v35", "v36", "v37", "v38", "v39", "v40", "v41", "v42", "v43", "v44", "v45", "v46", "v47", "v48", "v49",
          "v50", "v51", "v52", "v53", "v54", "v55", "v56", "v57", "v58", "v59", "v60", "v61", "v62", "v63", "v64", "v65",
          "v66", "v67", "v68", "v69", "v70", "v71", "v72", "v73", "v74", "v75", "v76", "v77", "v78", "v79", "v80", "v81",
          "v82", "v83", "v84", "v85", "v86", "v87", "v88", "v89", "v90", "v91", "v92", "v93", "v94", "v95", "v96", "v97",
          "v98", "v99", "v100", "v101", "v102", "v103", "v104", "v105", "v106", "v107", "v108", "v109", "v110", "v111", "v112", "v113",
          "v114", "v115", "v116", "v117", "v118", "v119", "v120", "v121", "v122", "v123", "v124", "v125", "v126", "v127", "v128", "v129",
          "v130", "v131", "v132", "v133", "v134", "v135", "v136", "v137", "v138", "v139", "v140", "v141", "v142", "v143", "v144", "v145",
          "v146", "v147", "v148", "v149", "v150", "v151", "v152", "v153", "v154", "v155", "v156", "v157", "v158", "v159", "v160", "v161",
          "v162", "v163", "v164", "v165", "v166", "v167", "v168", "v169", "v170", "v171", "v172", "v173", "v174", "v175", "v176", "v177",
          "v178", "v179", "v180", "v181", "v182", "v183", "v184", "v185", "v186", "v187", "v188", "v189", "v190", "v191", "v192", "v193",
          "v194", "v195", "v196", "v197", "v198", "v199", "v200", "v201", "v202", "v203", "v204", "v205", "v206", "v207", "v208", "v209",
          "v210", "v211", "v212", "v213", "v214", "v215", "v216", "v217", "v218", "v219", "v220", "v221", "v222", "v223", "v224", "v225",
          "v226", "v227", "v228", "v229", "v230", "v231", "v232", "v233", "v234", "v235", "v236", "v237", "v238", "v239", "v240", "v241",
          "v242", "v243", "v244", "v245", "v246", "v247", "v248", "v249", "v250", "v251", "v252", "v253", "v254", "v255"
    );
}

extern "C" void kernel_launch(void* const* d_in, const int* in_sizes, int n_in,
                              void* d_out, int out_size, void* d_ws, size_t ws_size,
                              hipStream_t stream) {
    Args a;
    a.x = d_in[0]; a.wqkv = d_in[1]; a.wo = d_in[2]; a.wg = d_in[4];
    a.wu = d_in[5]; a.wd = d_in[6]; a.out = d_out; a.ws = d_ws;
    fused_block_kernel<<<256, 512, 0, stream>>>(a);
}
```
